```python
import torch

def _softmax(x, axis=-1):
    return torch.softmax(x, dim=axis)
def _gelu(x):
    return torch.nn.functional.gelu(x, approximate="tanh")
def _silu(x):
    return torch.nn.functional.silu(x)
def _sigmoid(x):
    return torch.sigmoid(x)
def _layernorm(x, axis=-1, eps=1e-5):
    m = x.mean(dim=axis, keepdim=True)
    v = x.var(dim=axis, keepdim=True, unbiased=False)
    return (x - m) / torch.sqrt(v + eps)
def _scatter_add(tgt, idx, src, axis=0):
    return tgt.clone().index_add_(axis, idx, src)
def _segment_sum(src, seg, n):
    out = torch.zeros((n,) + src.shape[1:], dtype=src.dtype)
    return out.index_add_(0, seg, src)
def _pad(x, pw):
    flat = tuple(v for pair in reversed(pw) for v in pair)
    return torch.nn.functional.pad(x, flat)

kernel_name = 'sequential_preLN__compressed_kv_cla__dense_activation_gelu__prompt'

def setup_inputs():

    _w = (lambda a: (lambda t: t.to(torch.bfloat16) if t.is_floating_point() else t)(torch.as_tensor(a)))
    def _A_setup():
        B, H, Sq, Sk, D, M = 1, 32, 2048, 2048, 128, 640
        Dc = 4 * D
        return {
            "x": _w((lambda *s: torch.randn(*s, dtype=torch.bfloat16) * 0.05)(B, Sq, M)),
            "c_kv": _w((lambda *s: torch.randn(*s, dtype=torch.bfloat16) * 0.4)(B, Sk, Dc)),
            "w_q": _w((lambda *s: torch.randn(*s, dtype=torch.bfloat16) * 1.6)(M, H, D) * (M ** -0.5)),
            "w_uk": _w((lambda *s: torch.randn(*s, dtype=torch.bfloat16) * 1.6)(Dc, H, D) * (Dc ** -0.5)),
            "w_uv": _w((lambda *s: torch.randn(*s, dtype=torch.bfloat16) * 0.4)(Dc, H, D) * (Dc ** -0.5)),
            "w_o": _w((lambda *s: torch.randn(*s, dtype=torch.bfloat16) * 0.4)(H, D, M) * ((H * D) ** -0.5)),
        }
    _A_in = _A_setup()

    def _B_setup():
        B, S, D, F = 1, 2048, 640, 1728
        return {
            "x": _w((lambda *s: torch.randn(*s, dtype=torch.bfloat16) * 0.05)(B, S, D)),
            "w1": _w((lambda *s: torch.randn(*s, dtype=torch.bfloat16) * 0.8)(D, F) * (D ** -0.5)),
            "b1": _w((lambda *s: torch.randn(*s, dtype=torch.bfloat16) * 12.8)(F) * 0.01),
            "w2": _w((lambda *s: torch.randn(*s, dtype=torch.bfloat16) * 0.2)(F, D) * (F ** -0.5)),
            "b2": _w((lambda *s: torch.randn(*s, dtype=torch.bfloat16) * 3.2)(D) * 0.01),
        }
    _B_in = _B_setup()

    return {
        "A_x": _A_in["x"],
        "A_c_kv": _A_in["c_kv"],
        "A_w_q": _A_in["w_q"],
        "A_w_uk": _A_in["w_uk"],
        "A_w_uv": _A_in["w_uv"],
        "A_w_o": _A_in["w_o"],
        "B_x": _B_in["x"],
        "B_w1": _B_in["w1"],
        "B_b1": _B_in["b1"],
        "B_w2": _B_in["w2"],
        "B_b2": _B_in["b2"],
    }

def reference(A_x, A_c_kv, A_w_q, A_w_uk, A_w_uv, A_w_o, B_x, B_w1, B_b1, B_w2, B_b2):
    def _A_ref(x, c_kv, w_q, w_uk, w_uv, w_o):
        B, Sq, M = x.shape[0], x.shape[1], x.shape[2]
        Sk = c_kv.shape[1]
        H, D = w_q.shape[1], w_q.shape[2]
        q = torch.einsum("bqm,mhd->bhqd", x, w_q)
        k = torch.einsum("bkc,chd->bhkd", c_kv, w_uk)
        v = torch.einsum("bkc,chd->bhkd", c_kv, w_uv)
        scores = torch.einsum("bhqd,bhkd->bhqk", q, k) * (D ** -0.5)
        off = Sk - Sq
        qi = torch.arange(Sq)[:, None]
        kj = torch.arange(Sk)[None, :]
        cmask = kj <= (qi + off)
        scores = torch.where(cmask[None, None], scores, -1e9)
        p = _softmax(scores, axis=-1)
        ctx = torch.einsum("bhqk,bhkd->bhqd", p, v)
        return torch.einsum("bhqd,hdm->bqm", ctx, w_o)

    def _B_ref(x, w1, b1, w2, b2):
        h = torch.einsum("bsd,df->bsf", x, w1) + b1
        h = _gelu(h)
        return torch.einsum("bsf,fd->bsd", h, w2) + b2

    x = A_x
    h = x + _A_ref(_layernorm(x, axis=-1), A_c_kv, A_w_q, A_w_uk, A_w_uv, A_w_o)
    h2 = h + _B_ref(_layernorm(h, axis=-1), B_w1, B_b1, B_w2, B_b2)
    return h2
```

```cpp
#include <hip/hip_runtime.h>
#include <stdint.h>

typedef unsigned short bf16_t;
typedef short bf16x8 __attribute__((ext_vector_type(8)));
typedef short s16x4 __attribute__((ext_vector_type(4)));
typedef float f32x16 __attribute__((ext_vector_type(16)));
typedef float f32x4 __attribute__((ext_vector_type(4)));
typedef float f32x2 __attribute__((ext_vector_type(2)));
typedef unsigned u32x4 __attribute__((ext_vector_type(4)));
typedef unsigned u32x2 __attribute__((ext_vector_type(2)));
typedef __bf16 bf16x2_t __attribute__((ext_vector_type(2)));
#define LDS_AS __attribute__((address_space(3)))
#define DI __device__ __forceinline__
#define SBAR() __builtin_amdgcn_sched_barrier(0)
#define PIN(x) asm volatile("" : "+v"(x))

constexpr int S = 2048, DM = 640, NH = 32, HD = 128, DC = 512, FF = 1728;
constexpr float LN_EPS = 1e-5f;
constexpr size_t MiB = 1024 * 1024;
constexpr size_t WS_XN = 0, WS_KIMG = 4 * MiB, WS_VIMG = 20 * MiB, WS_CTX = 36 * MiB, WS_WO = 52 * MiB, WS_W1 = 58 * MiB,
                 WS_W2 = 61 * MiB, WS_H = 64 * MiB, WS_ST = 67 * MiB, WS_G = 68 * MiB, WS_END = 76 * MiB;

DI unsigned cvtpk(float lo, float hi) {
  f32x2 v = {lo, hi};
  bf16x2_t b = __builtin_convertvector(v, bf16x2_t);
  return __builtin_bit_cast(unsigned, b);
}
DI float bf_lo(unsigned u) { return __uint_as_float(u << 16); }
DI float bf_hi(unsigned u) { return __uint_as_float(u & 0xffff0000u); }
DI unsigned rfl(unsigned x) { return __builtin_amdgcn_readfirstlane(x); }
#define MFMA32(a, b, c) __builtin_amdgcn_mfma_f32_32x32x16_bf16(a, b, c, 0, 0, 0)
#define MFMA16(a, b, c) __builtin_amdgcn_mfma_f32_16x16x32_bf16(a, b, c, 0, 0, 0)

DI void glds1(const void* sbase, unsigned voff, unsigned lds_base) {
  unsigned sv;
  asm volatile("s_mov_b32 %0, m0\n\ts_mov_b32 m0, %3\n\ts_nop 0\n\tglobal_load_lds_dwordx4 %1, %2\n\ts_mov_b32 m0, %0"
               : "=&s"(sv) : "v"(voff), "s"(sbase), "s"(lds_base) : "memory");
}
DI void glds4(const void* sbase, unsigned voff, unsigned lds_base) {
  unsigned sv;
  asm volatile("s_mov_b32 %0, m0\n\ts_mov_b32 m0, %3\n\ts_nop 0\n\t"
               "global_load_lds_dwordx4 %1, %2\n\t"
               "global_load_lds_dwordx4 %1, %2 offset:1024\n\t"
               "global_load_lds_dwordx4 %1, %2 offset:2048\n\t"
               "global_load_lds_dwordx4 %1, %2 offset:3072\n\t"
               "s_mov_b32 m0, %0"
               : "=&s"(sv) : "v"(voff), "s"(sbase), "s"(lds_base) : "memory");
}
#define WAIT_VM0_BAR() asm volatile("s_waitcnt vmcnt(0) lgkmcnt(0)\n\ts_barrier" ::: "memory")
#define LGKM_BAR() asm volatile("s_waitcnt lgkmcnt(0)\n\ts_barrier" ::: "memory")

DI bf16x8 lds_rd128(const char* lds, int off) { return *(const LDS_AS bf16x8*)((const LDS_AS char*)lds + off); }
DI s16x4 lds_tr64(const char* lds, int off) {
  return __builtin_bit_cast(s16x4, __builtin_amdgcn_ds_read_tr16_b64_v4i16((LDS_AS s16x4*)((LDS_AS char*)lds + off)));
}
DI bf16x8 lds_trfrag(const char* lds, int off) {
  s16x4 a = lds_tr64(lds, off), b = lds_tr64(lds, off + 256);
  return (bf16x8){a[0], a[1], a[2], a[3], b[0], b[1], b[2], b[3]};
}
DI u32x4 pack8(const f32x16& v, int b, float sc) {
  u32x4 w = {cvtpk(v[b + 0] * sc, v[b + 1] * sc), cvtpk(v[b + 2] * sc, v[b + 3] * sc), cvtpk(v[b + 4] * sc, v[b + 5] * sc),
             cvtpk(v[b + 6] * sc, v[b + 7] * sc)};
  return w;
}

constexpr int NFR_WO = 40 * 128, NFR_W1 = 112 * 20, NFR_W2 = 40 * 54, NFR = NFR_WO + NFR_W1 + NFR_W2;
__global__ __launch_bounds__(256) void k_prep(const bf16_t* __restrict__ x, const bf16_t* __restrict__ wo, const bf16_t* __restrict__ w1,
                                              const bf16_t* __restrict__ w2, char* __restrict__ ws) {
  const int lane = threadIdx.x & 63, gw = blockIdx.x * 4 + (threadIdx.x >> 6), nw = gridDim.x * 4;
  for (int row = gw; row < S; row += nw) {
    const unsigned* xr = (const unsigned*)(x + (size_t)row * DM);
    unsigned u[5];
    float s = 0.f;
#pragma unroll
    for (int i = 0; i < 5; ++i) { u[i] = xr[lane + 64 * i]; s += bf_lo(u[i]) + bf_hi(u[i]); }
#pragma unroll
    for (int o = 32; o; o >>= 1) s += __shfl_xor(s, o);
    const float mean = s * (1.f / DM);
    float q = 0.f;
#pragma unroll
    for (int i = 0; i < 5; ++i) { float a = bf_lo(u[i]) - mean, b = bf_hi(u[i]) - mean; q += a * a + b * b; }
#pragma unroll
    for (int o = 32; o; o >>= 1) q += __shfl_xor(q, o);
    const float rstd = rsqrtf(q * (1.f / DM) + LN_EPS);
    unsigned* o_ = (unsigned*)(ws + WS_XN) + (size_t)row * (DM / 2);
#pragma unroll
    for (int i = 0; i < 5; ++i) o_[lane + 64 * i] = cvtpk((bf_lo(u[i]) - mean) * rstd, (bf_hi(u[i]) - mean) * rstd);
  }
  const int n = lane & 15, q4 = lane >> 4;
  for (int f = gw; f < NFR; f += nw) {
    const bf16_t* src; char* dst; int ld, k0, k1; bool valid = true;
    if (f < NFR_WO) {
      const int i = f % 5, s = (f / 5) % 128, g = f / 640;
      src = wo + 16 * (5 * g + i) + n; ld = DM; k0 = 32 * s + 16 * (q4 >> 1) + 4 * (q4 & 1); k1 = k0 + 8; dst = ws + WS_WO + (size_t)f * 1024;
    } else if (f < NFR_WO + NFR_W1) {
      const int ff = f - NFR_WO, i = ff % 7, s = (ff / 7) % 20, g = ff / 140, nb = 7 * g + i;
      valid = nb < FF / 16;
      src = w1 + (valid ? 16 * nb + n : 0); ld = FF; k0 = 32 * s + 4 * q4; k1 = k0 + 16; dst = ws + WS_W1 + (size_t)ff * 1024;
    } else {
      const int ff = f - NFR_WO - NFR_W1, i = ff % 5, s = (ff / 5) % 54, g = ff / 270;
      src = w2 + 16 * (5 * g + i) + n; ld = DM; k0 = 32 * s + 4 * q4; k1 = k0 + 16; dst = ws + WS_W2 + (size_t)ff * 1024;
    }
    unsigned e[8];
#pragma unroll
    for (int j = 0; j < 4; ++j) { e[j] = src[(size_t)(k0 + j) * ld]; e[4 + j] = src[(size_t)(k1 + j) * ld]; }
    u32x4 w = {e[0] | (e[1] << 16), e[2] | (e[3] << 16), e[4] | (e[5] << 16), e[6] | (e[7] << 16)};
    if (!valid) w = (u32x4){0u, 0u, 0u, 0u};
    *(u32x4*)(dst + lane * 16) = w;
  }
}

template <bool ISV>
DI void kv_compute(const char* lds, int stage, const int (&abase)[4], int wbase, f32x16 (&acc)[4][2]) {
#pragma unroll
  for (int ks = 0; ks < 4; ++ks) {
    bf16x8 af[4], wf[2];
#pragma unroll
    for (int nb = 0; nb < 2; ++nb) wf[nb] = lds_trfrag(lds, stage + wbase + ks * 8192 + nb * 512);
#pragma unroll
    for (int rb = 0; rb < 4; ++rb) af[rb] = lds_rd128(lds, stage + abase[ks] + rb * 4096);
#pragma unroll
    for (int rb = 0; rb < 4; ++rb)
#pragma unroll
      for (int nb = 0; nb < 2; ++nb) {
        if (ISV) acc[rb][nb] = MFMA32(af[rb], wf[nb], acc[rb][nb]);
        else     acc[rb][nb] = MFMA32(wf[nb], af[rb], acc[rb][nb]);
      }
  }
}
template <bool ISV>
DI void kv_body(const bf16_t* __restrict__ ckv, const bf16_t* __restrict__ wuk, const bf16_t* __restrict__ wuv, char* __restrict__ ws, char* lds) {
  const int tid = threadIdx.x, lane = tid & 63, w = rfl(tid >> 6);
  const int b = blockIdx.x, h = 4 * (b & 7) + ((b >> 3) & 3), j = b >> 5;
  const unsigned lds0 = (unsigned)(size_t)lds;
  const int kh = w >> 2, cg = w & 3;
  const unsigned a_src = (unsigned)((8 * w + (lane >> 3)) * (DC * 2) + (((lane & 7) ^ ((4 * w + (lane >> 4)) & 7)) * 16));
  const unsigned w_src = (unsigned)((8 * w + ((lane >> 2) & 7)) * (NH * HD * 2) + (lane >> 5) * 64 + (lane & 3) * 16);
  const char* abase_g = (const char*)ckv + (size_t)(256 * j) * DC * 2;
  const char* kbase_g = (const char*)wuk + h * HD * 2;
  const char* vbase_g = (const char*)wuv + h * HD * 2;
  auto stage_load = [&](int kt, int st) {
    const unsigned sb = lds0 + st * 65536;
#pragma unroll
    for (int n = 0; n < 4; ++n) glds1(abase_g + kt * 128 + n * 64 * DC * 2, a_src, sb + (w + 8 * n) * 1024);
#pragma unroll
    for (int p = 0; p < 4; ++p)
      glds1((p < 2 ? kbase_g : vbase_g) + (size_t)kt * 64 * NH * HD * 2 + (p & 1) * 128, w_src, sb + 32768 + (w * 8 + 2 * p) * 512);
  };
  const int r32 = lane & 31, hh = lane >> 5;
  int abase[4];
#pragma unroll
  for (int ks = 0; ks < 4; ++ks) abase[ks] = (kh * 128 + r32) * 128 + (((2 * ks + hh) ^ ((r32 >> 1) & 7)) * 16);
  const int wbase = 32768 + hh * 4096 + cg * 1024 + ((lane & 15) >> 2) * 64 + ((lane >> 4) & 1) * 32 + (lane & 3) * 8;
  f32x16 acc[4][2] = {};
  stage_load(0, 0);
  for (int kt = 0; kt < 8; kt += 2) {
    WAIT_VM0_BAR();
    stage_load(kt + 1, 1);
    kv_compute<ISV>(lds, 0, abase, wbase, acc);
    WAIT_VM0_BAR();
    if (kt + 2 < 8) stage_load(kt + 2, 0);
    kv_compute<ISV>(lds, 65536, abase, wbase, acc);
  }
#pragma unroll
  for (int rb = 0; rb < 4; ++rb) {
    const int key32 = 8 * j + 4 * kh + rb, tile = key32 >> 1, kb = key32 & 1;
    char* img = ws + (ISV ? WS_VIMG : WS_KIMG) + ((size_t)(h * 32 + tile) * 16384) + lane * 16;
#pragma unroll
    for (int nb = 0; nb < 2; ++nb) {
      const int db = 2 * (cg & 1) + nb;
#pragma unroll
      for (int s = 0; s < 2; ++s) {
        const int fr = ISV ? db * 4 + 2 * kb + s : kb * 8 + 2 * db + s;
        *(u32x4*)(img + fr * 1024) = pack8(acc[rb][nb], 8 * s, 1.f);
      }
    }
  }
}
__global__ __launch_bounds__(512, 2) void k_kvproj(const bf16_t* __restrict__ ckv, const bf16_t* __restrict__ wuk, const bf16_t* __restrict__ wuv,
                                                   char* __restrict__ ws) {
  extern __shared__ __attribute__((aligned(16))) char lds[];
  if (rfl(threadIdx.x >> 6) & 2) kv_body<true>(ckv, wuk, wuv, ws, lds);
  else kv_body<false>(ckv, wuk, wuv, ws, lds);
}

constexpr float QSCALE = 0.08838834764831845f * 1.4426950408889634f;
constexpr float RESC_THR = 8.f;
constexpr int L_K = 0, L_V = 65536, L_Q = 98304;
DI float max3f(float a, float b, float c) { return __builtin_fmaxf(__builtin_fmaxf(a, b), c); }
DI float rowmax32(const f32x16& p0, const f32x16& p1) {
  float a = max3f(p0[0], p0[1], p1[0]), b = max3f(p0[2], p0[3], p1[1]);
  a = max3f(a, p1[2], p1[3]);
#pragma unroll
  for (int r = 4; r < 16; r += 4) {
    a = max3f(a, p0[r], p0[r + 1]); b = max3f(b, p0[r + 2], p0[r + 3]);
    a = max3f(a, p1[r], p1[r + 1]); b = max3f(b, p1[r + 2], p1[r + 3]);
  }
  float m = __builtin_fmaxf(a, b);
  auto rr = __builtin_amdgcn_permlane32_swap(__float_as_uint(m), __float_as_uint(m), false, false);
  return __builtin_fmaxf(__uint_as_float(rr[0]), __uint_as_float(rr[1]));
}
DI void cmask(f32x16& p0, f32x16& p1, int dq  ) {
#pragma unroll
  for (int r = 0; r < 16; ++r) {
    const int c = (r & 3) + 8 * (r >> 2);
    if (c > dq) p0[r] = -__builtin_inff();
    if (c + 32 > dq) p1[r] = -__builtin_inff();
  }
}

__global__ __launch_bounds__(512, 2) void k_attn(const bf16_t* __restrict__ wq, char* __restrict__ ws) {
  extern __shared__ __attribute__((aligned(16))) char lds[];
  const int tid = threadIdx.x, lane = tid & 63, w = rfl(tid >> 6);
  const int b = blockIdx.x, h = 4 * (b & 7) + ((b >> 3) & 3), j = b >> 5;
  const unsigned lds0 = (unsigned)(size_t)lds;
  const int r32 = lane & 31, hh = lane >> 5;
  {
    const char* xn = ws + WS_XN;
    const unsigned a_src = (unsigned)((8 * w + (lane >> 3)) * (DM * 2) + (((lane & 7) ^ ((4 * w + (lane >> 4)) & 7)) * 16));
    const unsigned w_src = (unsigned)((8 * w + ((lane >> 2) & 7)) * (NH * HD * 2) + (lane >> 5) * 64 + (lane & 3) * 16);
    const char* xa = xn + (size_t)(128 * j) * DM * 2;
    const char* xb = xn + (size_t)(128 * (15 - j)) * DM * 2;
    const char* wg = (const char*)wq + h * HD * 2;
    auto stage_load = [&](int kt, int st) {
      const unsigned sb = lds0 + st * 49152;
#pragma unroll
      for (int n = 0; n < 4; ++n) glds1((n < 2 ? xa : xb) + kt * 128 + (n & 1) * 64 * DM * 2, a_src, sb + (w + 8 * n) * 1024);
#pragma unroll
      for (int p = 0; p < 2; ++p) glds1(wg + (size_t)kt * 64 * NH * HD * 2 + p * 128, w_src, sb + 32768 + (w * 4 + 2 * p) * 512);
    };
    const int rg = w >> 1, dh = w & 1;
    int abase[4];
#pragma unroll
    for (int ks = 0; ks < 4; ++ks) abase[ks] = (rg * 64 + r32) * 128 + (((2 * ks + hh) ^ ((r32 >> 1) & 7)) * 16);
    const int wbase = 32768 + hh * 2048 + dh * 1024 + ((lane & 15) >> 2) * 64 + ((lane >> 4) & 1) * 32 + (lane & 3) * 8;
    f32x16 acc[2][2] = {};
    auto compute = [&](int stage) {
#pragma unroll
      for (int ks = 0; ks < 4; ++ks) {
        bf16x8 af[2], wf[2];
#pragma unroll
        for (int nb = 0; nb < 2; ++nb) wf[nb] = lds_trfrag(lds, stage + wbase + ks * 4096 + nb * 512);
#pragma unroll
        for (int rb = 0; rb < 2; ++rb) af[rb] = lds_rd128(lds, stage + abase[ks] + rb * 4096);
#pragma unroll
        for (int rb = 0; rb < 2; ++rb)
#pragma unroll
          for (int nb = 0; nb < 2; ++nb) acc[rb][nb] = MFMA32(wf[nb], af[rb], acc[rb][nb]);
      }
    };
    stage_load(0, 0);
    for (int kt = 0; kt < 10; kt += 2) {
      WAIT_VM0_BAR();
      stage_load(kt + 1, 1);
      compute(0);
      WAIT_VM0_BAR();
      if (kt + 2 < 10) stage_load(kt + 2, 0);
      compute(49152);
    }
#pragma unroll
    for (int rb = 0; rb < 2; ++rb)
#pragma unroll
      for (int nb = 0; nb < 2; ++nb)
#pragma unroll
        for (int s = 0; s < 2; ++s)
          *(LDS_AS u32x4*)((LDS_AS char*)lds + L_Q + ((2 * rg + rb) * 8 + 2 * (2 * dh + nb) + s) * 1024 + lane * 16) = pack8(acc[rb][nb], 8 * s, QSCALE);
    LGKM_BAR();
  }
  const int g = w >> 2, rw = w & 3;
  const char* kimg = ws + WS_KIMG + (size_t)h * 32 * 16384;
  const char* vimg = ws + WS_VIMG + (size_t)h * 32 * 16384;
  const unsigned dsrc = (unsigned)(w * 4096 + lane * 16);
  const int kb0 = L_K + g * 16384 + lane * 16, vb0 = L_V + g * 16384 + lane * 16;
#define DMA_K(t, slot) glds4(kimg + (size_t)(t) * 32768, dsrc, lds0 + L_K + (slot) * 32768 + w * 4096)
#define DMA_V(t, slot) glds4(vimg + (size_t)(t) * 32768, dsrc, lds0 + L_V + (slot) * 32768 + w * 4096)
  for (int blk = 0; blk < 2; ++blk) {
    const int qb = blk ? 15 - j : j, NT = qb + 1;
    DMA_K(0, 0);
    bf16x8 qf[8];
#pragma unroll
    for (int d0 = 0; d0 < 8; ++d0) qf[d0] = lds_rd128(lds, L_Q + ((4 * blk + rw) * 8 + d0) * 1024 + lane * 16);
    const int dq = 32 * rw + r32 - 64 * g - 4 * hh;
    f32x16 o[4] = {};
    f32x16 pA0, pA1, pB0, pB1, negm;
    float mref, lsum = 0.f;
    WAIT_VM0_BAR();
    if (NT > 1) DMA_K(1, 1);
    DMA_V(0, 0);
    pA0 = f32x16{}; pA1 = f32x16{};
#pragma unroll
    for (int d0 = 0; d0 < 8; ++d0) {
      pA0 = MFMA32(lds_rd128(lds, kb0 + d0 * 1024), qf[d0], pA0);
      pA1 = MFMA32(lds_rd128(lds, kb0 + (8 + d0) * 1024), qf[d0], pA1);
    }
    if (NT == 1) cmask(pA0, pA1, dq);
    mref = __builtin_fmaxf(rowmax32(pA0, pA1), -1e30f);
#pragma unroll
    for (int r = 0; r < 16; ++r) {
      pA0[r] = __builtin_amdgcn_exp2f(pA0[r] - mref); pA1[r] = __builtin_amdgcn_exp2f(pA1[r] - mref); negm[r] = -mref;
    }
    WAIT_VM0_BAR();
#define SUMPACK(P0, P1)                                                                                     \
  float sacc = 0.f;                                                                                         \
  _Pragma("unroll") for (int r = 0; r < 16; ++r) sacc += P0[r];                                             \
  _Pragma("unroll") for (int r = 0; r < 16; ++r) sacc += P1[r];                                             \
  lsum += sacc;                                                                                             \
  const bf16x8 pk0 = __builtin_bit_cast(bf16x8, pack8(P0, 0, 1.f)), pk1 = __builtin_bit_cast(bf16x8, pack8(P0, 8, 1.f)), \
               pk2 = __builtin_bit_cast(bf16x8, pack8(P1, 0, 1.f)), pk3 = __builtin_bit_cast(bf16x8, pack8(P1, 8, 1.f));
#define PV(vslot)                                                                                           \
  _Pragma("unroll") for (int db = 0; db < 4; ++db) {                                                        \
    o[db] = MFMA32(lds_rd128(lds, vb0 + (vslot) * 32768 + (db * 4 + 0) * 1024), pk0, o[db]);                  \
    o[db] = MFMA32(lds_rd128(lds, vb0 + (vslot) * 32768 + (db * 4 + 1) * 1024), pk1, o[db]);                  \
    o[db] = MFMA32(lds_rd128(lds, vb0 + (vslot) * 32768 + (db * 4 + 2) * 1024), pk2, o[db]);                  \
    o[db] = MFMA32(lds_rd128(lds, vb0 + (vslot) * 32768 + (db * 4 + 3) * 1024), pk3, o[db]);                  \
  }
#define STEP(C0, C1, P0, P1, t, par)                                                                        \
  do {                                                                                                      \
    if ((t) + 1 < NT) DMA_K((t) + 1, (par) ^ 1);                                                            \
    DMA_V((t), (par));                                                                                      \
    C0 = MFMA32(lds_rd128(lds, kb0 + (par) * 32768), qf[0], negm);                                          \
    C1 = MFMA32(lds_rd128(lds, kb0 + (par) * 32768 + 8 * 1024), qf[0], negm);                               \
    _Pragma("unroll") for (int d0 = 1; d0 < 8; ++d0) {                                                      \
      C0 = MFMA32(lds_rd128(lds, kb0 + (par) * 32768 + d0 * 1024), qf[d0], C0);                             \
      C1 = MFMA32(lds_rd128(lds, kb0 + (par) * 32768 + (8 + d0) * 1024), qf[d0], C1);                       \
    }                                                                                                       \
    SUMPACK(P0, P1)                                                                                         \
    if ((t) == NT - 1) cmask(C0, C1, dq);                                                                   \
    const float rm = rowmax32(C0, C1);                                                                      \
    float fsc = 1.f;                                                                                        \
    const bool resc = __any(rm > RESC_THR);                                                                 \
    if (__builtin_expect(resc, 0)) {                                                                        \
      const float dl = __builtin_fmaxf(rm, 0.f);                                                            \
      mref += dl; fsc = __builtin_amdgcn_exp2f(-dl); lsum *= fsc;                                           \
      _Pragma("unroll") for (int r = 0; r < 16; ++r) { C0[r] -= dl; C1[r] -= dl; negm[r] = -mref; }         \
    }                                                                                                       \
    PV((par) ^ 1)                                                                                           \
    _Pragma("unroll") for (int r = 0; r < 16; ++r) { C0[r] = __builtin_amdgcn_exp2f(C0[r]); C1[r] = __builtin_amdgcn_exp2f(C1[r]); } \
    if (__builtin_expect(resc, 0)) {                                                                        \
      _Pragma("unroll") for (int db = 0; db < 4; ++db) _Pragma("unroll") for (int r = 0; r < 16; ++r) o[db][r] *= fsc; \
    }                                                                                                       \
    WAIT_VM0_BAR();                                                                                         \
  } while (0)
    int t = 1;
    for (; t + 1 < NT; t += 2) {
      STEP(pB0, pB1, pA0, pA1, t, 1);
      STEP(pA0, pA1, pB0, pB1, t + 1, 0);
    }
    if (t < NT) {
      STEP(pB0, pB1, pA0, pA1, t, 1);
      SUMPACK(pB0, pB1)
      PV(1)
    } else {
      SUMPACK(pA0, pA1)
      PV(0)
    }
    {
      auto rr = __builtin_amdgcn_permlane32_swap(__float_as_uint(lsum), __float_as_uint(lsum), false, false);
      lsum = __uint_as_float(rr[0]) + __uint_as_float(rr[1]);
    }
    LGKM_BAR();
    {
      LDS_AS char* mb = (LDS_AS char*)lds + w * 8192 + lane * 16;
#pragma unroll
      for (int i = 0; i < 2; ++i) {
        const f32x16 v = g ? o[i] : o[2 + i];
#pragma unroll
        for (int c = 0; c < 4; ++c) *(LDS_AS f32x4*)(mb + (i * 4 + c) * 1024) = (f32x4){v[4 * c], v[4 * c + 1], v[4 * c + 2], v[4 * c + 3]};
      }
      *(LDS_AS f32x2*)((LDS_AS char*)lds + L_V + w * 512 + lane * 8) = (f32x2){mref, lsum};
    }
    LGKM_BAR();
    {
      const f32x2 st = *(const LDS_AS f32x2*)((const LDS_AS char*)lds + L_V + (w ^ 4) * 512 + lane * 8);
      const float mm = __builtin_fmaxf(mref, st[0]);
      float a = __builtin_amdgcn_exp2f(mref - mm), bb = __builtin_amdgcn_exp2f(st[0] - mm);
      const float inv = 1.f / (a * lsum + bb * st[1]);
      a *= inv; bb *= inv;
      const LDS_AS char* mb = (const LDS_AS char*)lds + (w ^ 4) * 8192 + lane * 16;
      const int row = 128 * qb + 32 * rw;
      char* cbase = ws + WS_CTX + ((size_t)(row >> 6) * 128 * 4 + ((row >> 4) & 3) + (r32 >> 4)) * 1024 + ((r32 & 15) + 16 * hh) * 16;
#pragma unroll
      for (int i = 0; i < 2; ++i) {
        f32x16 v = g ? o[2 + i] : o[i];
#pragma unroll
        for (int c = 0; c < 4; ++c) {
          const f32x4 pv = *(const LDS_AS f32x4*)(mb + (i * 4 + c) * 1024);
#pragma unroll
          for (int e = 0; e < 4; ++e) v[4 * c + e] = v[4 * c + e] * a + pv[e] * bb;
        }
        const int s = 4 * h + 2 * g + i;
        *(u32x4*)(cbase + (size_t)s * 4096) = pack8(v, 0, 1.f);
        *(u32x4*)(cbase + (size_t)s * 4096 + 512) = pack8(v, 8, 1.f);
      }
    }
    LGKM_BAR();
  }
#undef STEP
#undef PV
#undef SUMPACK
#undef DMA_K
#undef DMA_V
}

DI size_t h_off(int row, int nb, int q) {
  return ((size_t)((row >> 5) * 20 + (nb >> 1)) * 2 + ((row >> 4) & 1)) * 1024 + ((row & 15) + 16 * q) * 16 + (nb & 1) * 8;
}
template <int MODE>
__global__ __launch_bounds__(256, 1) void k_skinny(const bf16_t* __restrict__ x, const bf16_t* __restrict__ b2, char* __restrict__ ws,
                                                   bf16_t* __restrict__ out) {
  constexpr int KS = MODE == 0 ? 128 : 54;
  extern __shared__ __attribute__((aligned(16))) char lds[];
  const int tid = threadIdx.x, lane = tid & 63, w = rfl(tid >> 6);
  const int b = blockIdx.x, R = 4 * (b & 7) + ((b >> 3) & 3), g = b >> 5;
  const char* ag = ws + (MODE == 0 ? WS_CTX : WS_G) + (size_t)R * KS * 4096 + lane * 16;
  const char* wg = ws + (MODE == 0 ? WS_WO : WS_W2) + (size_t)g * KS * 5120 + lane * 16;
  f32x4 acc[4][5] = {};
  constexpr int PD = 4;
  bf16x8 af[PD][4], wf[PD][5];
  auto load = [&](int s, int slot) {
#pragma unroll
    for (int i = 0; i < 4; ++i) af[slot][i] = *(const bf16x8*)(ag + (size_t)s * 4096 + i * 1024);
#pragma unroll
    for (int i = 0; i < 5; ++i) wf[slot][i] = *(const bf16x8*)(wg + (size_t)s * 5120 + i * 1024);
  };
  auto comp = [&](int slot) {
#pragma unroll
    for (int rb = 0; rb < 4; ++rb)
#pragma unroll
      for (int nb = 0; nb < 5; ++nb) acc[rb][nb] = MFMA16(wf[slot][nb], af[slot][rb], acc[rb][nb]);
  };
  const int n_it = (KS - w + 3) / 4;
#pragma unroll
  for (int p = 0; p < PD; ++p) load(w + 4 * p, p);
  int i = 0;
  for (; i + 2 * PD <= n_it; i += PD) {
#pragma unroll
    for (int p = 0; p < PD; ++p) { comp(p); load(w + 4 * (i + PD + p), p); }
  }
#pragma unroll
  for (int p = 0; p < PD; ++p) { comp(p); if (i + PD + p < n_it) load(w + 4 * (i + PD + p), p); }
#pragma unroll
  for (int p = 0; p < PD; ++p) if (i + PD + p < n_it) comp(p);
#pragma unroll
  for (int rb = 0; rb < 4; ++rb)
#pragma unroll
    for (int nb = 0; nb < 5; ++nb) *(LDS_AS f32x4*)((LDS_AS char*)lds + ((w * 4 + rb) * 5 + nb) * 1024 + lane * 16) = acc[rb][nb];
  __syncthreads();
  const int m = lane & 15, q = lane >> 4, row = 64 * R + 16 * w + m;
  f32x4 v[5];
#pragma unroll
  for (int nb = 0; nb < 5; ++nb) {
    v[nb] = *(const LDS_AS f32x4*)((const LDS_AS char*)lds + ((0 * 4 + w) * 5 + nb) * 1024 + lane * 16);
#pragma unroll
    for (int u = 1; u < 4; ++u) v[nb] += *(const LDS_AS f32x4*)((const LDS_AS char*)lds + ((u * 4 + w) * 5 + nb) * 1024 + lane * 16);
  }
  if (MODE == 0) {
    float s = 0.f;
#pragma unroll
    for (int nb = 0; nb < 5; ++nb) {
      const int col = 80 * g + 16 * nb + 4 * q;
      const u32x2 xr = *(const u32x2*)(x + (size_t)row * DM + col);
      u32x2 hv = {cvtpk(v[nb][0] + bf_lo(xr[0]), v[nb][1] + bf_hi(xr[0])), cvtpk(v[nb][2] + bf_lo(xr[1]), v[nb][3] + bf_hi(xr[1]))};
      *(u32x2*)(ws + WS_H + h_off(row, 5 * g + nb, q)) = hv;
      v[nb] = (f32x4){bf_lo(hv[0]), bf_hi(hv[0]), bf_lo(hv[1]), bf_hi(hv[1])};
      s += (v[nb][0] + v[nb][1]) + (v[nb][2] + v[nb][3]);
    }
    s += __shfl_xor(s, 16); s += __shfl_xor(s, 32);
    const float mean = s * (1.f / 80.f);
    float m2 = 0.f;
#pragma unroll
    for (int nb = 0; nb < 5; ++nb)
#pragma unroll
      for (int e = 0; e < 4; ++e) { const float d = v[nb][e] - mean; m2 += d * d; }
    m2 += __shfl_xor(m2, 16); m2 += __shfl_xor(m2, 32);
    if (q == 0) *(f32x2*)(ws + WS_ST + ((size_t)row * 8 + g) * 8) = (f32x2){mean, m2};
  } else {
#pragma unroll
    for (int nb = 0; nb < 5; ++nb) {
      const int col = 80 * g + 16 * nb + 4 * q;
      const u32x2 hr = *(const u32x2*)(ws + WS_H + h_off(row, 5 * g + nb, q));
      const u32x2 br = *(const u32x2*)(b2 + col);
      u32x2 ov = {cvtpk(v[nb][0] + bf_lo(br[0]) + bf_lo(hr[0]), v[nb][1] + bf_hi(br[0]) + bf_hi(hr[0])),
                  cvtpk(v[nb][2] + bf_lo(br[1]) + bf_lo(hr[1]), v[nb][3] + bf_hi(br[1]) + bf_hi(hr[1]))};
      *(u32x2*)(out + (size_t)row * DM + col) = ov;
    }
  }
}

DI float gelu_tanh(float v) {
  const float u = v * (0.7978845608028654f + 0.035677408136300125f * v * v);
  return v * __builtin_amdgcn_rcpf(1.f + __builtin_amdgcn_exp2f(-2.885390081777927f * u));
}
__global__ __launch_bounds__(256, 1) void k_mlp1(const bf16_t* __restrict__ b1, char* __restrict__ ws) {
  extern __shared__ __attribute__((aligned(16))) char lds[];
  const int tid = threadIdx.x, lane = tid & 63, w = rfl(tid >> 6);
  const int b = blockIdx.x, R = 2 * (b & 7) + ((b >> 3) & 1), g = b >> 4;
  const unsigned lds0 = (unsigned)(size_t)lds;
  const char* wsrc = ws + WS_W1 + (size_t)g * 140 * 1024;
  for (int p = w; p < 35; p += 4) glds4(wsrc + p * 4096, lane * 16, lds0 + p * 4096);
  const char* ag = ws + WS_H + (size_t)(4 * R + w) * 40 * 1024 + lane * 16;
  bf16x8 af[20][2];
#pragma unroll
  for (int s = 0; s < 20; ++s)
#pragma unroll
    for (int rb = 0; rb < 2; ++rb) af[s][rb] = *(const bf16x8*)(ag + (s * 2 + rb) * 1024);
  const int m = lane & 15, q = lane >> 4;
  float mean[2], rstd[2];
#pragma unroll
  for (int rb = 0; rb < 2; ++rb) {
    const f32x4* sp = (const f32x4*)(ws + WS_ST + (size_t)(128 * R + 32 * w + 16 * rb + m) * 64);
    f32x4 sv[4];
#pragma unroll
    for (int i = 0; i < 4; ++i) sv[i] = sp[i];
    float ms = 0.f, m2 = 0.f;
#pragma unroll
    for (int i = 0; i < 4; ++i) { ms += sv[i][0] + sv[i][2]; m2 += sv[i][1] + sv[i][3]; }
    ms *= 0.125f;
#pragma unroll
    for (int i = 0; i < 4; ++i) { const float d0 = sv[i][0] - ms, d1 = sv[i][2] - ms; m2 += 80.f * (d0 * d0 + d1 * d1); }
    mean[rb] = ms; rstd[rb] = rsqrtf(m2 * (1.f / DM) + LN_EPS);
  }
  WAIT_VM0_BAR();
  f32x4 acc[2][7] = {}, cs[7] = {};
  const bf16x8 ones = {0x3f80, 0x3f80, 0x3f80, 0x3f80, 0x3f80, 0x3f80, 0x3f80, 0x3f80};
#pragma unroll
  for (int s = 0; s < 20; ++s) {
#pragma unroll
    for (int nb = 0; nb < 7; ++nb) {
      const bf16x8 wf = lds_rd128(lds, (s * 7 + nb) * 1024 + lane * 16);
      acc[0][nb] = MFMA16(wf, af[s][0], acc[0][nb]);
      acc[1][nb] = MFMA16(wf, af[s][1], acc[1][nb]);
      if ((s & 3) == w) cs[nb] = MFMA16(wf, ones, cs[nb]);
    }
  }
  LDS_AS char* cb = (LDS_AS char*)lds + 140 * 1024;
  if (m == 0) {
#pragma unroll
    for (int nb = 0; nb < 7; ++nb) *(LDS_AS f32x4*)(cb + ((w * 7 + nb) * 4 + q) * 16) = cs[nb];
  }
  LGKM_BAR();
#pragma unroll
  for (int nb = 0; nb < 7; ++nb) {
    const int nbg = 7 * g + nb;
    if (nbg < FF / 16) {
      f32x4 c = *(const LDS_AS f32x4*)(cb + ((0 * 7 + nb) * 4 + q) * 16);
#pragma unroll
      for (int u = 1; u < 4; ++u) c += *(const LDS_AS f32x4*)(cb + ((u * 7 + nb) * 4 + q) * 16);
      const u32x2 br = *(const u32x2*)(b1 + 16 * nbg + 4 * q);
      const float bb[4] = {bf_lo(br[0]), bf_hi(br[0]), bf_lo(br[1]), bf_hi(br[1])};
#pragma unroll
      for (int rb = 0; rb < 2; ++rb) {
        float y[4];
#pragma unroll
        for (int e = 0; e < 4; ++e) y[e] = gelu_tanh(rstd[rb] * (acc[rb][nb][e] - mean[rb] * c[e]) + bb[e]);
        const int row = 128 * R + 32 * w + 16 * rb;
        char* dst = ws + WS_G + ((size_t)((row >> 6) * 54 + (nbg >> 1)) * 4 + ((row >> 4) & 3)) * 1024 + lane * 16 + (nbg & 1) * 8;
        *(u32x2*)dst = (u32x2){cvtpk(y[0], y[1]), cvtpk(y[2], y[3])};
      }
    }
  }
}

extern "C" void kernel_launch(void* const* d_in, const int* in_sizes, int n_in, void* d_out, int out_size, void* d_ws, size_t ws_size,
                              hipStream_t stream) {
  const bf16_t* x = (const bf16_t*)d_in[0];
  const bf16_t* ckv = (const bf16_t*)d_in[1];
  const bf16_t* wq = (const bf16_t*)d_in[2];
  const bf16_t* wuk = (const bf16_t*)d_in[3];
  const bf16_t* wuv = (const bf16_t*)d_in[4];
  const bf16_t* wo = (const bf16_t*)d_in[5];
  const bf16_t* w1 = (const bf16_t*)d_in[7];
  const bf16_t* b1 = (const bf16_t*)d_in[8];
  const bf16_t* w2 = (const bf16_t*)d_in[9];
  const bf16_t* b2 = (const bf16_t*)d_in[10];
  char* ws = (char*)d_ws;
  (void)hipFuncSetAttribute((const void*)k_kvproj, hipFuncAttributeMaxDynamicSharedMemorySize, 131072);
  (void)hipFuncSetAttribute((const void*)k_attn, hipFuncAttributeMaxDynamicSharedMemorySize, 163840);
  (void)hipFuncSetAttribute((const void*)k_skinny<0>, hipFuncAttributeMaxDynamicSharedMemorySize, 81920);
  (void)hipFuncSetAttribute((const void*)k_skinny<1>, hipFuncAttributeMaxDynamicSharedMemorySize, 81920);
  (void)hipFuncSetAttribute((const void*)k_mlp1, hipFuncAttributeMaxDynamicSharedMemorySize, 145408);
  k_prep<<<1024, 256, 0, stream>>>(x, wo, w1, w2, ws);
  k_kvproj<<<256, 512, 131072, stream>>>(ckv, wuk, wuv, ws);
  k_attn<<<256, 512, 163840, stream>>>(wq, ws);
  k_skinny<0><<<256, 256, 81920, stream>>>(x, b2, ws, (bf16_t*)d_out);
  k_mlp1<<<256, 256, 145408, stream>>>(b1, ws);
  k_skinny<1><<<256, 256, 81920, stream>>>(x, b2, ws, (bf16_t*)d_out);
}
```

```cpp
#include <hip/hip_runtime.h>
#include <stdint.h>

typedef unsigned short bf16_t;
typedef short bf16x8 __attribute__((ext_vector_type(8)));
typedef short s16x4 __attribute__((ext_vector_type(4)));
typedef float f32x16 __attribute__((ext_vector_type(16)));
typedef float f32x4 __attribute__((ext_vector_type(4)));
typedef float f32x2 __attribute__((ext_vector_type(2)));
typedef unsigned u32x4 __attribute__((ext_vector_type(4)));
typedef unsigned u32x2 __attribute__((ext_vector_type(2)));
typedef __bf16 bf16x2_t __attribute__((ext_vector_type(2)));
#define LDS_AS __attribute__((address_space(3)))
#define DI __device__ __forceinline__
#define SBAR() __builtin_amdgcn_sched_barrier(0)
#define PIN(x) asm volatile("" : "+v"(x))

constexpr int S = 2048, DM = 640, NH = 32, HD = 128, DC = 512, FF = 1728;
constexpr float LN_EPS = 1e-5f;
constexpr size_t MiB = 1024 * 1024;
constexpr size_t WS_LN = 0, WS_TC = 16384, WS_HS = 65536, WS_KIMG = 4 * MiB, WS_VIMG = 20 * MiB, WS_CTX = 36 * MiB, WS_WO = 52 * MiB,
                 WS_W1 = 58 * MiB, WS_W2 = 61 * MiB,
                 WS_H = WS_KIMG, WS_ST = WS_KIMG + 3 * MiB, WS_G = WS_KIMG + 4 * MiB;
constexpr int TAIL_KB = 16;

DI unsigned cvtpk(float lo, float hi) {
  f32x2 v = {lo, hi};
  bf16x2_t b = __builtin_convertvector(v, bf16x2_t);
  return __builtin_bit_cast(unsigned, b);
}
DI float bf_lo(unsigned u) { return __uint_as_float(u << 16); }
DI float bf_hi(unsigned u) { return __uint_as_float(u & 0xffff0000u); }
DI unsigned rfl(unsigned x) { return __builtin_amdgcn_readfirstlane(x); }
#define MFMA16(a, b, c) __builtin_amdgcn_mfma_f32_16x16x32_bf16(a, b, c, 0, 0, 0)

DI void glds1(const void* sbase, unsigned voff, unsigned lds_base) {
  unsigned sv;
  asm volatile("s_mov_b32 %0, m0\n\ts_mov_b32 m0, %3\n\ts_nop 0\n\tglobal_load_lds_dwordx4 %1, %2\n\ts_mov_b32 m0, %0"
               : "=&s"(sv) : "v"(voff), "s"(sbase), "s"(lds_base) : "memory");
}
#define LGKM_BAR() asm volatile("s_waitcnt lgkmcnt(0)\n\ts_barrier" ::: "memory")

DI bf16x8 lds_rd128(const char* lds, int off) { return *(const LDS_AS bf16x8*)((const LDS_AS char*)lds + off); }

__global__ __launch_bounds__(256, 1) void k_front(const bf16_t* x, const bf16_t* ckv, const bf16_t* wuk, const bf16_t* wuv, const bf16_t* wq,
                                                  const bf16_t* wo, const bf16_t* w1, const bf16_t* w2, char* ws) {
  extern __shared__ __attribute__((aligned(16))) char lds[];
  const int w = rfl(threadIdx.x >> 6);
  const int b = blockIdx.x, h = 4 * (b & 7) + ((b >> 3) & 3), j = b >> 5;
  const unsigned lds0 = (unsigned)(size_t)lds;
  const char* kimg = ws + WS_KIMG + (size_t)h * 32 * 16384;
  const char* vimg = ws + WS_VIMG + (size_t)h * 32 * 16384;
  const int g = w >> 1, rh = w & 1;
  const char* c2 = ws + WS_CTX + (size_t)((2 * j + rh) * 512 + 16 * h + 8 * g) * 1024 + 4096;
  const char* c1 = ws + WS_CTX + (size_t)((2 * (15 - j) + rh) * 512 + 16 * h + 8 * g) * 1024 + 4096;
  int gw = 4 * b + w;
  if (gw >= 948) gw -= 948;
  const char *rsrc, *rd0, *rd1;
  int rld;
  if (gw < 512) {
    const int s = gw >> 2, c = gw & 3;
    rsrc = (const char*)wo + (size_t)(32 * s * DM + 160 * c) * 2; rld = DM * 2;
    rd0 = ws + WS_WO + (size_t)((2 * c * 128 + s) * 5) * 1024 + 2048; rd1 = rd0 + 128 * 5 * 1024;
  } else if (gw < 728) {
    const int s = (gw - 512) >> 2, c = gw & 3;
    rsrc = (const char*)w2 + (size_t)(32 * s * DM + 160 * c) * 2; rld = DM * 2;
    rd0 = ws + WS_W2 + (size_t)((2 * c * 54 + s) * 5) * 1024 + 2048; rd1 = rd0 + 54 * 5 * 1024;
  } else {
    const int s = (gw - 728) / 11, c = (gw - 728) % 11, nb0 = min(10 * c, 98);
    rsrc = (const char*)w1 + (size_t)(32 * s * FF + 16 * nb0) * 2; rld = FF * 2;
    rd0 = ws + WS_W1 + (size_t)(s * 108 + nb0) * 1024 + 2048; rd1 = rd0 + 5 * 1024;
  }
  if (b < 16 && threadIdx.x < 2) *(unsigned*)(ws + WS_TC + b * 2048 + threadIdx.x * 256) = 0u;
  const int lnrow = 128 * (h < 16 ? 15 - j : j) + 8 * (h & 15) + 2 * w;
  const char* p_ckv0 = (const char*)ckv + (size_t)(128 * (15 - j)) * DC * 2;
  const char* p_ckv1 = (const char*)ckv + (size_t)(128 * j) * DC * 2;
  const char* p_wuk = (const char*)wuk + h * HD * 2;
  const char* p_wuv = (const char*)wuv + h * HD * 2;
  const char* p_x0 = (const char*)x + (size_t)(128 * (15 - j)) * DM * 2;
  const char* p_x1 = (const char*)x + (size_t)(128 * j) * DM * 2;
  const char* p_wq = (const char*)wq + h * HD * 2;
  asm volatile(
      "s_mov_b32 s50, m0\n"
      "v_mbcnt_lo_u32_b32 v248, -1, 0\n"
      "v_mbcnt_hi_u32_b32 v248, -1, v248\n"
      "s_lshl_b32 s53, %20, 8\n"
      "s_add_u32 s53, s53, 4096\n"
      "v_lshl_add_u32 v254, v248, 2, s53\n"
      "global_load_dword v254, v254, %15 sc1\n"
      "s_getpc_b64 s[84:85]\n"
      "L_pc_%=:\n"
      "s_lshl_b32 s86, %24, 10\n"
      "s_min_u32 s86, s86, L_end_%=-L_pc_%=-1024\n"
      "v_lshlrev_b32 v0, 4, v248\n"
      "v_add_u32 v0, s86, v0\n"
      "global_load_dwordx4 v[4:7], v0, s[84:85]\n"
      "s_sub_u32 s62, s62, 4096\n"
      "s_subb_u32 s63, s63, 0\n"
      "s_sub_u32 s64, s64, 4096\n"
      "s_subb_u32 s65, s65, 0\n"
      "s_sub_u32 s70, s70, 4096\n"
      "s_subb_u32 s71, s71, 0\n"
      "s_lshl_b32 s86, %12, 12\n"
      "s_add_u32 s82, %11, s86\n"
      "v_lshrrev_b32 v0, 3, v248\n"
      "s_lshl_b32 s86, %12, 5\n"
      "v_add_u32 v0, s86, v0\n"
      "v_lshrrev_b32 v1, 4, v248\n"
      "v_and_b32 v2, 7, v248\n"
      "v_xor_b32 v1, v1, v2\n"
      "v_lshlrev_b32 v1, 4, v1\n"
      "v_xor_b32 v2, 64, v1\n"
      "v_lshlrev_b32 v3, 10, v0\n"
      "v_add_u32 v224, v3, v1\n"
      "v_add_u32 v224, 0, v224\n"
      "v_add_u32 v225, v3, v2\n"
      "v_add_u32 v225, 7168, v225\n"
      "v_add_u32 v226, v3, v1\n"
      "v_add_u32 v226, 14336, v226\n"
      "v_add_u32 v227, v3, v2\n"
      "v_add_u32 v227, 21504, v227\n"
      "v_mul_u32_u24 v3, 1280, v0\n"
      "v_add_u32 v228, v3, v1\n"
      "v_add_u32 v228, 0, v228\n"
      "v_add_u32 v229, v3, v2\n"
      "v_add_u32 v229, 9216, v229\n"
      "v_add_u32 v230, v3, v1\n"
      "v_add_u32 v230, 18432, v230\n"
      "v_add_u32 v231, v3, v2\n"
      "v_add_u32 v231, 27648, v231\n"
      "v_bfe_u32 v0, v248, 2, 3\n"
      "s_lshl_b32 s86, %12, 4\n"
      "v_add_u32 v0, s86, v0\n"
      "v_lshlrev_b32 v0, 13, v0\n"
      "v_lshrrev_b32 v1, 5, v248\n"
      "v_lshlrev_b32 v1, 6, v1\n"
      "v_and_b32 v2, 3, v248\n"
      "v_lshlrev_b32 v2, 4, v2\n"
      "v_add3_u32 v0, v0, v1, v2\n"
      "v_xor_b32 v1, 32, v0\n"
      "v_add_u32 v232, 4096, v0\n"
      "v_add_u32 v233, 3200, v0\n"
      "v_add_u32 v234, 67584, v1\n"
      "v_add_u32 v235, 66688, v1\n"
      "s_add_u32 s80, s56, 0\n"
      "s_addc_u32 s81, s57, 0\n"
      "s_add_u32 m0, s82, 131072\n"
      "s_nop 0\n"
      "global_load_lds_dwordx4 v224, s[80:81] offset:0\n"
      "global_load_lds_dwordx4 v225, s[80:81] offset:1024\n"
      "global_load_lds_dwordx4 v226, s[80:81] offset:2048\n"
      "global_load_lds_dwordx4 v227, s[80:81] offset:3072\n"
      "s_add_u32 s80, s58, 0\n"
      "s_addc_u32 s81, s59, 0\n"
      "s_add_u32 m0, s82, 147456\n"
      "s_nop 0\n"
      "global_load_lds_dwordx4 v224, s[80:81] offset:0\n"
      "global_load_lds_dwordx4 v225, s[80:81] offset:1024\n"
      "global_load_lds_dwordx4 v226, s[80:81] offset:2048\n"
      "global_load_lds_dwordx4 v227, s[80:81] offset:3072\n"
      "s_add_u32 s80, s62, 0\n"
      "s_addc_u32 s81, s63, 0\n"
      "s_add_u32 m0, s82, 0\n"
      "s_nop 0\n"
      "global_load_lds_dwordx4 v232, s[80:81] offset:0\n"
      "global_load_lds_dwordx4 v233, s[80:81] offset:1024\n"
      "global_load_lds_dwordx4 v234, s[80:81] offset:2048\n"
      "global_load_lds_dwordx4 v235, s[80:81] offset:3072\n"
      "s_add_u32 s80, s64, 0\n"
      "s_addc_u32 s81, s65, 0\n"
      "s_add_u32 m0, s82, 16384\n"
      "s_nop 0\n"
      "global_load_lds_dwordx4 v232, s[80:81] offset:0\n"
      "global_load_lds_dwordx4 v233, s[80:81] offset:1024\n"
      "global_load_lds_dwordx4 v234, s[80:81] offset:2048\n"
      "global_load_lds_dwordx4 v235, s[80:81] offset:3072\n"
      "s_barrier\n"
      "s_add_u32 s80, s56, 128\n"
      "s_addc_u32 s81, s57, 0\n"
      "s_add_u32 m0, s82, 32768\n"
      "s_nop 0\n"
      "global_load_lds_dwordx4 v224, s[80:81] offset:0\n"
      "global_load_lds_dwordx4 v225, s[80:81] offset:1024\n"
      "global_load_lds_dwordx4 v226, s[80:81] offset:2048\n"
      "global_load_lds_dwordx4 v227, s[80:81] offset:3072\n"
      "s_add_u32 s80, s58, 128\n"
      "s_addc_u32 s81, s59, 0\n"
      "s_add_u32 m0, s82, 49152\n"
      "s_nop 0\n"
      "global_load_lds_dwordx4 v224, s[80:81] offset:0\n"
      "global_load_lds_dwordx4 v225, s[80:81] offset:1024\n"
      "global_load_lds_dwordx4 v226, s[80:81] offset:2048\n"
      "global_load_lds_dwordx4 v227, s[80:81] offset:3072\n"
      "s_add_u32 s90, s72, 4096\n"
      "s_addc_u32 s91, s73, 0\n"
      "s_add_u32 s92, s74, 4096\n"
      "s_addc_u32 s93, s75, 0\n"
      "v_and_b32 v0, 15, v248\n"
      "v_lshrrev_b32 v1, 4, v248\n"
      "s_lshr_b32 s86, %12, 1\n"
      "s_lshl_b32 s86, s86, 14\n"
      "s_add_u32 s86, s86, %11\n"
      "v_lshlrev_b32 v2, 7, v0\n"
      "v_add_u32 v2, s86, v2\n"
      "v_bfe_u32 v3, v248, 1, 3\n"
      "v_xor_b32 v3, v3, v1\n"
      "v_xor_b32 v236, 0, v3\n"
      "v_lshl_add_u32 v236, v236, 4, v2\n"
      "v_xor_b32 v237, 4, v3\n"
      "v_lshl_add_u32 v237, v237, 4, v2\n"
      "v_lshlrev_b32 v244, 4, v248\n"
      "v_and_b32 v2, 1, v1\n"
      "s_and_b32 s86, %12, 1\n"
      "s_lshl_b32 s87, s86, 10\n"
      "s_add_u32 s87, s87, %11\n"
      "v_lshlrev_b32 v238, 11, v1\n"
      "v_add_u32 v238, s87, v238\n"
      "v_bfe_u32 v3, v248, 2, 2\n"
      "v_lshl_add_u32 v238, v3, 6, v238\n"
      "v_and_b32 v3, 3, v248\n"
      "v_lshl_add_u32 v238, v3, 3, v238\n"
      "v_xor_b32 v3, 1, v2\n"
      "v_lshl_add_u32 v239, v3, 5, v238\n"
      "v_lshl_add_u32 v238, v2, 5, v238\n"
      "v_add_u32 v240, 131072, v236\n"
      "v_add_u32 v241, 131072, v237\n"
      "v_add_u32 v242, 0, v238\n"
      "v_add_u32 v243, 0, v239\n"
      "s_waitcnt vmcnt(8)\n"
      "s_barrier\n"
      "ds_read_b64_tr_b16 v[160:161], v242 offset:0\n"
      "ds_read_b64_tr_b16 v[162:163], v242 offset:256\n"
      "ds_read_b128 v[128:131], v240 offset:0\n"
      "ds_read_b64_tr_b16 v[164:165], v243 offset:0\n"
      "ds_read_b64_tr_b16 v[166:167], v243 offset:256\n"
      "ds_read_b64_tr_b16 v[168:169], v242 offset:512\n"
      "ds_read_b64_tr_b16 v[170:171], v242 offset:768\n"
      "ds_read_b64_tr_b16 v[172:173], v243 offset:512\n"
      "ds_read_b64_tr_b16 v[174:175], v243 offset:768\n"
      "ds_read_b64_tr_b16 v[176:177], v242 offset:16384\n"
      "ds_read_b64_tr_b16 v[178:179], v242 offset:16640\n"
      "ds_read_b64_tr_b16 v[180:181], v243 offset:16384\n"
      "ds_read_b64_tr_b16 v[182:183], v243 offset:16640\n"
      "ds_read_b64_tr_b16 v[184:185], v242 offset:16896\n"
      "ds_read_b64_tr_b16 v[186:187], v242 offset:17152\n"
      "ds_read_b64_tr_b16 v[188:189], v243 offset:16896\n"
      "ds_read_b64_tr_b16 v[190:191], v243 offset:17152\n"
      "ds_read_b128 v[132:135], v240 offset:2048\n"
      "ds_read_b128 v[136:139], v240 offset:4096\n"
      "ds_read_b128 v[140:143], v240 offset:6144\n"
      "ds_read_b128 v[144:147], v240 offset:8192\n"
      "ds_read_b128 v[148:151], v240 offset:10240\n"
      "ds_read_b128 v[152:155], v240 offset:12288\n"
      "ds_read_b128 v[156:159], v240 offset:14336\n"
      "s_waitcnt lgkmcnt(15)\n"
      "v_mfma_f32_16x16x32_bf16 a[0:3], v[160:163], v[128:131], 0\n"
      "ds_read_b64_tr_b16 v[96:97], v242 offset:8192\n"
      "v_lshrrev_b32 v0, 5, v248\n"
      "v_mfma_f32_16x16x32_bf16 a[4:7], v[164:167], v[128:131], 0\n"
      "v_mfma_f32_16x16x32_bf16 a[8:11], v[168:171], v[128:131], 0\n"
      "s_add_u32 s80, s62, 524288\n"
      "s_addc_u32 s81, s63, 0\n"
      "s_add_u32 m0, s82, 65536\n"
      "s_nop 0\n"
      "global_load_lds_dwordx4 v232, s[80:81] offset:0\n"
      "v_mfma_f32_16x16x32_bf16 a[12:15], v[172:175], v[128:131], 0\n"
      "ds_read_b64_tr_b16 v[98:99], v242 offset:8448\n"
      "v_mul_u32_u24 v0, 1280, v0\n"
      "s_waitcnt lgkmcnt(9)\n"
      "v_mfma_f32_16x16x32_bf16 a[128:131], v[128:131], v[176:179], 0\n"
      "v_mfma_f32_16x16x32_bf16 a[160:163], v[128:131], v[180:183], 0\n"
      "v_mfma_f32_16x16x32_bf16 a[192:195], v[128:131], v[184:187], 0\n"
      "ds_read_b64_tr_b16 v[100:101], v243 offset:8192\n"
      "global_load_lds_dwordx4 v233, s[80:81] offset:1024\n"
      "v_and_b32 v1, 31, v248\n"
      "v_mfma_f32_16x16x32_bf16 a[224:227], v[128:131], v[188:191], 0\n"
      "ds_read_b128 v[128:131], v241 offset:0\n"
      "s_waitcnt lgkmcnt(4)\n"
      "v_mfma_f32_16x16x32_bf16 a[16:19], v[160:163], v[132:135], 0\n"
      "v_mfma_f32_16x16x32_bf16 a[20:23], v[164:167], v[132:135], 0\n"
      "ds_read_b64_tr_b16 v[102:103], v243 offset:8448\n"
      "v_mfma_f32_16x16x32_bf16 a[24:27], v[168:171], v[132:135], 0\n"
      "global_load_lds_dwordx4 v234, s[80:81] offset:2048\n"
      "v_lshl_add_u32 v13, v1, 3, v0\n"
      "v_mfma_f32_16x16x32_bf16 a[28:31], v[172:175], v[132:135], 0\n"
      "v_mfma_f32_16x16x32_bf16 a[132:135], v[132:135], v[176:179], 0\n"
      "ds_read_b64_tr_b16 v[104:105], v242 offset:8704\n"
      "v_mfma_f32_16x16x32_bf16 a[164:167], v[132:135], v[180:183], 0\n"
      "global_load_dwordx2 v[212:213], v13, %21 offset:0\n"
      "v_mfma_f32_16x16x32_bf16 a[196:199], v[132:135], v[184:187], 0\n"
      "global_load_lds_dwordx4 v235, s[80:81] offset:3072\n"
      "v_mfma_f32_16x16x32_bf16 a[228:231], v[132:135], v[188:191], 0\n"
      "ds_read_b128 v[132:135], v241 offset:2048\n"
      "ds_read_b64_tr_b16 v[106:107], v242 offset:8960\n"
      "v_mfma_f32_16x16x32_bf16 a[32:35], v[160:163], v[136:139], 0\n"
      "global_load_dwordx2 v[214:215], v13, %21 offset:256\n"
      "v_mfma_f32_16x16x32_bf16 a[36:39], v[164:167], v[136:139], 0\n"
      "v_mfma_f32_16x16x32_bf16 a[40:43], v[168:171], v[136:139], 0\n"
      "ds_read_b64_tr_b16 v[108:109], v243 offset:8704\n"
      "s_add_u32 s80, s64, 524288\n"
      "s_addc_u32 s81, s65, 0\n"
      "s_add_u32 m0, s82, 81920\n"
      "s_nop 0\n"
      "global_load_lds_dwordx4 v232, s[80:81] offset:0\n"
      "v_mfma_f32_16x16x32_bf16 a[44:47], v[172:175], v[136:139], 0\n"
      "v_mfma_f32_16x16x32_bf16 a[136:139], v[136:139], v[176:179], 0\n"
      "global_load_dwordx2 v[216:217], v13, %21 offset:512\n"
      "v_mfma_f32_16x16x32_bf16 a[168:171], v[136:139], v[180:183], 0\n"
      "ds_read_b64_tr_b16 v[110:111], v243 offset:8960\n"
      "v_mfma_f32_16x16x32_bf16 a[200:203], v[136:139], v[184:187], 0\n"
      "global_load_lds_dwordx4 v233, s[80:81] offset:1024\n"
      "v_mfma_f32_16x16x32_bf16 a[232:235], v[136:139], v[188:191], 0\n"
      "ds_read_b128 v[136:139], v241 offset:4096\n"
      "global_load_dwordx2 v[218:219], v13, %21 offset:768\n"
      "v_mfma_f32_16x16x32_bf16 a[48:51], v[160:163], v[140:143], 0\n"
      "ds_read_b64_tr_b16 v[112:113], v242 offset:24576\n"
      "v_mfma_f32_16x16x32_bf16 a[52:55], v[164:167], v[140:143], 0\n"
      "v_mfma_f32_16x16x32_bf16 a[56:59], v[168:171], v[140:143], 0\n"
      "global_load_lds_dwordx4 v234, s[80:81] offset:2048\n"
      "global_load_dwordx2 v[220:221], v13, %21 offset:1024\n"
      "v_mfma_f32_16x16x32_bf16 a[60:63], v[172:175], v[140:143], 0\n"
      "ds_read_b64_tr_b16 v[114:115], v242 offset:24832\n"
      "v_mfma_f32_16x16x32_bf16 a[140:143], v[140:143], v[176:179], 0\n"
      "v_mfma_f32_16x16x32_bf16 a[172:175], v[140:143], v[180:183], 0\n"
      "v_mfma_f32_16x16x32_bf16 a[204:207], v[140:143], v[184:187], 0\n"
      "ds_read_b64_tr_b16 v[116:117], v243 offset:24576\n"
      "global_load_lds_dwordx4 v235, s[80:81] offset:3072\n"
      "s_and_b32 s76, %12, 1\n"
      "v_mfma_f32_16x16x32_bf16 a[236:239], v[140:143], v[188:191], 0\n"
      "ds_read_b128 v[140:143], v241 offset:6144\n"
      "v_mfma_f32_16x16x32_bf16 a[64:67], v[160:163], v[144:147], 0\n"
      "v_mfma_f32_16x16x32_bf16 a[68:71], v[164:167], v[144:147], 0\n"
      "ds_read_b64_tr_b16 v[118:119], v243 offset:24832\n"
      "s_sub_u32 s77, 15, %14\n"
      "v_mfma_f32_16x16x32_bf16 a[72:75], v[168:171], v[144:147], 0\n"
      "s_add_u32 s80, s56, 256\n"
      "s_addc_u32 s81, s57, 0\n"
      "s_add_u32 m0, s82, 98304\n"
      "s_nop 0\n"
      "global_load_lds_dwordx4 v224, s[80:81] offset:0\n"
      "v_mfma_f32_16x16x32_bf16 a[76:79], v[172:175], v[144:147], 0\n"
      "v_mfma_f32_16x16x32_bf16 a[144:147], v[144:147], v[176:179], 0\n"
      "ds_read_b64_tr_b16 v[120:121], v242 offset:25088\n"
      "v_mfma_f32_16x16x32_bf16 a[176:179], v[144:147], v[180:183], 0\n"
      "s_cmp_lt_u32 %12, 2\n s_cselect_b32 s77, s77, %14\n"
      "v_mfma_f32_16x16x32_bf16 a[208:211], v[144:147], v[184:187], 0\n"
      "global_load_lds_dwordx4 v225, s[80:81] offset:1024\n"
      "v_mfma_f32_16x16x32_bf16 a[240:243], v[144:147], v[188:191], 0\n"
      "ds_read_b128 v[144:147], v241 offset:8192\n"
      "ds_read_b64_tr_b16 v[122:123], v242 offset:25344\n"
      "v_mfma_f32_16x16x32_bf16 a[80:83], v[160:163], v[148:151], 0\n"
      "s_lshl_b32 s77, s77, 15\n"
      "v_mfma_f32_16x16x32_bf16 a[84:87], v[164:167], v[148:151], 0\n"
      "v_mfma_f32_16x16x32_bf16 a[88:91], v[168:171], v[148:151], 0\n"
      "ds_read_b64_tr_b16 v[124:125], v243 offset:25088\n"
      "global_load_lds_dwordx4 v226, s[80:81] offset:2048\n"
      "v_mfma_f32_16x16x32_bf16 a[92:95], v[172:175], v[148:151], 0\n"
      "v_add_u32 v2, s77, v244\n"
      "v_mfma_f32_16x16x32_bf16 a[148:151], v[148:151], v[176:179], 0\n"
      "v_mfma_f32_16x16x32_bf16 a[180:183], v[148:151], v[180:183], 0\n"
      "ds_read_b64_tr_b16 v[126:127], v243 offset:25344\n"
      "v_mfma_f32_16x16x32_bf16 a[212:215], v[148:151], v[184:187], 0\n"
      "global_load_lds_dwordx4 v227, s[80:81] offset:3072\n"
      "v_mfma_f32_16x16x32_bf16 a[244:247], v[148:151], v[188:191], 0\n"
      "ds_read_b128 v[148:151], v241 offset:10240\n"
      "s_lshl_b32 s77, s76, 11\n"
      "v_mfma_f32_16x16x32_bf16 a[96:99], v[160:163], v[152:155], 0\n"
      "v_mfma_f32_16x16x32_bf16 a[100:103], v[164:167], v[152:155], 0\n"
      "v_mfma_f32_16x16x32_bf16 a[104:107], v[168:171], v[152:155], 0\n"
      "s_add_u32 s80, s58, 256\n"
      "s_addc_u32 s81, s59, 0\n"
      "s_add_u32 m0, s82, 114688\n"
      "s_nop 0\n"
      "global_load_lds_dwordx4 v224, s[80:81] offset:0\n"
      "v_add_u32 v246, s77, v2\n"
      "v_mfma_f32_16x16x32_bf16 a[108:111], v[172:175], v[152:155], 0\n"
      "v_mfma_f32_16x16x32_bf16 a[152:155], v[152:155], v[176:179], 0\n"
      "v_mfma_f32_16x16x32_bf16 a[184:187], v[152:155], v[180:183], 0\n"
      "s_lshl_b32 s88, s76, 13\n"
      "v_mfma_f32_16x16x32_bf16 a[216:219], v[152:155], v[184:187], 0\n"
      "global_load_lds_dwordx4 v225, s[80:81] offset:1024\n"
      "v_mfma_f32_16x16x32_bf16 a[248:251], v[152:155], v[188:191], 0\n"
      "ds_read_b128 v[152:155], v241 offset:12288\n"
      "v_mfma_f32_16x16x32_bf16 a[112:115], v[160:163], v[156:159], 0\n"
      "v_mfma_f32_16x16x32_bf16 a[116:119], v[164:167], v[156:159], 0\n"
      "v_add_u32 v247, s88, v2\n"
      "v_mfma_f32_16x16x32_bf16 a[120:123], v[168:171], v[156:159], 0\n"
      "global_load_lds_dwordx4 v226, s[80:81] offset:2048\n"
      "v_mfma_f32_16x16x32_bf16 a[124:127], v[172:175], v[156:159], 0\n"
      "v_mfma_f32_16x16x32_bf16 a[156:159], v[156:159], v[176:179], 0\n"
      "s_xor_b32 s89, s88, 8192\n"
      "v_mfma_f32_16x16x32_bf16 a[188:191], v[156:159], v[180:183], 0\n"
      "v_mfma_f32_16x16x32_bf16 a[220:223], v[156:159], v[184:187], 0\n"
      "global_load_lds_dwordx4 v227, s[80:81] offset:3072\n"
      "v_mfma_f32_16x16x32_bf16 a[252:255], v[156:159], v[188:191], 0\n"
      "ds_read_b128 v[156:159], v241 offset:14336\n"
      "s_waitcnt lgkmcnt(14)\n"
      "v_mfma_f32_16x16x32_bf16 a[0:3], v[96:99], v[128:131], a[0:3]\n"
      "v_mfma_f32_16x16x32_bf16 a[4:7], v[100:103], v[128:131], a[4:7]\n"
      "v_mfma_f32_16x16x32_bf16 a[8:11], v[104:107], v[128:131], a[8:11]\n"
      "v_mfma_f32_16x16x32_bf16 a[12:15], v[108:111], v[128:131], a[12:15]\n"
      "s_waitcnt lgkmcnt(5)\n"
      "v_mfma_f32_16x16x32_bf16 a[128:131], v[128:131], v[112:115], a[128:131]\n"
      "v_mfma_f32_16x16x32_bf16 a[160:163], v[128:131], v[116:119], a[160:163]\n"
      "v_mfma_f32_16x16x32_bf16 a[192:195], v[128:131], v[120:123], a[192:195]\n"
      "s_waitcnt lgkmcnt(0)\n"
      "v_mfma_f32_16x16x32_bf16 a[224:227], v[128:131], v[124:127], a[224:227]\n"
      "s_waitcnt vmcnt(8) lgkmcnt(0)\n"
      "s_barrier\n"
      "v_add_u32 v240, 32768, v236\n"
      "v_add_u32 v241, 32768, v237\n"
      "v_add_u32 v242, 65536, v238\n"
      "v_add_u32 v243, 65536, v239\n"
      "ds_read_b128 v[128:131], v240 offset:0\n"
      "v_mfma_f32_16x16x32_bf16 a[16:19], v[96:99], v[132:135], a[16:19]\n"
      "ds_read_b64_tr_b16 v[160:161], v242 offset:0\n"
      "s_mul_i32 s76, %14, 80\n"
      "v_mfma_f32_16x16x32_bf16 a[20:23], v[100:103], v[132:135], a[20:23]\n"
      "v_mfma_f32_16x16x32_bf16 a[24:27], v[104:107], v[132:135], a[24:27]\n"
      "ds_read_b64_tr_b16 v[162:163], v242 offset:256\n"
      "s_mul_i32 s77, %12, 20\n"
      "v_mfma_f32_16x16x32_bf16 a[28:31], v[108:111], v[132:135], a[28:31]\n"
      "s_add_u32 s80, s62, 1048576\n"
      "s_addc_u32 s81, s63, 0\n"
      "s_add_u32 m0, s82, 131072\n"
      "s_nop 0\n"
      "global_load_lds_dwordx4 v232, s[80:81] offset:0\n"
      "v_mfma_f32_16x16x32_bf16 a[132:135], v[132:135], v[112:115], a[132:135]\n"
      "v_mfma_f32_16x16x32_bf16 a[164:167], v[132:135], v[116:119], a[164:167]\n"
      "ds_read_b64_tr_b16 v[164:165], v243 offset:0\n"
      "s_add_u32 s76, s76, s77\n"
      "v_mfma_f32_16x16x32_bf16 a[196:199], v[132:135], v[120:123], a[196:199]\n"
      "v_mfma_f32_16x16x32_bf16 a[228:231], v[132:135], v[124:127], a[228:231]\n"
      "ds_read_b128 v[132:135], v240 offset:2048\n"
      "ds_read_b64_tr_b16 v[166:167], v243 offset:256\n"
      "v_mfma_f32_16x16x32_bf16 a[32:35], v[96:99], v[136:139], a[32:35]\n"
      "v_lshrrev_b32 v0, 4, v248\n"
      "v_mfma_f32_16x16x32_bf16 a[36:39], v[100:103], v[136:139], a[36:39]\n"
      "v_mfma_f32_16x16x32_bf16 a[40:43], v[104:107], v[136:139], a[40:43]\n"
      "ds_read_b64_tr_b16 v[168:169], v242 offset:512\n"
      "global_load_lds_dwordx4 v233, s[80:81] offset:1024\n"
      "v_mfma_f32_16x16x32_bf16 a[44:47], v[108:111], v[136:139], a[44:47]\n"
      "v_add_u32 v2, s76, v0\n"
      "v_mfma_f32_16x16x32_bf16 a[136:139], v[136:139], v[112:115], a[136:139]\n"
      "v_mfma_f32_16x16x32_bf16 a[168:171], v[136:139], v[116:119], a[168:171]\n"
      "ds_read_b64_tr_b16 v[170:171], v242 offset:768\n"
      "v_mfma_f32_16x16x32_bf16 a[200:203], v[136:139], v[120:123], a[200:203]\n"
      "v_lshlrev_b32 v2, 13, v2\n"
      "v_mfma_f32_16x16x32_bf16 a[232:235], v[136:139], v[124:127], a[232:235]\n"
      "ds_read_b128 v[136:139], v240 offset:4096\n"
      "ds_read_b64_tr_b16 v[172:173], v243 offset:512\n"
      "v_mfma_f32_16x16x32_bf16 a[48:51], v[96:99], v[140:143], a[48:51]\n"
      "v_mfma_f32_16x16x32_bf16 a[52:55], v[100:103], v[140:143], a[52:55]\n"
      "global_load_lds_dwordx4 v234, s[80:81] offset:2048\n"
      "v_and_b32 v1, 15, v248\n"
      "v_mfma_f32_16x16x32_bf16 a[56:59], v[104:107], v[140:143], a[56:59]\n"
      "ds_read_b64_tr_b16 v[174:175], v243 offset:768\n"
      "v_mfma_f32_16x16x32_bf16 a[60:63], v[108:111], v[140:143], a[60:63]\n"
      "v_mfma_f32_16x16x32_bf16 a[140:143], v[140:143], v[112:115], a[140:143]\n"
      "v_lshl_add_u32 v2, v1, 4, v2\n"
      "v_mfma_f32_16x16x32_bf16 a[172:175], v[140:143], v[116:119], a[172:175]\n"
      "ds_read_b64_tr_b16 v[176:177], v242 offset:16384\n"
      "v_mfma_f32_16x16x32_bf16 a[204:207], v[140:143], v[120:123], a[204:207]\n"
      "v_mfma_f32_16x16x32_bf16 a[236:239], v[140:143], v[124:127], a[236:239]\n"
      "ds_read_b128 v[140:143], v240 offset:6144\n"
      "ds_read_b64_tr_b16 v[178:179], v242 offset:16640\n"
      "v_add_u32 v8, 4096, v2\n"
      "v_mfma_f32_16x16x32_bf16 a[64:67], v[96:99], v[144:147], a[64:67]\n"
      "global_load_lds_dwordx4 v235, s[80:81] offset:3072\n"
      "v_mfma_f32_16x16x32_bf16 a[68:71], v[100:103], v[144:147], a[68:71]\n"
      "v_mfma_f32_16x16x32_bf16 a[72:75], v[104:107], v[144:147], a[72:75]\n"
      "ds_read_b64_tr_b16 v[180:181], v243 offset:16384\n"
      "v_add_u32 v9, 36864, v2\n"
      "v_mfma_f32_16x16x32_bf16 a[76:79], v[108:111], v[144:147], a[76:79]\n"
      "v_mfma_f32_16x16x32_bf16 a[144:147], v[144:147], v[112:115], a[144:147]\n"
      "ds_read_b64_tr_b16 v[182:183], v243 offset:16640\n"
      "v_mfma_f32_16x16x32_bf16 a[176:179], v[144:147], v[116:119], a[176:179]\n"
      "v_add_u32 v10, 69632, v2\n"
      "v_mfma_f32_16x16x32_bf16 a[208:211], v[144:147], v[120:123], a[208:211]\n"
      "v_mfma_f32_16x16x32_bf16 a[240:243], v[144:147], v[124:127], a[240:243]\n"
      "ds_read_b128 v[144:147], v240 offset:8192\n"
      "ds_read_b64_tr_b16 v[184:185], v242 offset:16896\n"
      "s_add_u32 s80, s64, 1048576\n"
      "s_addc_u32 s81, s65, 0\n"
      "s_add_u32 m0, s82, 147456\n"
      "s_nop 0\n"
      "global_load_lds_dwordx4 v232, s[80:81] offset:0\n"
      "v_mfma_f32_16x16x32_bf16 a[80:83], v[96:99], v[148:151], a[80:83]\n"
      "v_add_u32 v11, 102400, v2\n"
      "v_mfma_f32_16x16x32_bf16 a[84:87], v[100:103], v[148:151], a[84:87]\n"
      "v_mfma_f32_16x16x32_bf16 a[88:91], v[104:107], v[148:151], a[88:91]\n"
      "ds_read_b64_tr_b16 v[186:187], v242 offset:17152\n"
      "v_mfma_f32_16x16x32_bf16 a[92:95], v[108:111], v[148:151], a[92:95]\n"
      "v_add_u32 v12, 135168, v2\n"
      "v_mfma_f32_16x16x32_bf16 a[148:151], v[148:151], v[112:115], a[148:151]\n"
      "ds_read_b64_tr_b16 v[188:189], v243 offset:16896\n"
      "v_mfma_f32_16x16x32_bf16 a[180:183], v[148:151], v[116:119], a[180:183]\n"
      "v_mfma_f32_16x16x32_bf16 a[212:215], v[148:151], v[120:123], a[212:215]\n"
      "global_load_lds_dwordx4 v233, s[80:81] offset:1024\n"
      "v_mov_b32 v250, 0x3f803f80\n"
      "v_mfma_f32_16x16x32_bf16 a[244:247], v[148:151], v[124:127], a[244:247]\n"
      "ds_read_b128 v[148:151], v240 offset:10240\n"
      "ds_read_b64_tr_b16 v[190:191], v243 offset:17152\n"
      "v_mfma_f32_16x16x32_bf16 a[96:99], v[96:99], v[152:155], a[96:99]\n"
      "v_mfma_f32_16x16x32_bf16 a[100:103], v[100:103], v[152:155], a[100:103]\n"
      "v_mov_b32 v251, 0x3f803f80\n"
      "v_mfma_f32_16x16x32_bf16 a[104:107], v[104:107], v[152:155], a[104:107]\n"
      "v_mfma_f32_16x16x32_bf16 a[108:111], v[108:111], v[152:155], a[108:111]\n"
      "v_mfma_f32_16x16x32_bf16 a[152:155], v[152:155], v[112:115], a[152:155]\n"
      "v_mov_b32 v252, 0x3f803f80\n"
      "v_mfma_f32_16x16x32_bf16 a[184:187], v[152:155], v[116:119], a[184:187]\n"
      "global_load_lds_dwordx4 v234, s[80:81] offset:2048\n"
      "v_mfma_f32_16x16x32_bf16 a[216:219], v[152:155], v[120:123], a[216:219]\n"
      "v_mfma_f32_16x16x32_bf16 a[248:251], v[152:155], v[124:127], a[248:251]\n"
      "ds_read_b128 v[152:155], v240 offset:12288\n"
      "v_mov_b32 v253, 0x3f803f80\n"
      "v_mfma_f32_16x16x32_bf16 a[112:115], v[96:99], v[156:159], a[112:115]\n"
      "v_mfma_f32_16x16x32_bf16 a[116:119], v[100:103], v[156:159], a[116:119]\n"
      "v_mfma_f32_16x16x32_bf16 a[120:123], v[104:107], v[156:159], a[120:123]\n"
      "s_and_b32 s53, %20, 31\n s_andn2_b32 s54, %20, 31\n s_sub_u32 s54, s54, 32\n v_lshl_add_u32 v245, v248, 5, s53\n v_mov_b32 v0, %20\n v_cmp_gt_u32 vcc, 8, v248\n s_nop 1\n v_cndmask_b32 v245, v0, v245, vcc\n v_add_u32 v0, s54, v248\n v_cmp_lt_u32 vcc, 31, v248\n s_nop 1\n v_cndmask_b32 v245, v245, v0, vcc\n s_add_u32 s53, s53, 32\n v_mov_b32 v249, %14\n v_mov_b32 v0, s53\n v_cndmask_b32 v249, v249, v0, vcc\n v_lshl_add_u32 v249, v245, 6, v249\n v_lshlrev_b32 v249, 2, v249\n v_add_u32 v249, 4096, v249\n v_lshlrev_b32 v245, 2, v245\n"
      "v_mfma_f32_16x16x32_bf16 a[124:127], v[108:111], v[156:159], a[124:127]\n"
      "v_mfma_f32_16x16x32_bf16 a[156:159], v[156:159], v[112:115], a[156:159]\n"
      "global_load_lds_dwordx4 v235, s[80:81] offset:3072\n"
      "v_mfma_f32_16x16x32_bf16 a[188:191], v[156:159], v[116:119], a[188:191]\n"
      "s_cmp_eq_u32 %12, 0\n s_cbranch_scc1 L_tok_%=\n L_tokback_%=:\n"
      "v_mfma_f32_16x16x32_bf16 a[220:223], v[156:159], v[120:123], a[220:223]\n"
      "v_mfma_f32_16x16x32_bf16 a[252:255], v[156:159], v[124:127], a[252:255]\n"
      "ds_read_b128 v[156:159], v240 offset:14336\n"
      "s_waitcnt lgkmcnt(15)\n"
      "v_mfma_f32_16x16x32_bf16 a[0:3], v[160:163], v[128:131], a[0:3]\n"
      "ds_read_b64_tr_b16 v[96:97], v242 offset:8192\n"
      "global_load_dwordx4 v[4:7], v8, s[70:71]\n"
      "v_mfma_f32_16x16x32_bf16 a[4:7], v[164:167], v[128:131], a[4:7]\n"
      "v_mfma_f32_16x16x32_bf16 a[8:11], v[168:171], v[128:131], a[8:11]\n"
      "s_waitcnt lgkmcnt(8)\n"
      "v_mfma_f32_16x16x32_bf16 a[12:15], v[172:175], v[128:131], a[12:15]\n"
      "ds_read_b64_tr_b16 v[98:99], v242 offset:8448\n"
      "v_mfma_f32_16x16x32_bf16 a[128:131], v[128:131], v[176:179], a[128:131]\n"
      "s_add_u32 s80, s56, 384\n"
      "s_addc_u32 s81, s57, 0\n"
      "s_add_u32 m0, s82, 0\n"
      "s_nop 0\n"
      "global_load_lds_dwordx4 v224, s[80:81] offset:0\n"
      "v_mfma_f32_16x16x32_bf16 a[160:163], v[128:131], v[180:183], a[160:163]\n"
      "s_waitcnt lgkmcnt(1)\n"
      "v_mfma_f32_16x16x32_bf16 a[192:195], v[128:131], v[184:187], a[192:195]\n"
      "ds_read_b64_tr_b16 v[100:101], v243 offset:8192\n"
      "v_mfma_f32_16x16x32_bf16 a[224:227], v[128:131], v[188:191], a[224:227]\n"
      "ds_read_b128 v[128:131], v241 offset:0\n"
      "v_mfma_f32_16x16x32_bf16 a[16:19], v[160:163], v[132:135], a[16:19]\n"
      "v_mfma_f32_16x16x32_bf16 a[20:23], v[164:167], v[132:135], a[20:23]\n"
      "ds_read_b64_tr_b16 v[102:103], v243 offset:8448\n"
      "v_mfma_f32_16x16x32_bf16 a[24:27], v[168:171], v[132:135], a[24:27]\n"
      "v_mfma_f32_16x16x32_bf16 a[28:31], v[172:175], v[132:135], a[28:31]\n"
      "v_mfma_f32_16x16x32_bf16 a[132:135], v[132:135], v[176:179], a[132:135]\n"
      "ds_read_b64_tr_b16 v[104:105], v242 offset:8704\n"
      "global_load_lds_dwordx4 v225, s[80:81] offset:1024\n"
      "v_mfma_f32_16x16x32_bf16 a[164:167], v[132:135], v[180:183], a[164:167]\n"
      "v_mfma_f32_16x16x32_bf16 a[196:199], v[132:135], v[184:187], a[196:199]\n"
      "v_mfma_f32_16x16x32_bf16 a[228:231], v[132:135], v[188:191], a[228:231]\n"
      "ds_read_b128 v[132:135], v241 offset:2048\n"
      "ds_read_b64_tr_b16 v[106:107], v242 offset:8960\n"
      "v_mfma_f32_16x16x32_bf16 a[32:35], v[160:163], v[136:139], a[32:35]\n"
      "v_mfma_f32_16x16x32_bf16 a[36:39], v[164:167], v[136:139], a[36:39]\n"
      "v_mfma_f32_16x16x32_bf16 a[40:43], v[168:171], v[136:139], a[40:43]\n"
      "ds_read_b64_tr_b16 v[108:109], v243 offset:8704\n"
      "v_mfma_f32_16x16x32_bf16 a[44:47], v[172:175], v[136:139], a[44:47]\n"
      "v_mfma_f32_16x16x32_bf16 a[136:139], v[136:139], v[176:179], a[136:139]\n"
      "global_load_lds_dwordx4 v226, s[80:81] offset:2048\n"
      "v_mfma_f32_16x16x32_bf16 a[168:171], v[136:139], v[180:183], a[168:171]\n"
      "ds_read_b64_tr_b16 v[110:111], v243 offset:8960\n"
      "v_mfma_f32_16x16x32_bf16 a[200:203], v[136:139], v[184:187], a[200:203]\n"
      "v_mfma_f32_16x16x32_bf16 a[232:235], v[136:139], v[188:191], a[232:235]\n"
      "ds_read_b128 v[136:139], v241 offset:4096\n"
      "v_mfma_f32_16x16x32_bf16 a[48:51], v[160:163], v[140:143], a[48:51]\n"
      "ds_read_b64_tr_b16 v[112:113], v242 offset:24576\n"
      "v_mfma_f32_16x16x32_bf16 a[52:55], v[164:167], v[140:143], a[52:55]\n"
      "v_mfma_f32_16x16x32_bf16 a[56:59], v[168:171], v[140:143], a[56:59]\n"
      "v_mfma_f32_16x16x32_bf16 a[60:63], v[172:175], v[140:143], a[60:63]\n"
      "ds_read_b64_tr_b16 v[114:115], v242 offset:24832\n"
      "v_mfma_f32_16x16x32_bf16 a[140:143], v[140:143], v[176:179], a[140:143]\n"
      "global_load_lds_dwordx4 v227, s[80:81] offset:3072\n"
      "v_mfma_f32_16x16x32_bf16 a[172:175], v[140:143], v[180:183], a[172:175]\n"
      "v_mfma_f32_16x16x32_bf16 a[204:207], v[140:143], v[184:187], a[204:207]\n"
      "ds_read_b64_tr_b16 v[116:117], v243 offset:24576\n"
      "v_mfma_f32_16x16x32_bf16 a[236:239], v[140:143], v[188:191], a[236:239]\n"
      "ds_read_b128 v[140:143], v241 offset:6144\n"
      "v_mfma_f32_16x16x32_bf16 a[64:67], v[160:163], v[144:147], a[64:67]\n"
      "global_load_dwordx4 v[4:7], v9, s[70:71]\n"
      "v_mfma_f32_16x16x32_bf16 a[68:71], v[164:167], v[144:147], a[68:71]\n"
      "ds_read_b64_tr_b16 v[118:119], v243 offset:24832\n"
      "v_mfma_f32_16x16x32_bf16 a[72:75], v[168:171], v[144:147], a[72:75]\n"
      "v_mfma_f32_16x16x32_bf16 a[76:79], v[172:175], v[144:147], a[76:79]\n"
      "v_mfma_f32_16x16x32_bf16 a[144:147], v[144:147], v[176:179], a[144:147]\n"
      "ds_read_b64_tr_b16 v[120:121], v242 offset:25088\n"
      "s_add_u32 s80, s58, 384\n"
      "s_addc_u32 s81, s59, 0\n"
      "s_add_u32 m0, s82, 16384\n"
      "s_nop 0\n"
      "global_load_lds_dwordx4 v224, s[80:81] offset:0\n"
      "v_mfma_f32_16x16x32_bf16 a[176:179], v[144:147], v[180:183], a[176:179]\n"
      "v_mfma_f32_16x16x32_bf16 a[208:211], v[144:147], v[184:187], a[208:211]\n"
      "v_mfma_f32_16x16x32_bf16 a[240:243], v[144:147], v[188:191], a[240:243]\n"
      "ds_read_b128 v[144:147], v241 offset:8192\n"
      "ds_read_b64_tr_b16 v[122:123], v242 offset:25344\n"
      "v_mfma_f32_16x16x32_bf16 a[80:83], v[160:163], v[148:151], a[80:83]\n"
      "v_mfma_f32_16x16x32_bf16 a[84:87], v[164:167], v[148:151], a[84:87]\n"
      "v_mfma_f32_16x16x32_bf16 a[88:91], v[168:171], v[148:151], a[88:91]\n"
      "ds_read_b64_tr_b16 v[124:125], v243 offset:25088\n"
      "v_mfma_f32_16x16x32_bf16 a[92:95], v[172:175], v[148:151], a[92:95]\n"
      "v_mfma_f32_16x16x32_bf16 a[148:151], v[148:151], v[176:179], a[148:151]\n"
      "global_load_lds_dwordx4 v225, s[80:81] offset:1024\n"
      "v_mfma_f32_16x16x32_bf16 a[180:183], v[148:151], v[180:183], a[180:183]\n"
      "ds_read_b64_tr_b16 v[126:127], v243 offset:25344\n"
      "v_mfma_f32_16x16x32_bf16 a[212:215], v[148:151], v[184:187], a[212:215]\n"
      "v_mfma_f32_16x16x32_bf16 a[244:247], v[148:151], v[188:191], a[244:247]\n"
      "ds_read_b128 v[148:151], v241 offset:10240\n"
      "v_mfma_f32_16x16x32_bf16 a[96:99], v[160:163], v[152:155], a[96:99]\n"
      "v_mfma_f32_16x16x32_bf16 a[100:103], v[164:167], v[152:155], a[100:103]\n"
      "v_mfma_f32_16x16x32_bf16 a[104:107], v[168:171], v[152:155], a[104:107]\n"
      "v_mfma_f32_16x16x32_bf16 a[108:111], v[172:175], v[152:155], a[108:111]\n"
      "v_mfma_f32_16x16x32_bf16 a[152:155], v[152:155], v[176:179], a[152:155]\n"
      "global_load_lds_dwordx4 v226, s[80:81] offset:2048\n"
      "v_mfma_f32_16x16x32_bf16 a[184:187], v[152:155], v[180:183], a[184:187]\n"
      "v_mfma_f32_16x16x32_bf16 a[216:219], v[152:155], v[184:187], a[216:219]\n"
      "v_mfma_f32_16x16x32_bf16 a[248:251], v[152:155], v[188:191], a[248:251]\n"
      "ds_read_b128 v[152:155], v241 offset:12288\n"
      "v_mfma_f32_16x16x32_bf16 a[112:115], v[160:163], v[156:159], a[112:115]\n"
      "v_mfma_f32_16x16x32_bf16 a[116:119], v[164:167], v[156:159], a[116:119]\n"
      "v_mfma_f32_16x16x32_bf16 a[120:123], v[168:171], v[156:159], a[120:123]\n"
      "v_mfma_f32_16x16x32_bf16 a[124:127], v[172:175], v[156:159], a[124:127]\n"
      "v_mfma_f32_16x16x32_bf16 a[156:159], v[156:159], v[176:179], a[156:159]\n"
      "global_load_lds_dwordx4 v227, s[80:81] offset:3072\n"
      "v_mfma_f32_16x16x32_bf16 a[188:191], v[156:159], v[180:183], a[188:191]\n"
      "v_mfma_f32_16x16x32_bf16 a[220:223], v[156:159], v[184:187], a[220:223]\n"
      "v_mfma_f32_16x16x32_bf16 a[252:255], v[156:159], v[188:191], a[252:255]\n"
      "ds_read_b128 v[156:159], v241 offset:14336\n"
      "s_waitcnt lgkmcnt(14)\n"
      "v_mfma_f32_16x16x32_bf16 a[0:3], v[96:99], v[128:131], a[0:3]\n"
      "v_mfma_f32_16x16x32_bf16 a[4:7], v[100:103], v[128:131], a[4:7]\n"
      "v_mfma_f32_16x16x32_bf16 a[8:11], v[104:107], v[128:131], a[8:11]\n"
      "v_mfma_f32_16x16x32_bf16 a[12:15], v[108:111], v[128:131], a[12:15]\n"
      "s_waitcnt lgkmcnt(5)\n"
      "v_mfma_f32_16x16x32_bf16 a[128:131], v[128:131], v[112:115], a[128:131]\n"
      "v_mfma_f32_16x16x32_bf16 a[160:163], v[128:131], v[116:119], a[160:163]\n"
      "v_mfma_f32_16x16x32_bf16 a[192:195], v[128:131], v[120:123], a[192:195]\n"
      "s_waitcnt lgkmcnt(0)\n"
      "v_mfma_f32_16x16x32_bf16 a[224:227], v[128:131], v[124:127], a[224:227]\n"
      "s_waitcnt vmcnt(10) lgkmcnt(0)\n"
      "s_barrier\n"
      "v_add_u32 v240, 98304, v236\n"
      "v_add_u32 v241, 98304, v237\n"
      "v_add_u32 v242, 131072, v238\n"
      "v_add_u32 v243, 131072, v239\n"
      "ds_read_b128 v[128:131], v240 offset:0\n"
      "v_mfma_f32_16x16x32_bf16 a[16:19], v[96:99], v[132:135], a[16:19]\n"
      "ds_read_b64_tr_b16 v[160:161], v242 offset:0\n"
      "global_load_dwordx4 v[4:7], v10, s[70:71]\n"
      "v_mfma_f32_16x16x32_bf16 a[20:23], v[100:103], v[132:135], a[20:23]\n"
      "v_mfma_f32_16x16x32_bf16 a[24:27], v[104:107], v[132:135], a[24:27]\n"
      "ds_read_b64_tr_b16 v[162:163], v242 offset:256\n"
      "v_mfma_f32_16x16x32_bf16 a[28:31], v[108:111], v[132:135], a[28:31]\n"
      "s_add_u32 s80, s62, 1572864\n"
      "s_addc_u32 s81, s63, 0\n"
      "s_add_u32 m0, s82, 32768\n"
      "s_nop 0\n"
      "global_load_lds_dwordx4 v232, s[80:81] offset:0\n"
      "v_mfma_f32_16x16x32_bf16 a[132:135], v[132:135], v[112:115], a[132:135]\n"
      "v_mfma_f32_16x16x32_bf16 a[164:167], v[132:135], v[116:119], a[164:167]\n"
      "ds_read_b64_tr_b16 v[164:165], v243 offset:0\n"
      "v_mfma_f32_16x16x32_bf16 a[196:199], v[132:135], v[120:123], a[196:199]\n"
      "v_mfma_f32_16x16x32_bf16 a[228:231], v[132:135], v[124:127], a[228:231]\n"
      "ds_read_b128 v[132:135], v240 offset:2048\n"
      "ds_read_b64_tr_b16 v[166:167], v243 offset:256\n"
      "v_mfma_f32_16x16x32_bf16 a[32:35], v[96:99], v[136:139], a[32:35]\n"
      "v_mfma_f32_16x16x32_bf16 a[36:39], v[100:103], v[136:139], a[36:39]\n"
      "v_mfma_f32_16x16x32_bf16 a[40:43], v[104:107], v[136:139], a[40:43]\n"
      "ds_read_b64_tr_b16 v[168:169], v242 offset:512\n"
      "global_load_lds_dwordx4 v233, s[80:81] offset:1024\n"
      "v_mfma_f32_16x16x32_bf16 a[44:47], v[108:111], v[136:139], a[44:47]\n"
      "v_mfma_f32_16x16x32_bf16 a[136:139], v[136:139], v[112:115], a[136:139]\n"
      "v_mfma_f32_16x16x32_bf16 a[168:171], v[136:139], v[116:119], a[168:171]\n"
      "ds_read_b64_tr_b16 v[170:171], v242 offset:768\n"
      "v_mfma_f32_16x16x32_bf16 a[200:203], v[136:139], v[120:123], a[200:203]\n"
      "v_mfma_f32_16x16x32_bf16 a[232:235], v[136:139], v[124:127], a[232:235]\n"
      "ds_read_b128 v[136:139], v240 offset:4096\n"
      "ds_read_b64_tr_b16 v[172:173], v243 offset:512\n"
      "v_mfma_f32_16x16x32_bf16 a[48:51], v[96:99], v[140:143], a[48:51]\n"
      "v_mfma_f32_16x16x32_bf16 a[52:55], v[100:103], v[140:143], a[52:55]\n"
      "global_load_lds_dwordx4 v234, s[80:81] offset:2048\n"
      "v_mfma_f32_16x16x32_bf16 a[56:59], v[104:107], v[140:143], a[56:59]\n"
      "ds_read_b64_tr_b16 v[174:175], v243 offset:768\n"
      "v_mfma_f32_16x16x32_bf16 a[60:63], v[108:111], v[140:143], a[60:63]\n"
      "v_mfma_f32_16x16x32_bf16 a[140:143], v[140:143], v[112:115], a[140:143]\n"
      "v_mfma_f32_16x16x32_bf16 a[172:175], v[140:143], v[116:119], a[172:175]\n"
      "ds_read_b64_tr_b16 v[176:177], v242 offset:16384\n"
      "v_mfma_f32_16x16x32_bf16 a[204:207], v[140:143], v[120:123], a[204:207]\n"
      "v_mfma_f32_16x16x32_bf16 a[236:239], v[140:143], v[124:127], a[236:239]\n"
      "ds_read_b128 v[140:143], v240 offset:6144\n"
      "ds_read_b64_tr_b16 v[178:179], v242 offset:16640\n"
      "v_mfma_f32_16x16x32_bf16 a[64:67], v[96:99], v[144:147], a[64:67]\n"
      "global_load_lds_dwordx4 v235, s[80:81] offset:3072\n"
      "v_mfma_f32_16x16x32_bf16 a[68:71], v[100:103], v[144:147], a[68:71]\n"
      "v_mfma_f32_16x16x32_bf16 a[72:75], v[104:107], v[144:147], a[72:75]\n"
      "ds_read_b64_tr_b16 v[180:181], v243 offset:16384\n"
      "v_mfma_f32_16x16x32_bf16 a[76:79], v[108:111], v[144:147], a[76:79]\n"
      "v_mfma_f32_16x16x32_bf16 a[144:147], v[144:147], v[112:115], a[144:147]\n"
      "ds_read_b64_tr_b16 v[182:183], v243 offset:16640\n"
      "global_load_dwordx4 v[4:7], v11, s[70:71]\n"
      "v_mfma_f32_16x16x32_bf16 a[176:179], v[144:147], v[116:119], a[176:179]\n"
      "v_mfma_f32_16x16x32_bf16 a[208:211], v[144:147], v[120:123], a[208:211]\n"
      "v_mfma_f32_16x16x32_bf16 a[240:243], v[144:147], v[124:127], a[240:243]\n"
      "ds_read_b128 v[144:147], v240 offset:8192\n"
      "ds_read_b64_tr_b16 v[184:185], v242 offset:16896\n"
      "s_add_u32 s80, s64, 1572864\n"
      "s_addc_u32 s81, s65, 0\n"
      "s_add_u32 m0, s82, 49152\n"
      "s_nop 0\n"
      "global_load_lds_dwordx4 v232, s[80:81] offset:0\n"
      "v_mfma_f32_16x16x32_bf16 a[80:83], v[96:99], v[148:151], a[80:83]\n"
      "v_mfma_f32_16x16x32_bf16 a[84:87], v[100:103], v[148:151], a[84:87]\n"
      "v_mfma_f32_16x16x32_bf16 a[88:91], v[104:107], v[148:151], a[88:91]\n"
      "ds_read_b64_tr_b16 v[186:187], v242 offset:17152\n"
      "v_mfma_f32_16x16x32_bf16 a[92:95], v[108:111], v[148:151], a[92:95]\n"
      "v_mfma_f32_16x16x32_bf16 a[148:151], v[148:151], v[112:115], a[148:151]\n"
      "ds_read_b64_tr_b16 v[188:189], v243 offset:16896\n"
      "v_mfma_f32_16x16x32_bf16 a[180:183], v[148:151], v[116:119], a[180:183]\n"
      "v_mfma_f32_16x16x32_bf16 a[212:215], v[148:151], v[120:123], a[212:215]\n"
      "global_load_lds_dwordx4 v233, s[80:81] offset:1024\n"
      "v_mfma_f32_16x16x32_bf16 a[244:247], v[148:151], v[124:127], a[244:247]\n"
      "ds_read_b128 v[148:151], v240 offset:10240\n"
      "ds_read_b64_tr_b16 v[190:191], v243 offset:17152\n"
      "v_mfma_f32_16x16x32_bf16 a[96:99], v[96:99], v[152:155], a[96:99]\n"
      "v_mfma_f32_16x16x32_bf16 a[100:103], v[100:103], v[152:155], a[100:103]\n"
      "v_mfma_f32_16x16x32_bf16 a[104:107], v[104:107], v[152:155], a[104:107]\n"
      "v_mfma_f32_16x16x32_bf16 a[108:111], v[108:111], v[152:155], a[108:111]\n"
      "v_mfma_f32_16x16x32_bf16 a[152:155], v[152:155], v[112:115], a[152:155]\n"
      "v_mfma_f32_16x16x32_bf16 a[184:187], v[152:155], v[116:119], a[184:187]\n"
      "global_load_lds_dwordx4 v234, s[80:81] offset:2048\n"
      "v_mfma_f32_16x16x32_bf16 a[216:219], v[152:155], v[120:123], a[216:219]\n"
      "v_mfma_f32_16x16x32_bf16 a[248:251], v[152:155], v[124:127], a[248:251]\n"
      "ds_read_b128 v[152:155], v240 offset:12288\n"
      "v_mfma_f32_16x16x32_bf16 a[112:115], v[96:99], v[156:159], a[112:115]\n"
      "v_mfma_f32_16x16x32_bf16 a[116:119], v[100:103], v[156:159], a[116:119]\n"
      "v_mfma_f32_16x16x32_bf16 a[120:123], v[104:107], v[156:159], a[120:123]\n"
      "v_mfma_f32_16x16x32_bf16 a[124:127], v[108:111], v[156:159], a[124:127]\n"
      "v_mfma_f32_16x16x32_bf16 a[156:159], v[156:159], v[112:115], a[156:159]\n"
      "global_load_lds_dwordx4 v235, s[80:81] offset:3072\n"
      "v_mfma_f32_16x16x32_bf16 a[188:191], v[156:159], v[116:119], a[188:191]\n"
      "v_mfma_f32_16x16x32_bf16 a[220:223], v[156:159], v[120:123], a[220:223]\n"
      "v_mfma_f32_16x16x32_bf16 a[252:255], v[156:159], v[124:127], a[252:255]\n"
      "ds_read_b128 v[156:159], v240 offset:14336\n"
      "s_waitcnt lgkmcnt(15)\n"
      "v_mfma_f32_16x16x32_bf16 a[0:3], v[160:163], v[128:131], a[0:3]\n"
      "ds_read_b64_tr_b16 v[96:97], v242 offset:8192\n"
      "global_load_dwordx4 v[4:7], v12, s[70:71]\n"
      "v_mfma_f32_16x16x32_bf16 a[4:7], v[164:167], v[128:131], a[4:7]\n"
      "v_lshlrev_b32 v192, 16, v212\n"
      "v_mfma_f32_16x16x32_bf16 a[8:11], v[168:171], v[128:131], a[8:11]\n"
      "s_waitcnt lgkmcnt(8)\n"
      "v_mfma_f32_16x16x32_bf16 a[12:15], v[172:175], v[128:131], a[12:15]\n"
      "ds_read_b64_tr_b16 v[98:99], v242 offset:8448\n"
      "v_and_b32 v193, 0xffff0000, v212\n"
      "v_mfma_f32_16x16x32_bf16 a[128:131], v[128:131], v[176:179], a[128:131]\n"
      "s_add_u32 s80, s56, 512\n"
      "s_addc_u32 s81, s57, 0\n"
      "s_add_u32 m0, s82, 65536\n"
      "s_nop 0\n"
      "global_load_lds_dwordx4 v224, s[80:81] offset:0\n"
      "v_mfma_f32_16x16x32_bf16 a[160:163], v[128:131], v[180:183], a[160:163]\n"
      "v_lshlrev_b32 v194, 16, v213\n"
      "s_waitcnt lgkmcnt(1)\n"
      "v_mfma_f32_16x16x32_bf16 a[192:195], v[128:131], v[184:187], a[192:195]\n"
      "ds_read_b64_tr_b16 v[100:101], v243 offset:8192\n"
      "v_mfma_f32_16x16x32_bf16 a[224:227], v[128:131], v[188:191], a[224:227]\n"
      "ds_read_b128 v[128:131], v241 offset:0\n"
      "v_and_b32 v195, 0xffff0000, v213\n"
      "v_mfma_f32_16x16x32_bf16 a[16:19], v[160:163], v[132:135], a[16:19]\n"
      "v_lshlrev_b32 v196, 16, v214\n"
      "v_mfma_f32_16x16x32_bf16 a[20:23], v[164:167], v[132:135], a[20:23]\n"
      "ds_read_b64_tr_b16 v[102:103], v243 offset:8448\n"
      "v_mfma_f32_16x16x32_bf16 a[24:27], v[168:171], v[132:135], a[24:27]\n"
      "v_and_b32 v197, 0xffff0000, v214\n"
      "v_mfma_f32_16x16x32_bf16 a[28:31], v[172:175], v[132:135], a[28:31]\n"
      "v_mfma_f32_16x16x32_bf16 a[132:135], v[132:135], v[176:179], a[132:135]\n"
      "ds_read_b64_tr_b16 v[104:105], v242 offset:8704\n"
      "global_load_lds_dwordx4 v225, s[80:81] offset:1024\n"
      "v_lshlrev_b32 v198, 16, v215\n"
      "v_mfma_f32_16x16x32_bf16 a[164:167], v[132:135], v[180:183], a[164:167]\n"
      "v_mfma_f32_16x16x32_bf16 a[196:199], v[132:135], v[184:187], a[196:199]\n"
      "v_and_b32 v199, 0xffff0000, v215\n"
      "v_mfma_f32_16x16x32_bf16 a[228:231], v[132:135], v[188:191], a[228:231]\n"
      "ds_read_b128 v[132:135], v241 offset:2048\n"
      "ds_read_b64_tr_b16 v[106:107], v242 offset:8960\n"
      "v_mfma_f32_16x16x32_bf16 a[32:35], v[160:163], v[136:139], a[32:35]\n"
      "v_lshlrev_b32 v200, 16, v216\n"
      "v_mfma_f32_16x16x32_bf16 a[36:39], v[164:167], v[136:139], a[36:39]\n"
      "v_and_b32 v201, 0xffff0000, v216\n"
      "v_mfma_f32_16x16x32_bf16 a[40:43], v[168:171], v[136:139], a[40:43]\n"
      "ds_read_b64_tr_b16 v[108:109], v243 offset:8704\n"
      "v_mfma_f32_16x16x32_bf16 a[44:47], v[172:175], v[136:139], a[44:47]\n"
      "v_lshlrev_b32 v202, 16, v217\n"
      "v_mfma_f32_16x16x32_bf16 a[136:139], v[136:139], v[176:179], a[136:139]\n"
      "global_load_lds_dwordx4 v226, s[80:81] offset:2048\n"
      "v_mfma_f32_16x16x32_bf16 a[168:171], v[136:139], v[180:183], a[168:171]\n"
      "ds_read_b64_tr_b16 v[110:111], v243 offset:8960\n"
      "v_and_b32 v203, 0xffff0000, v217\n"
      "v_mfma_f32_16x16x32_bf16 a[200:203], v[136:139], v[184:187], a[200:203]\n"
      "v_mfma_f32_16x16x32_bf16 a[232:235], v[136:139], v[188:191], a[232:235]\n"
      "ds_read_b128 v[136:139], v241 offset:4096\n"
      "v_lshlrev_b32 v204, 16, v218\n"
      "v_mfma_f32_16x16x32_bf16 a[48:51], v[160:163], v[140:143], a[48:51]\n"
      "ds_read_b64_tr_b16 v[112:113], v242 offset:24576\n"
      "v_and_b32 v205, 0xffff0000, v218\n"
      "v_mfma_f32_16x16x32_bf16 a[52:55], v[164:167], v[140:143], a[52:55]\n"
      "v_mfma_f32_16x16x32_bf16 a[56:59], v[168:171], v[140:143], a[56:59]\n"
      "v_lshlrev_b32 v206, 16, v219\n"
      "v_mfma_f32_16x16x32_bf16 a[60:63], v[172:175], v[140:143], a[60:63]\n"
      "ds_read_b64_tr_b16 v[114:115], v242 offset:24832\n"
      "v_mfma_f32_16x16x32_bf16 a[140:143], v[140:143], v[176:179], a[140:143]\n"
      "global_load_lds_dwordx4 v227, s[80:81] offset:3072\n"
      "v_and_b32 v207, 0xffff0000, v219\n"
      "v_mfma_f32_16x16x32_bf16 a[172:175], v[140:143], v[180:183], a[172:175]\n"
      "v_mfma_f32_16x16x32_bf16 a[204:207], v[140:143], v[184:187], a[204:207]\n"
      "ds_read_b64_tr_b16 v[116:117], v243 offset:24576\n"
      "v_lshlrev_b32 v208, 16, v220\n"
      "v_mfma_f32_16x16x32_bf16 a[236:239], v[140:143], v[188:191], a[236:239]\n"
      "ds_read_b128 v[140:143], v241 offset:6144\n"
      "v_mfma_f32_16x16x32_bf16 a[64:67], v[160:163], v[144:147], a[64:67]\n"
      "v_and_b32 v209, 0xffff0000, v220\n"
      "v_mfma_f32_16x16x32_bf16 a[68:71], v[164:167], v[144:147], a[68:71]\n"
      "ds_read_b64_tr_b16 v[118:119], v243 offset:24832\n"
      "v_lshlrev_b32 v210, 16, v221\n"
      "v_mfma_f32_16x16x32_bf16 a[72:75], v[168:171], v[144:147], a[72:75]\n"
      "v_mfma_f32_16x16x32_bf16 a[76:79], v[172:175], v[144:147], a[76:79]\n"
      "v_and_b32 v211, 0xffff0000, v221\n"
      "v_mfma_f32_16x16x32_bf16 a[144:147], v[144:147], v[176:179], a[144:147]\n"
      "ds_read_b64_tr_b16 v[120:121], v242 offset:25088\n"
      "s_add_u32 s80, s58, 512\n"
      "s_addc_u32 s81, s59, 0\n"
      "s_add_u32 m0, s82, 81920\n"
      "s_nop 0\n"
      "global_load_lds_dwordx4 v224, s[80:81] offset:0\n"
      "v_mfma_f32_16x16x32_bf16 a[176:179], v[144:147], v[180:183], a[176:179]\n"
      "v_mov_b32 v222, v192\n"
      "v_mfma_f32_16x16x32_bf16 a[208:211], v[144:147], v[184:187], a[208:211]\n"
      "v_mfma_f32_16x16x32_bf16 a[240:243], v[144:147], v[188:191], a[240:243]\n"
      "ds_read_b128 v[144:147], v241 offset:8192\n"
      "ds_read_b64_tr_b16 v[122:123], v242 offset:25344\n"
      "v_mov_b32 v223, v193\n"
      "v_mfma_f32_16x16x32_bf16 a[80:83], v[160:163], v[148:151], a[80:83]\n"
      "v_add_f32 v222, v222, v194\n"
      "v_mfma_f32_16x16x32_bf16 a[84:87], v[164:167], v[148:151], a[84:87]\n"
      "v_mfma_f32_16x16x32_bf16 a[88:91], v[168:171], v[148:151], a[88:91]\n"
      "ds_read_b64_tr_b16 v[124:125], v243 offset:25088\n"
      "v_add_f32 v223, v223, v195\n"
      "v_mfma_f32_16x16x32_bf16 a[92:95], v[172:175], v[148:151], a[92:95]\n"
      "v_mfma_f32_16x16x32_bf16 a[148:151], v[148:151], v[176:179], a[148:151]\n"
      "global_load_lds_dwordx4 v225, s[80:81] offset:1024\n"
      "v_add_f32 v222, v222, v196\n"
      "v_mfma_f32_16x16x32_bf16 a[180:183], v[148:151], v[180:183], a[180:183]\n"
      "ds_read_b64_tr_b16 v[126:127], v243 offset:25344\n"
      "v_mfma_f32_16x16x32_bf16 a[212:215], v[148:151], v[184:187], a[212:215]\n"
      "v_add_f32 v223, v223, v197\n"
      "v_mfma_f32_16x16x32_bf16 a[244:247], v[148:151], v[188:191], a[244:247]\n"
      "ds_read_b128 v[148:151], v241 offset:10240\n"
      "v_mfma_f32_16x16x32_bf16 a[96:99], v[160:163], v[152:155], a[96:99]\n"
      "v_add_f32 v222, v222, v198\n"
      "v_mfma_f32_16x16x32_bf16 a[100:103], v[164:167], v[152:155], a[100:103]\n"
      "v_add_f32 v223, v223, v199\n"
      "v_mfma_f32_16x16x32_bf16 a[104:107], v[168:171], v[152:155], a[104:107]\n"
      "v_mfma_f32_16x16x32_bf16 a[108:111], v[172:175], v[152:155], a[108:111]\n"
      "v_add_f32 v222, v222, v200\n"
      "v_mfma_f32_16x16x32_bf16 a[152:155], v[152:155], v[176:179], a[152:155]\n"
      "global_load_lds_dwordx4 v226, s[80:81] offset:2048\n"
      "v_mfma_f32_16x16x32_bf16 a[184:187], v[152:155], v[180:183], a[184:187]\n"
      "v_add_f32 v223, v223, v201\n"
      "v_mfma_f32_16x16x32_bf16 a[216:219], v[152:155], v[184:187], a[216:219]\n"
      "v_mfma_f32_16x16x32_bf16 a[248:251], v[152:155], v[188:191], a[248:251]\n"
      "ds_read_b128 v[152:155], v241 offset:12288\n"
      "v_add_f32 v222, v222, v202\n"
      "v_mfma_f32_16x16x32_bf16 a[112:115], v[160:163], v[156:159], a[112:115]\n"
      "v_add_f32 v223, v223, v203\n"
      "v_mfma_f32_16x16x32_bf16 a[116:119], v[164:167], v[156:159], a[116:119]\n"
      "v_mfma_f32_16x16x32_bf16 a[120:123], v[168:171], v[156:159], a[120:123]\n"
      "v_add_f32 v222, v222, v204\n"
      "v_mfma_f32_16x16x32_bf16 a[124:127], v[172:175], v[156:159], a[124:127]\n"
      "v_mfma_f32_16x16x32_bf16 a[156:159], v[156:159], v[176:179], a[156:159]\n"
      "global_load_lds_dwordx4 v227, s[80:81] offset:3072\n"
      "v_add_f32 v223, v223, v205\n"
      "v_mfma_f32_16x16x32_bf16 a[188:191], v[156:159], v[180:183], a[188:191]\n"
      "v_mfma_f32_16x16x32_bf16 a[220:223], v[156:159], v[184:187], a[220:223]\n"
      "v_add_f32 v222, v222, v206\n"
      "v_mfma_f32_16x16x32_bf16 a[252:255], v[156:159], v[188:191], a[252:255]\n"
      "ds_read_b128 v[156:159], v241 offset:14336\n"
      "s_waitcnt lgkmcnt(14)\n"
      "v_mfma_f32_16x16x32_bf16 a[0:3], v[96:99], v[128:131], a[0:3]\n"
      "v_mfma_f32_16x16x32_bf16 a[4:7], v[100:103], v[128:131], a[4:7]\n"
      "v_mfma_f32_16x16x32_bf16 a[8:11], v[104:107], v[128:131], a[8:11]\n"
      "v_mfma_f32_16x16x32_bf16 a[12:15], v[108:111], v[128:131], a[12:15]\n"
      "s_waitcnt lgkmcnt(5)\n"
      "v_mfma_f32_16x16x32_bf16 a[128:131], v[128:131], v[112:115], a[128:131]\n"
      "v_mfma_f32_16x16x32_bf16 a[160:163], v[128:131], v[116:119], a[160:163]\n"
      "v_mfma_f32_16x16x32_bf16 a[192:195], v[128:131], v[120:123], a[192:195]\n"
      "s_waitcnt lgkmcnt(0)\n"
      "v_mfma_f32_16x16x32_bf16 a[224:227], v[128:131], v[124:127], a[224:227]\n"
      "s_waitcnt vmcnt(9) lgkmcnt(0)\n"
      "s_barrier\n"
      "v_add_u32 v240, 0, v236\n"
      "v_add_u32 v241, 0, v237\n"
      "v_add_u32 v242, 32768, v238\n"
      "v_add_u32 v243, 32768, v239\n"
      "ds_read_b128 v[128:131], v240 offset:0\n"
      "v_mfma_f32_16x16x32_bf16 a[16:19], v[96:99], v[132:135], a[16:19]\n"
      "ds_read_b64_tr_b16 v[160:161], v242 offset:0\n"
      "v_add_f32 v223, v223, v207\n"
      "v_mfma_f32_16x16x32_bf16 a[20:23], v[100:103], v[132:135], a[20:23]\n"
      "v_add_f32 v222, v222, v208\n"
      "v_mfma_f32_16x16x32_bf16 a[24:27], v[104:107], v[132:135], a[24:27]\n"
      "ds_read_b64_tr_b16 v[162:163], v242 offset:256\n"
      "v_mfma_f32_16x16x32_bf16 a[28:31], v[108:111], v[132:135], a[28:31]\n"
      "s_add_u32 s80, s62, 2097152\n"
      "s_addc_u32 s81, s63, 0\n"
      "s_add_u32 m0, s82, 98304\n"
      "s_nop 0\n"
      "global_load_lds_dwordx4 v232, s[80:81] offset:0\n"
      "v_add_f32 v223, v223, v209\n"
      "v_mfma_f32_16x16x32_bf16 a[132:135], v[132:135], v[112:115], a[132:135]\n"
      "v_add_f32 v222, v222, v210\n"
      "v_mfma_f32_16x16x32_bf16 a[164:167], v[132:135], v[116:119], a[164:167]\n"
      "ds_read_b64_tr_b16 v[164:165], v243 offset:0\n"
      "v_mfma_f32_16x16x32_bf16 a[196:199], v[132:135], v[120:123], a[196:199]\n"
      "v_add_f32 v223, v223, v211\n"
      "v_mfma_f32_16x16x32_bf16 a[228:231], v[132:135], v[124:127], a[228:231]\n"
      "ds_read_b128 v[132:135], v240 offset:2048\n"
      "ds_read_b64_tr_b16 v[166:167], v243 offset:256\n"
      "v_mfma_f32_16x16x32_bf16 a[32:35], v[96:99], v[136:139], a[32:35]\n"
      "v_add_f32 v222, v222, v223\n"
      "v_mfma_f32_16x16x32_bf16 a[36:39], v[100:103], v[136:139], a[36:39]\n"
      "s_nop 1\n v_add_f32_dpp v222, v222, v222 quad_perm:[1,0,3,2] row_mask:0xf bank_mask:0xf\n"
      "v_mfma_f32_16x16x32_bf16 a[40:43], v[104:107], v[136:139], a[40:43]\n"
      "ds_read_b64_tr_b16 v[168:169], v242 offset:512\n"
      "global_load_lds_dwordx4 v233, s[80:81] offset:1024\n"
      "v_mfma_f32_16x16x32_bf16 a[44:47], v[108:111], v[136:139], a[44:47]\n"
      "s_nop 1\n v_add_f32_dpp v222, v222, v222 quad_perm:[2,3,0,1] row_mask:0xf bank_mask:0xf\n"
      "v_mfma_f32_16x16x32_bf16 a[136:139], v[136:139], v[112:115], a[136:139]\n"
      "s_nop 1\n v_add_f32_dpp v222, v222, v222 row_half_mirror row_mask:0xf bank_mask:0xf\n"
      "v_mfma_f32_16x16x32_bf16 a[168:171], v[136:139], v[116:119], a[168:171]\n"
      "ds_read_b64_tr_b16 v[170:171], v242 offset:768\n"
      "v_mfma_f32_16x16x32_bf16 a[200:203], v[136:139], v[120:123], a[200:203]\n"
      "s_nop 1\n v_add_f32_dpp v222, v222, v222 row_mirror row_mask:0xf bank_mask:0xf\n"
      "v_mfma_f32_16x16x32_bf16 a[232:235], v[136:139], v[124:127], a[232:235]\n"
      "ds_read_b128 v[136:139], v240 offset:4096\n"
      "ds_read_b64_tr_b16 v[172:173], v243 offset:512\n"
      "v_mfma_f32_16x16x32_bf16 a[48:51], v[96:99], v[140:143], a[48:51]\n"
      "v_mov_b32 v223, v222\n"
      "v_mfma_f32_16x16x32_bf16 a[52:55], v[100:103], v[140:143], a[52:55]\n"
      "global_load_lds_dwordx4 v234, s[80:81] offset:2048\n"
      "s_nop 1\n v_permlane16_swap_b32 v222, v223\n s_nop 1\n v_add_f32 v222, v222, v223\n"
      "v_mfma_f32_16x16x32_bf16 a[56:59], v[104:107], v[140:143], a[56:59]\n"
      "ds_read_b64_tr_b16 v[174:175], v243 offset:768\n"
      "v_mfma_f32_16x16x32_bf16 a[60:63], v[108:111], v[140:143], a[60:63]\n"
      "v_mul_f32 v0, 0x3acccccd, v222\n"
      "v_mfma_f32_16x16x32_bf16 a[140:143], v[140:143], v[112:115], a[140:143]\n"
      "v_sub_f32 v192, v192, v0\n"
      "v_mfma_f32_16x16x32_bf16 a[172:175], v[140:143], v[116:119], a[172:175]\n"
      "ds_read_b64_tr_b16 v[176:177], v242 offset:16384\n"
      "v_mfma_f32_16x16x32_bf16 a[204:207], v[140:143], v[120:123], a[204:207]\n"
      "v_sub_f32 v193, v193, v0\n"
      "v_mfma_f32_16x16x32_bf16 a[236:239], v[140:143], v[124:127], a[236:239]\n"
      "ds_read_b128 v[140:143], v240 offset:6144\n"
      "ds_read_b64_tr_b16 v[178:179], v242 offset:16640\n"
      "v_mfma_f32_16x16x32_bf16 a[64:67], v[96:99], v[144:147], a[64:67]\n"
      "global_load_lds_dwordx4 v235, s[80:81] offset:3072\n"
      "v_sub_f32 v194, v194, v0\n"
      "v_mfma_f32_16x16x32_bf16 a[68:71], v[100:103], v[144:147], a[68:71]\n"
      "v_sub_f32 v195, v195, v0\n"
      "v_mfma_f32_16x16x32_bf16 a[72:75], v[104:107], v[144:147], a[72:75]\n"
      "ds_read_b64_tr_b16 v[180:181], v243 offset:16384\n"
      "v_mfma_f32_16x16x32_bf16 a[76:79], v[108:111], v[144:147], a[76:79]\n"
      "v_sub_f32 v196, v196, v0\n"
      "v_mfma_f32_16x16x32_bf16 a[144:147], v[144:147], v[112:115], a[144:147]\n"
      "ds_read_b64_tr_b16 v[182:183], v243 offset:16640\n"
      "v_sub_f32 v197, v197, v0\n"
      "v_mfma_f32_16x16x32_bf16 a[176:179], v[144:147], v[116:119], a[176:179]\n"
      "v_mfma_f32_16x16x32_bf16 a[208:211], v[144:147], v[120:123], a[208:211]\n"
      "v_sub_f32 v198, v198, v0\n"
      "v_mfma_f32_16x16x32_bf16 a[240:243], v[144:147], v[124:127], a[240:243]\n"
      "ds_read_b128 v[144:147], v240 offset:8192\n"
      "ds_read_b64_tr_b16 v[184:185], v242 offset:16896\n"
      "s_add_u32 s80, s64, 2097152\n"
      "s_addc_u32 s81, s65, 0\n"
      "s_add_u32 m0, s82, 114688\n"
      "s_nop 0\n"
      "global_load_lds_dwordx4 v232, s[80:81] offset:0\n"
      "v_mfma_f32_16x16x32_bf16 a[80:83], v[96:99], v[148:151], a[80:83]\n"
      "v_sub_f32 v199, v199, v0\n"
      "v_mfma_f32_16x16x32_bf16 a[84:87], v[100:103], v[148:151], a[84:87]\n"
      "v_sub_f32 v200, v200, v0\n"
      "v_mfma_f32_16x16x32_bf16 a[88:91], v[104:107], v[148:151], a[88:91]\n"
      "ds_read_b64_tr_b16 v[186:187], v242 offset:17152\n"
      "v_mfma_f32_16x16x32_bf16 a[92:95], v[108:111], v[148:151], a[92:95]\n"
      "v_sub_f32 v201, v201, v0\n"
      "v_mfma_f32_16x16x32_bf16 a[148:151], v[148:151], v[112:115], a[148:151]\n"
      "ds_read_b64_tr_b16 v[188:189], v243 offset:16896\n"
      "v_sub_f32 v202, v202, v0\n"
      "v_mfma_f32_16x16x32_bf16 a[180:183], v[148:151], v[116:119], a[180:183]\n"
      "v_mfma_f32_16x16x32_bf16 a[212:215], v[148:151], v[120:123], a[212:215]\n"
      "global_load_lds_dwordx4 v233, s[80:81] offset:1024\n"
      "v_sub_f32 v203, v203, v0\n"
      "v_mfma_f32_16x16x32_bf16 a[244:247], v[148:151], v[124:127], a[244:247]\n"
      "ds_read_b128 v[148:151], v240 offset:10240\n"
      "ds_read_b64_tr_b16 v[190:191], v243 offset:17152\n"
      "v_mfma_f32_16x16x32_bf16 a[96:99], v[96:99], v[152:155], a[96:99]\n"
      "v_sub_f32 v204, v204, v0\n"
      "v_mfma_f32_16x16x32_bf16 a[100:103], v[100:103], v[152:155], a[100:103]\n"
      "v_sub_f32 v205, v205, v0\n"
      "v_mfma_f32_16x16x32_bf16 a[104:107], v[104:107], v[152:155], a[104:107]\n"
      "v_mfma_f32_16x16x32_bf16 a[108:111], v[108:111], v[152:155], a[108:111]\n"
      "v_sub_f32 v206, v206, v0\n"
      "v_mfma_f32_16x16x32_bf16 a[152:155], v[152:155], v[112:115], a[152:155]\n"
      "v_sub_f32 v207, v207, v0\n"
      "v_mfma_f32_16x16x32_bf16 a[184:187], v[152:155], v[116:119], a[184:187]\n"
      "global_load_lds_dwordx4 v234, s[80:81] offset:2048\n"
      "v_mfma_f32_16x16x32_bf16 a[216:219], v[152:155], v[120:123], a[216:219]\n"
      "v_sub_f32 v208, v208, v0\n"
      "v_mfma_f32_16x16x32_bf16 a[248:251], v[152:155], v[124:127], a[248:251]\n"
      "ds_read_b128 v[152:155], v240 offset:12288\n"
      "v_mfma_f32_16x16x32_bf16 a[112:115], v[96:99], v[156:159], a[112:115]\n"
      "v_sub_f32 v209, v209, v0\n"
      "v_mfma_f32_16x16x32_bf16 a[116:119], v[100:103], v[156:159], a[116:119]\n"
      "v_sub_f32 v210, v210, v0\n"
      "v_mfma_f32_16x16x32_bf16 a[120:123], v[104:107], v[156:159], a[120:123]\n"
      "v_mfma_f32_16x16x32_bf16 a[124:127], v[108:111], v[156:159], a[124:127]\n"
      "v_sub_f32 v211, v211, v0\n"
      "v_mfma_f32_16x16x32_bf16 a[156:159], v[156:159], v[112:115], a[156:159]\n"
      "global_load_lds_dwordx4 v235, s[80:81] offset:3072\n"
      "v_mul_f32 v222, v192, v192\n"
      "v_mfma_f32_16x16x32_bf16 a[188:191], v[156:159], v[116:119], a[188:191]\n"
      "v_mfma_f32_16x16x32_bf16 a[220:223], v[156:159], v[120:123], a[220:223]\n"
      "v_mul_f32 v223, v193, v193\n"
      "v_mfma_f32_16x16x32_bf16 a[252:255], v[156:159], v[124:127], a[252:255]\n"
      "ds_read_b128 v[156:159], v240 offset:14336\n"
      "s_waitcnt lgkmcnt(15)\n"
      "v_mfma_f32_16x16x32_bf16 a[0:3], v[160:163], v[128:131], a[0:3]\n"
      "ds_read_b64_tr_b16 v[96:97], v242 offset:8192\n"
      "v_fmac_f32 v222, v194, v194\n"
      "v_mfma_f32_16x16x32_bf16 a[4:7], v[164:167], v[128:131], a[4:7]\n"
      "v_fmac_f32 v223, v195, v195\n"
      "v_mfma_f32_16x16x32_bf16 a[8:11], v[168:171], v[128:131], a[8:11]\n"
      "s_waitcnt lgkmcnt(8)\n"
      "v_mfma_f32_16x16x32_bf16 a[12:15], v[172:175], v[128:131], a[12:15]\n"
      "ds_read_b64_tr_b16 v[98:99], v242 offset:8448\n"
      "v_fmac_f32 v222, v196, v196\n"
      "v_mfma_f32_16x16x32_bf16 a[128:131], v[128:131], v[176:179], a[128:131]\n"
      "s_add_u32 s80, s56, 640\n"
      "s_addc_u32 s81, s57, 0\n"
      "s_add_u32 m0, s82, 131072\n"
      "s_nop 0\n"
      "global_load_lds_dwordx4 v224, s[80:81] offset:0\n"
      "v_mfma_f32_16x16x32_bf16 a[160:163], v[128:131], v[180:183], a[160:163]\n"
      "v_fmac_f32 v223, v197, v197\n"
      "s_waitcnt lgkmcnt(1)\n"
      "v_mfma_f32_16x16x32_bf16 a[192:195], v[128:131], v[184:187], a[192:195]\n"
      "ds_read_b64_tr_b16 v[100:101], v243 offset:8192\n"
      "v_mfma_f32_16x16x32_bf16 a[224:227], v[128:131], v[188:191], a[224:227]\n"
      "ds_read_b128 v[128:131], v241 offset:0\n"
      "v_fmac_f32 v222, v198, v198\n"
      "v_mfma_f32_16x16x32_bf16 a[16:19], v[160:163], v[132:135], a[16:19]\n"
      "v_mfma_f32_16x16x32_bf16 a[20:23], v[164:167], v[132:135], a[20:23]\n"
      "ds_read_b64_tr_b16 v[102:103], v243 offset:8448\n"
      "v_fmac_f32 v223, v199, v199\n"
      "v_mfma_f32_16x16x32_bf16 a[24:27], v[168:171], v[132:135], a[24:27]\n"
      "v_mfma_f32_16x16x32_bf16 a[28:31], v[172:175], v[132:135], a[28:31]\n"
      "v_fmac_f32 v222, v200, v200\n"
      "v_mfma_f32_16x16x32_bf16 a[132:135], v[132:135], v[176:179], a[132:135]\n"
      "ds_read_b64_tr_b16 v[104:105], v242 offset:8704\n"
      "global_load_lds_dwordx4 v225, s[80:81] offset:1024\n"
      "v_mfma_f32_16x16x32_bf16 a[164:167], v[132:135], v[180:183], a[164:167]\n"
      "v_fmac_f32 v223, v201, v201\n"
      "v_mfma_f32_16x16x32_bf16 a[196:199], v[132:135], v[184:187], a[196:199]\n"
      "v_mfma_f32_16x16x32_bf16 a[228:231], v[132:135], v[188:191], a[228:231]\n"
      "ds_read_b128 v[132:135], v241 offset:2048\n"
      "ds_read_b64_tr_b16 v[106:107], v242 offset:8960\n"
      "v_fmac_f32 v222, v202, v202\n"
      "v_mfma_f32_16x16x32_bf16 a[32:35], v[160:163], v[136:139], a[32:35]\n"
      "v_fmac_f32 v223, v203, v203\n"
      "v_mfma_f32_16x16x32_bf16 a[36:39], v[164:167], v[136:139], a[36:39]\n"
      "v_mfma_f32_16x16x32_bf16 a[40:43], v[168:171], v[136:139], a[40:43]\n"
      "ds_read_b64_tr_b16 v[108:109], v243 offset:8704\n"
      "v_fmac_f32 v222, v204, v204\n"
      "v_mfma_f32_16x16x32_bf16 a[44:47], v[172:175], v[136:139], a[44:47]\n"
      "v_mfma_f32_16x16x32_bf16 a[136:139], v[136:139], v[176:179], a[136:139]\n"
      "global_load_lds_dwordx4 v226, s[80:81] offset:2048\n"
      "v_fmac_f32 v223, v205, v205\n"
      "v_mfma_f32_16x16x32_bf16 a[168:171], v[136:139], v[180:183], a[168:171]\n"
      "ds_read_b64_tr_b16 v[110:111], v243 offset:8960\n"
      "v_mfma_f32_16x16x32_bf16 a[200:203], v[136:139], v[184:187], a[200:203]\n"
      "v_fmac_f32 v222, v206, v206\n"
      "v_mfma_f32_16x16x32_bf16 a[232:235], v[136:139], v[188:191], a[232:235]\n"
      "ds_read_b128 v[136:139], v241 offset:4096\n"
      "v_mfma_f32_16x16x32_bf16 a[48:51], v[160:163], v[140:143], a[48:51]\n"
      "ds_read_b64_tr_b16 v[112:113], v242 offset:24576\n"
      "v_fmac_f32 v223, v207, v207\n"
      "v_mfma_f32_16x16x32_bf16 a[52:55], v[164:167], v[140:143], a[52:55]\n"
      "v_mfma_f32_16x16x32_bf16 a[56:59], v[168:171], v[140:143], a[56:59]\n"
      "v_fmac_f32 v222, v208, v208\n"
      "v_mfma_f32_16x16x32_bf16 a[60:63], v[172:175], v[140:143], a[60:63]\n"
      "ds_read_b64_tr_b16 v[114:115], v242 offset:24832\n"
      "v_mfma_f32_16x16x32_bf16 a[140:143], v[140:143], v[176:179], a[140:143]\n"
      "global_load_lds_dwordx4 v227, s[80:81] offset:3072\n"
      "v_fmac_f32 v223, v209, v209\n"
      "v_mfma_f32_16x16x32_bf16 a[172:175], v[140:143], v[180:183], a[172:175]\n"
      "v_mfma_f32_16x16x32_bf16 a[204:207], v[140:143], v[184:187], a[204:207]\n"
      "ds_read_b64_tr_b16 v[116:117], v243 offset:24576\n"
      "v_fmac_f32 v222, v210, v210\n"
      "v_mfma_f32_16x16x32_bf16 a[236:239], v[140:143], v[188:191], a[236:239]\n"
      "ds_read_b128 v[140:143], v241 offset:6144\n"
      "v_mfma_f32_16x16x32_bf16 a[64:67], v[160:163], v[144:147], a[64:67]\n"
      "v_fmac_f32 v223, v211, v211\n"
      "v_mfma_f32_16x16x32_bf16 a[68:71], v[164:167], v[144:147], a[68:71]\n"
      "ds_read_b64_tr_b16 v[118:119], v243 offset:24832\n"
      "v_add_f32 v222, v222, v223\n"
      "v_mfma_f32_16x16x32_bf16 a[72:75], v[168:171], v[144:147], a[72:75]\n"
      "v_mfma_f32_16x16x32_bf16 a[76:79], v[172:175], v[144:147], a[76:79]\n"
      "s_nop 1\n v_add_f32_dpp v222, v222, v222 quad_perm:[1,0,3,2] row_mask:0xf bank_mask:0xf\n"
      "v_mfma_f32_16x16x32_bf16 a[144:147], v[144:147], v[176:179], a[144:147]\n"
      "ds_read_b64_tr_b16 v[120:121], v242 offset:25088\n"
      "s_add_u32 s80, s58, 640\n"
      "s_addc_u32 s81, s59, 0\n"
      "s_add_u32 m0, s82, 147456\n"
      "s_nop 0\n"
      "global_load_lds_dwordx4 v224, s[80:81] offset:0\n"
      "v_mfma_f32_16x16x32_bf16 a[176:179], v[144:147], v[180:183], a[176:179]\n"
      "s_nop 1\n v_add_f32_dpp v222, v222, v222 quad_perm:[2,3,0,1] row_mask:0xf bank_mask:0xf\n"
      "v_mfma_f32_16x16x32_bf16 a[208:211], v[144:147], v[184:187], a[208:211]\n"
      "v_mfma_f32_16x16x32_bf16 a[240:243], v[144:147], v[188:191], a[240:243]\n"
      "ds_read_b128 v[144:147], v241 offset:8192\n"
      "ds_read_b64_tr_b16 v[122:123], v242 offset:25344\n"
      "s_nop 1\n v_add_f32_dpp v222, v222, v222 row_half_mirror row_mask:0xf bank_mask:0xf\n"
      "v_mfma_f32_16x16x32_bf16 a[80:83], v[160:163], v[148:151], a[80:83]\n"
      "v_mfma_f32_16x16x32_bf16 a[84:87], v[164:167], v[148:151], a[84:87]\n"
      "s_nop 1\n v_add_f32_dpp v222, v222, v222 row_mirror row_mask:0xf bank_mask:0xf\n"
      "v_mfma_f32_16x16x32_bf16 a[88:91], v[168:171], v[148:151], a[88:91]\n"
      "ds_read_b64_tr_b16 v[124:125], v243 offset:25088\n"
      "v_mfma_f32_16x16x32_bf16 a[92:95], v[172:175], v[148:151], a[92:95]\n"
      "v_mov_b32 v223, v222\n"
      "v_mfma_f32_16x16x32_bf16 a[148:151], v[148:151], v[176:179], a[148:151]\n"
      "global_load_lds_dwordx4 v225, s[80:81] offset:1024\n"
      "v_mfma_f32_16x16x32_bf16 a[180:183], v[148:151], v[180:183], a[180:183]\n"
      "ds_read_b64_tr_b16 v[126:127], v243 offset:25344\n"
      "s_nop 1\n v_permlane16_swap_b32 v222, v223\n s_nop 1\n v_add_f32 v222, v222, v223\n"
      "v_mfma_f32_16x16x32_bf16 a[212:215], v[148:151], v[184:187], a[212:215]\n"
      "v_mfma_f32_16x16x32_bf16 a[244:247], v[148:151], v[188:191], a[244:247]\n"
      "ds_read_b128 v[148:151], v241 offset:10240\n"
      "v_mul_f32 v222, 0x3acccccd, v222\n"
      "v_mfma_f32_16x16x32_bf16 a[96:99], v[160:163], v[152:155], a[96:99]\n"
      "v_add_f32 v222, 0x3727c5ac, v222\n"
      "v_mfma_f32_16x16x32_bf16 a[100:103], v[164:167], v[152:155], a[100:103]\n"
      "v_mfma_f32_16x16x32_bf16 a[104:107], v[168:171], v[152:155], a[104:107]\n"
      "v_rsq_f32 v222, v222\n s_nop 0\n"
      "v_mfma_f32_16x16x32_bf16 a[108:111], v[172:175], v[152:155], a[108:111]\n"
      "v_mfma_f32_16x16x32_bf16 a[152:155], v[152:155], v[176:179], a[152:155]\n"
      "global_load_lds_dwordx4 v226, s[80:81] offset:2048\n"
      "v_mul_f32 v223, 0x3e0293ee, v222\n"
      "v_mfma_f32_16x16x32_bf16 a[184:187], v[152:155], v[180:183], a[184:187]\n"
      "v_mfma_f32_16x16x32_bf16 a[216:219], v[152:155], v[184:187], a[216:219]\n"
      "v_max_f32 v223, 0x00800000, v223\n"
      "v_mfma_f32_16x16x32_bf16 a[248:251], v[152:155], v[188:191], a[248:251]\n"
      "ds_read_b128 v[152:155], v241 offset:12288\n"
      "v_mfma_f32_16x16x32_bf16 a[112:115], v[160:163], v[156:159], a[112:115]\n"
      "v_mul_f32 v222, v0, v223\n"
      "v_mfma_f32_16x16x32_bf16 a[116:119], v[164:167], v[156:159], a[116:119]\n"
      "v_mfma_f32_16x16x32_bf16 a[120:123], v[168:171], v[156:159], a[120:123]\n"
      "v_sub_f32 v222, 0, v222\n"
      "v_mfma_f32_16x16x32_bf16 a[124:127], v[172:175], v[156:159], a[124:127]\n"
      "v_mfma_f32_16x16x32_bf16 a[156:159], v[156:159], v[176:179], a[156:159]\n"
      "global_load_lds_dwordx4 v227, s[80:81] offset:3072\n"
      "v_lshrrev_b32 v0, 2, v248\n"
      "v_mfma_f32_16x16x32_bf16 a[188:191], v[156:159], v[180:183], a[188:191]\n"
      "v_mfma_f32_16x16x32_bf16 a[220:223], v[156:159], v[184:187], a[220:223]\n"
      "s_mov_b32 exec_lo, 1\n s_mov_b32 exec_hi, 1\n global_store_dwordx2 v0, v[222:223], %22 sc1\n s_mov_b64 exec, -1\n"
      "v_mfma_f32_16x16x32_bf16 a[252:255], v[156:159], v[188:191], a[252:255]\n"
      "ds_read_b128 v[156:159], v241 offset:14336\n"
      "s_waitcnt lgkmcnt(14)\n"
      "v_mfma_f32_16x16x32_bf16 a[0:3], v[96:99], v[128:131], a[0:3]\n"
      "v_mfma_f32_16x16x32_bf16 a[4:7], v[100:103], v[128:131], a[4:7]\n"
      "v_mfma_f32_16x16x32_bf16 a[8:11], v[104:107], v[128:131], a[8:11]\n"
      "v_mfma_f32_16x16x32_bf16 a[12:15], v[108:111], v[128:131], a[12:15]\n"
      "s_waitcnt lgkmcnt(5)\n"
      "v_mfma_f32_16x16x32_bf16 a[128:131], v[128:131], v[112:115], a[128:131]\n"
      "v_mfma_f32_16x16x32_bf16 a[160:163], v[128:131], v[116:119], a[160:163]\n"
      "v_mfma_f32_16x16x32_bf16 a[192:195], v[128:131], v[120:123], a[192:195]\n"
      "s_waitcnt lgkmcnt(0)\n"
      "v_mfma_f32_16x16x32_bf16 a[224:227], v[128:131], v[124:127], a[224:227]\n"
      "s_waitcnt vmcnt(9) lgkmcnt(0)\n"
      "s_barrier\n"
      "v_add_u32 v240, 65536, v236\n"
      "v_add_u32 v241, 65536, v237\n"
      "v_add_u32 v242, 98304, v238\n"
      "v_add_u32 v243, 98304, v239\n"
      "ds_read_b128 v[128:131], v240 offset:0\n"
      "v_mfma_f32_16x16x32_bf16 a[16:19], v[96:99], v[132:135], a[16:19]\n"
      "ds_read_b64_tr_b16 v[160:161], v242 offset:0\n"
      "global_load_dword v255, v245, %15 sc1\n"
      "v_mfma_f32_16x16x32_bf16 a[20:23], v[100:103], v[132:135], a[20:23]\n"
      "s_add_u32 s80, s62, 2621440\n"
      "s_addc_u32 s81, s63, 0\n"
      "s_add_u32 m0, s82, 0\n"
      "s_nop 0\n"
      "global_load_lds_dwordx4 v232, s[80:81] offset:0\n"
      "v_mfma_f32_16x16x32_bf16 a[24:27], v[104:107], v[132:135], a[24:27]\n"
      "ds_read_b64_tr_b16 v[162:163], v242 offset:256\n"
      "v_mfma_f32_16x16x32_bf16 a[28:31], v[108:111], v[132:135], a[28:31]\n"
      "v_mfma_f32_16x16x32_bf16 a[132:135], v[132:135], v[112:115], a[132:135]\n"
      "v_mfma_f32_16x16x32_bf16 a[164:167], v[132:135], v[116:119], a[164:167]\n"
      "ds_read_b64_tr_b16 v[164:165], v243 offset:0\n"
      "global_load_lds_dwordx4 v233, s[80:81] offset:1024\n"
      "v_mfma_f32_16x16x32_bf16 a[196:199], v[132:135], v[120:123], a[196:199]\n"
      "v_mfma_f32_16x16x32_bf16 a[228:231], v[132:135], v[124:127], a[228:231]\n"
      "ds_read_b128 v[132:135], v240 offset:2048\n"
      "ds_read_b64_tr_b16 v[166:167], v243 offset:256\n"
      "v_mfma_f32_16x16x32_bf16 a[32:35], v[96:99], v[136:139], a[32:35]\n"
      "global_load_lds_dwordx4 v234, s[80:81] offset:2048\n"
      "v_mfma_f32_16x16x32_bf16 a[36:39], v[100:103], v[136:139], a[36:39]\n"
      "v_mfma_f32_16x16x32_bf16 a[40:43], v[104:107], v[136:139], a[40:43]\n"
      "ds_read_b64_tr_b16 v[168:169], v242 offset:512\n"
      "v_mfma_f32_16x16x32_bf16 a[44:47], v[108:111], v[136:139], a[44:47]\n"
      "v_mfma_f32_16x16x32_bf16 a[136:139], v[136:139], v[112:115], a[136:139]\n"
      "global_load_lds_dwordx4 v235, s[80:81] offset:3072\n"
      "v_mfma_f32_16x16x32_bf16 a[168:171], v[136:139], v[116:119], a[168:171]\n"
      "ds_read_b64_tr_b16 v[170:171], v242 offset:768\n"
      "v_mfma_f32_16x16x32_bf16 a[200:203], v[136:139], v[120:123], a[200:203]\n"
      "v_mfma_f32_16x16x32_bf16 a[232:235], v[136:139], v[124:127], a[232:235]\n"
      "ds_read_b128 v[136:139], v240 offset:4096\n"
      "ds_read_b64_tr_b16 v[172:173], v243 offset:512\n"
      "s_add_u32 s80, s64, 2621440\n"
      "s_addc_u32 s81, s65, 0\n"
      "s_add_u32 m0, s82, 16384\n"
      "s_nop 0\n"
      "global_load_lds_dwordx4 v232, s[80:81] offset:0\n"
      "v_mfma_f32_16x16x32_bf16 a[48:51], v[96:99], v[140:143], a[48:51]\n"
      "v_mfma_f32_16x16x32_bf16 a[52:55], v[100:103], v[140:143], a[52:55]\n"
      "v_mfma_f32_16x16x32_bf16 a[56:59], v[104:107], v[140:143], a[56:59]\n"
      "ds_read_b64_tr_b16 v[174:175], v243 offset:768\n"
      "v_mfma_f32_16x16x32_bf16 a[60:63], v[108:111], v[140:143], a[60:63]\n"
      "global_load_lds_dwordx4 v233, s[80:81] offset:1024\n"
      "v_mfma_f32_16x16x32_bf16 a[140:143], v[140:143], v[112:115], a[140:143]\n"
      "v_mfma_f32_16x16x32_bf16 a[172:175], v[140:143], v[116:119], a[172:175]\n"
      "ds_read_b64_tr_b16 v[176:177], v242 offset:16384\n"
      "v_mfma_f32_16x16x32_bf16 a[204:207], v[140:143], v[120:123], a[204:207]\n"
      "global_load_lds_dwordx4 v234, s[80:81] offset:2048\n"
      "v_mfma_f32_16x16x32_bf16 a[236:239], v[140:143], v[124:127], a[236:239]\n"
      "ds_read_b128 v[140:143], v240 offset:6144\n"
      "ds_read_b64_tr_b16 v[178:179], v242 offset:16640\n"
      "v_mfma_f32_16x16x32_bf16 a[64:67], v[96:99], v[144:147], a[64:67]\n"
      "v_mfma_f32_16x16x32_bf16 a[68:71], v[100:103], v[144:147], a[68:71]\n"
      "v_mfma_f32_16x16x32_bf16 a[72:75], v[104:107], v[144:147], a[72:75]\n"
      "ds_read_b64_tr_b16 v[180:181], v243 offset:16384\n"
      "global_load_lds_dwordx4 v235, s[80:81] offset:3072\n"
      "v_mfma_f32_16x16x32_bf16 a[76:79], v[108:111], v[144:147], a[76:79]\n"
      "v_mfma_f32_16x16x32_bf16 a[144:147], v[144:147], v[112:115], a[144:147]\n"
      "ds_read_b64_tr_b16 v[182:183], v243 offset:16640\n"
      "v_mfma_f32_16x16x32_bf16 a[176:179], v[144:147], v[116:119], a[176:179]\n"
      "s_add_u32 s80, s56, 768\n"
      "s_addc_u32 s81, s57, 0\n"
      "s_add_u32 m0, s82, 32768\n"
      "s_nop 0\n"
      "global_load_lds_dwordx4 v224, s[80:81] offset:0\n"
      "v_mfma_f32_16x16x32_bf16 a[208:211], v[144:147], v[120:123], a[208:211]\n"
      "v_mfma_f32_16x16x32_bf16 a[240:243], v[144:147], v[124:127], a[240:243]\n"
      "ds_read_b128 v[144:147], v240 offset:8192\n"
      "ds_read_b64_tr_b16 v[184:185], v242 offset:16896\n"
      "v_mfma_f32_16x16x32_bf16 a[80:83], v[96:99], v[148:151], a[80:83]\n"
      "v_mfma_f32_16x16x32_bf16 a[84:87], v[100:103], v[148:151], a[84:87]\n"
      "global_load_lds_dwordx4 v225, s[80:81] offset:1024\n"
      "v_mfma_f32_16x16x32_bf16 a[88:91], v[104:107], v[148:151], a[88:91]\n"
      "ds_read_b64_tr_b16 v[186:187], v242 offset:17152\n"
      "v_mfma_f32_16x16x32_bf16 a[92:95], v[108:111], v[148:151], a[92:95]\n"
      "v_mfma_f32_16x16x32_bf16 a[148:151], v[148:151], v[112:115], a[148:151]\n"
      "ds_read_b64_tr_b16 v[188:189], v243 offset:16896\n"
      "global_load_lds_dwordx4 v226, s[80:81] offset:2048\n"
      "v_mfma_f32_16x16x32_bf16 a[180:183], v[148:151], v[116:119], a[180:183]\n"
      "v_mfma_f32_16x16x32_bf16 a[212:215], v[148:151], v[120:123], a[212:215]\n"
      "v_mfma_f32_16x16x32_bf16 a[244:247], v[148:151], v[124:127], a[244:247]\n"
      "ds_read_b128 v[148:151], v240 offset:10240\n"
      "ds_read_b64_tr_b16 v[190:191], v243 offset:17152\n"
      "v_mfma_f32_16x16x32_bf16 a[96:99], v[96:99], v[152:155], a[96:99]\n"
      "global_load_lds_dwordx4 v227, s[80:81] offset:3072\n"
      "v_mfma_f32_16x16x32_bf16 a[100:103], v[100:103], v[152:155], a[100:103]\n"
      "v_mfma_f32_16x16x32_bf16 a[104:107], v[104:107], v[152:155], a[104:107]\n"
      "v_mfma_f32_16x16x32_bf16 a[108:111], v[108:111], v[152:155], a[108:111]\n"
      "s_add_u32 s80, s58, 768\n"
      "s_addc_u32 s81, s59, 0\n"
      "s_add_u32 m0, s82, 49152\n"
      "s_nop 0\n"
      "global_load_lds_dwordx4 v224, s[80:81] offset:0\n"
      "v_mfma_f32_16x16x32_bf16 a[152:155], v[152:155], v[112:115], a[152:155]\n"
      "v_mfma_f32_16x16x32_bf16 a[184:187], v[152:155], v[116:119], a[184:187]\n"
      "v_mfma_f32_16x16x32_bf16 a[216:219], v[152:155], v[120:123], a[216:219]\n"
      "v_mfma_f32_16x16x32_bf16 a[248:251], v[152:155], v[124:127], a[248:251]\n"
      "ds_read_b128 v[152:155], v240 offset:12288\n"
      "global_load_lds_dwordx4 v225, s[80:81] offset:1024\n"
      "v_mfma_f32_16x16x32_bf16 a[112:115], v[96:99], v[156:159], a[112:115]\n"
      "v_mfma_f32_16x16x32_bf16 a[116:119], v[100:103], v[156:159], a[116:119]\n"
      "v_mfma_f32_16x16x32_bf16 a[120:123], v[104:107], v[156:159], a[120:123]\n"
      "global_load_lds_dwordx4 v226, s[80:81] offset:2048\n"
      "v_mfma_f32_16x16x32_bf16 a[124:127], v[108:111], v[156:159], a[124:127]\n"
      "v_mfma_f32_16x16x32_bf16 a[156:159], v[156:159], v[112:115], a[156:159]\n"
      "v_mfma_f32_16x16x32_bf16 a[188:191], v[156:159], v[116:119], a[188:191]\n"
      "v_mfma_f32_16x16x32_bf16 a[220:223], v[156:159], v[120:123], a[220:223]\n"
      "global_load_lds_dwordx4 v227, s[80:81] offset:3072\n"
      "v_mfma_f32_16x16x32_bf16 a[252:255], v[156:159], v[124:127], a[252:255]\n"
      "ds_read_b128 v[156:159], v240 offset:14336\n"
      "s_waitcnt lgkmcnt(15)\n"
      "v_mfma_f32_16x16x32_bf16 a[0:3], v[160:163], v[128:131], a[0:3]\n"
      "ds_read_b64_tr_b16 v[96:97], v242 offset:8192\n"
      "v_mfma_f32_16x16x32_bf16 a[4:7], v[164:167], v[128:131], a[4:7]\n"
      "v_mfma_f32_16x16x32_bf16 a[8:11], v[168:171], v[128:131], a[8:11]\n"
      "s_waitcnt lgkmcnt(8)\n"
      "v_mfma_f32_16x16x32_bf16 a[12:15], v[172:175], v[128:131], a[12:15]\n"
      "ds_read_b64_tr_b16 v[98:99], v242 offset:8448\n"
      "v_mfma_f32_16x16x32_bf16 a[128:131], v[128:131], v[176:179], a[128:131]\n"
      "v_mfma_f32_16x16x32_bf16 a[160:163], v[128:131], v[180:183], a[160:163]\n"
      "s_waitcnt lgkmcnt(1)\n"
      "v_mfma_f32_16x16x32_bf16 a[192:195], v[128:131], v[184:187], a[192:195]\n"
      "ds_read_b64_tr_b16 v[100:101], v243 offset:8192\n"
      "v_mfma_f32_16x16x32_bf16 a[224:227], v[128:131], v[188:191], a[224:227]\n"
      "ds_read_b128 v[128:131], v241 offset:0\n"
      "v_mfma_f32_16x16x32_bf16 a[16:19], v[160:163], v[132:135], a[16:19]\n"
      "v_mfma_f32_16x16x32_bf16 a[20:23], v[164:167], v[132:135], a[20:23]\n"
      "ds_read_b64_tr_b16 v[102:103], v243 offset:8448\n"
      "v_mfma_f32_16x16x32_bf16 a[24:27], v[168:171], v[132:135], a[24:27]\n"
      "v_mfma_f32_16x16x32_bf16 a[28:31], v[172:175], v[132:135], a[28:31]\n"
      "v_mfma_f32_16x16x32_bf16 a[132:135], v[132:135], v[176:179], a[132:135]\n"
      "ds_read_b64_tr_b16 v[104:105], v242 offset:8704\n"
      "v_mfma_f32_16x16x32_bf16 a[164:167], v[132:135], v[180:183], a[164:167]\n"
      "v_mfma_f32_16x16x32_bf16 a[196:199], v[132:135], v[184:187], a[196:199]\n"
      "v_mfma_f32_16x16x32_bf16 a[228:231], v[132:135], v[188:191], a[228:231]\n"
      "ds_read_b128 v[132:135], v241 offset:2048\n"
      "ds_read_b64_tr_b16 v[106:107], v242 offset:8960\n"
      "v_mfma_f32_16x16x32_bf16 a[32:35], v[160:163], v[136:139], a[32:35]\n"
      "v_mfma_f32_16x16x32_bf16 a[36:39], v[164:167], v[136:139], a[36:39]\n"
      "v_mfma_f32_16x16x32_bf16 a[40:43], v[168:171], v[136:139], a[40:43]\n"
      "ds_read_b64_tr_b16 v[108:109], v243 offset:8704\n"
      "v_mfma_f32_16x16x32_bf16 a[44:47], v[172:175], v[136:139], a[44:47]\n"
      "v_mfma_f32_16x16x32_bf16 a[136:139], v[136:139], v[176:179], a[136:139]\n"
      "v_mfma_f32_16x16x32_bf16 a[168:171], v[136:139], v[180:183], a[168:171]\n"
      "ds_read_b64_tr_b16 v[110:111], v243 offset:8960\n"
      "v_mfma_f32_16x16x32_bf16 a[200:203], v[136:139], v[184:187], a[200:203]\n"
      "v_mfma_f32_16x16x32_bf16 a[232:235], v[136:139], v[188:191], a[232:235]\n"
      "ds_read_b128 v[136:139], v241 offset:4096\n"
      "v_mfma_f32_16x16x32_bf16 a[48:51], v[160:163], v[140:143], a[48:51]\n"
      "ds_read_b64_tr_b16 v[112:113], v242 offset:24576\n"
      "v_mfma_f32_16x16x32_bf16 a[52:55], v[164:167], v[140:143], a[52:55]\n"
      "v_mfma_f32_16x16x32_bf16 a[56:59], v[168:171], v[140:143], a[56:59]\n"
      "v_mfma_f32_16x16x32_bf16 a[60:63], v[172:175], v[140:143], a[60:63]\n"
      "ds_read_b64_tr_b16 v[114:115], v242 offset:24832\n"
      "v_mfma_f32_16x16x32_bf16 a[140:143], v[140:143], v[176:179], a[140:143]\n"
      "v_mfma_f32_16x16x32_bf16 a[172:175], v[140:143], v[180:183], a[172:175]\n"
      "v_mfma_f32_16x16x32_bf16 a[204:207], v[140:143], v[184:187], a[204:207]\n"
      "ds_read_b64_tr_b16 v[116:117], v243 offset:24576\n"
      "v_mfma_f32_16x16x32_bf16 a[236:239], v[140:143], v[188:191], a[236:239]\n"
      "ds_read_b128 v[140:143], v241 offset:6144\n"
      "v_mfma_f32_16x16x32_bf16 a[64:67], v[160:163], v[144:147], a[64:67]\n"
      "v_mfma_f32_16x16x32_bf16 a[68:71], v[164:167], v[144:147], a[68:71]\n"
      "ds_read_b64_tr_b16 v[118:119], v243 offset:24832\n"
      "v_mfma_f32_16x16x32_bf16 a[72:75], v[168:171], v[144:147], a[72:75]\n"
      "v_mfma_f32_16x16x32_bf16 a[76:79], v[172:175], v[144:147], a[76:79]\n"
      "v_mfma_f32_16x16x32_bf16 a[144:147], v[144:147], v[176:179], a[144:147]\n"
      "ds_read_b64_tr_b16 v[120:121], v242 offset:25088\n"
      "v_mfma_f32_16x16x32_bf16 a[176:179], v[144:147], v[180:183], a[176:179]\n"
      "v_mfma_f32_16x16x32_bf16 a[208:211], v[144:147], v[184:187], a[208:211]\n"
      "v_mfma_f32_16x16x32_bf16 a[240:243], v[144:147], v[188:191], a[240:243]\n"
      "ds_read_b128 v[144:147], v241 offset:8192\n"
      "ds_read_b64_tr_b16 v[122:123], v242 offset:25344\n"
      "v_mfma_f32_16x16x32_bf16 a[80:83], v[160:163], v[148:151], a[80:83]\n"
      "v_mfma_f32_16x16x32_bf16 a[84:87], v[164:167], v[148:151], a[84:87]\n"
      "v_mfma_f32_16x16x32_bf16 a[88:91], v[168:171], v[148:151], a[88:91]\n"
      "ds_read_b64_tr_b16 v[124:125], v243 offset:25088\n"
      "v_mfma_f32_16x16x32_bf16 a[92:95], v[172:175], v[148:151], a[92:95]\n"
      "v_mfma_f32_16x16x32_bf16 a[148:151], v[148:151], v[176:179], a[148:151]\n"
      "v_mfma_f32_16x16x32_bf16 a[180:183], v[148:151], v[180:183], a[180:183]\n"
      "ds_read_b64_tr_b16 v[126:127], v243 offset:25344\n"
      "v_mfma_f32_16x16x32_bf16 a[212:215], v[148:151], v[184:187], a[212:215]\n"
      "v_mfma_f32_16x16x32_bf16 a[244:247], v[148:151], v[188:191], a[244:247]\n"
      "ds_read_b128 v[148:151], v241 offset:10240\n"
      "v_mfma_f32_16x16x32_bf16 a[96:99], v[160:163], v[152:155], a[96:99]\n"
      "v_mfma_f32_16x16x32_bf16 a[100:103], v[164:167], v[152:155], a[100:103]\n"
      "v_mfma_f32_16x16x32_bf16 a[104:107], v[168:171], v[152:155], a[104:107]\n"
      "v_mfma_f32_16x16x32_bf16 a[108:111], v[172:175], v[152:155], a[108:111]\n"
      "v_mfma_f32_16x16x32_bf16 a[152:155], v[152:155], v[176:179], a[152:155]\n"
      "v_mfma_f32_16x16x32_bf16 a[184:187], v[152:155], v[180:183], a[184:187]\n"
      "v_mfma_f32_16x16x32_bf16 a[216:219], v[152:155], v[184:187], a[216:219]\n"
      "v_mfma_f32_16x16x32_bf16 a[248:251], v[152:155], v[188:191], a[248:251]\n"
      "ds_read_b128 v[152:155], v241 offset:12288\n"
      "v_mfma_f32_16x16x32_bf16 a[112:115], v[160:163], v[156:159], a[112:115]\n"
      "v_mfma_f32_16x16x32_bf16 a[116:119], v[164:167], v[156:159], a[116:119]\n"
      "v_mfma_f32_16x16x32_bf16 a[120:123], v[168:171], v[156:159], a[120:123]\n"
      "v_mfma_f32_16x16x32_bf16 a[124:127], v[172:175], v[156:159], a[124:127]\n"
      "v_mfma_f32_16x16x32_bf16 a[156:159], v[156:159], v[176:179], a[156:159]\n"
      "v_mfma_f32_16x16x32_bf16 a[188:191], v[156:159], v[180:183], a[188:191]\n"
      "v_mfma_f32_16x16x32_bf16 a[220:223], v[156:159], v[184:187], a[220:223]\n"
      "v_mfma_f32_16x16x32_bf16 a[252:255], v[156:159], v[188:191], a[252:255]\n"
      "ds_read_b128 v[156:159], v241 offset:14336\n"
      "s_waitcnt lgkmcnt(14)\n"
      "v_mfma_f32_16x16x32_bf16 a[0:3], v[96:99], v[128:131], a[0:3]\n"
      "v_mfma_f32_16x16x32_bf16 a[4:7], v[100:103], v[128:131], a[4:7]\n"
      "v_mfma_f32_16x16x32_bf16 a[8:11], v[104:107], v[128:131], a[8:11]\n"
      "v_mfma_f32_16x16x32_bf16 a[12:15], v[108:111], v[128:131], a[12:15]\n"
      "s_waitcnt lgkmcnt(5)\n"
      "v_mfma_f32_16x16x32_bf16 a[128:131], v[128:131], v[112:115], a[128:131]\n"
      "v_mfma_f32_16x16x32_bf16 a[160:163], v[128:131], v[116:119], a[160:163]\n"
      "v_mfma_f32_16x16x32_bf16 a[192:195], v[128:131], v[120:123], a[192:195]\n"
      "s_waitcnt lgkmcnt(0)\n"
      "v_mfma_f32_16x16x32_bf16 a[224:227], v[128:131], v[124:127], a[224:227]\n"
      "s_waitcnt vmcnt(0) lgkmcnt(0)\n"
      "s_barrier\n"
      "s_cmp_eq_u32 %12, 0\n s_cbranch_scc0 L_e1_%=\n s_mov_b32 exec_lo, 0\n s_mov_b32 exec_hi, -1\n global_store_dword v249, v255, %15 sc1\n s_mov_b64 exec, -1\n L_e1_%=:\n"
      "v_add_u32 v240, 131072, v236\n"
      "v_add_u32 v241, 131072, v237\n"
      "v_add_u32 v242, 0, v238\n"
      "v_add_u32 v243, 0, v239\n"
      "ds_read_b128 v[128:131], v240 offset:0\n"
      "v_mfma_f32_16x16x32_bf16 a[16:19], v[96:99], v[132:135], a[16:19]\n"
      "ds_read_b64_tr_b16 v[160:161], v242 offset:0\n"
      "v_mfma_f32_16x16x32_bf16 a[20:23], v[100:103], v[132:135], a[20:23]\n"
      "v_mfma_f32_16x16x32_bf16 a[24:27], v[104:107], v[132:135], a[24:27]\n"
      "ds_read_b64_tr_b16 v[162:163], v242 offset:256\n"
      "v_mfma_f32_16x16x32_bf16 a[28:31], v[108:111], v[132:135], a[28:31]\n"
      "s_add_u32 s80, s62, 3145728\n"
      "s_addc_u32 s81, s63, 0\n"
      "s_add_u32 m0, s82, 65536\n"
      "s_nop 0\n"
      "global_load_lds_dwordx4 v232, s[80:81] offset:0\n"
      "v_mfma_f32_16x16x32_bf16 a[132:135], v[132:135], v[112:115], a[132:135]\n"
      "v_mfma_f32_16x16x32_bf16 a[164:167], v[132:135], v[116:119], a[164:167]\n"
      "ds_read_b64_tr_b16 v[164:165], v243 offset:0\n"
      "v_mfma_f32_16x16x32_bf16 a[196:199], v[132:135], v[120:123], a[196:199]\n"
      "v_mfma_f32_16x16x32_bf16 a[228:231], v[132:135], v[124:127], a[228:231]\n"
      "ds_read_b128 v[132:135], v240 offset:2048\n"
      "ds_read_b64_tr_b16 v[166:167], v243 offset:256\n"
      "v_mfma_f32_16x16x32_bf16 a[32:35], v[96:99], v[136:139], a[32:35]\n"
      "v_mfma_f32_16x16x32_bf16 a[36:39], v[100:103], v[136:139], a[36:39]\n"
      "v_mfma_f32_16x16x32_bf16 a[40:43], v[104:107], v[136:139], a[40:43]\n"
      "ds_read_b64_tr_b16 v[168:169], v242 offset:512\n"
      "global_load_lds_dwordx4 v233, s[80:81] offset:1024\n"
      "v_mfma_f32_16x16x32_bf16 a[44:47], v[108:111], v[136:139], a[44:47]\n"
      "v_mfma_f32_16x16x32_bf16 a[136:139], v[136:139], v[112:115], a[136:139]\n"
      "v_mfma_f32_16x16x32_bf16 a[168:171], v[136:139], v[116:119], a[168:171]\n"
      "ds_read_b64_tr_b16 v[170:171], v242 offset:768\n"
      "v_mfma_f32_16x16x32_bf16 a[200:203], v[136:139], v[120:123], a[200:203]\n"
      "v_mfma_f32_16x16x32_bf16 a[232:235], v[136:139], v[124:127], a[232:235]\n"
      "ds_read_b128 v[136:139], v240 offset:4096\n"
      "ds_read_b64_tr_b16 v[172:173], v243 offset:512\n"
      "v_mfma_f32_16x16x32_bf16 a[48:51], v[96:99], v[140:143], a[48:51]\n"
      "v_mfma_f32_16x16x32_bf16 a[52:55], v[100:103], v[140:143], a[52:55]\n"
      "global_load_lds_dwordx4 v234, s[80:81] offset:2048\n"
      "v_mfma_f32_16x16x32_bf16 a[56:59], v[104:107], v[140:143], a[56:59]\n"
      "ds_read_b64_tr_b16 v[174:175], v243 offset:768\n"
      "v_mfma_f32_16x16x32_bf16 a[60:63], v[108:111], v[140:143], a[60:63]\n"
      "v_mfma_f32_16x16x32_bf16 a[140:143], v[140:143], v[112:115], a[140:143]\n"
      "v_mfma_f32_16x16x32_bf16 a[172:175], v[140:143], v[116:119], a[172:175]\n"
      "ds_read_b64_tr_b16 v[176:177], v242 offset:16384\n"
      "v_mfma_f32_16x16x32_bf16 a[204:207], v[140:143], v[120:123], a[204:207]\n"
      "v_mfma_f32_16x16x32_bf16 a[236:239], v[140:143], v[124:127], a[236:239]\n"
      "ds_read_b128 v[140:143], v240 offset:6144\n"
      "ds_read_b64_tr_b16 v[178:179], v242 offset:16640\n"
      "v_mfma_f32_16x16x32_bf16 a[64:67], v[96:99], v[144:147], a[64:67]\n"
      "global_load_lds_dwordx4 v235, s[80:81] offset:3072\n"
      "v_mfma_f32_16x16x32_bf16 a[68:71], v[100:103], v[144:147], a[68:71]\n"
      "v_mfma_f32_16x16x32_bf16 a[72:75], v[104:107], v[144:147], a[72:75]\n"
      "ds_read_b64_tr_b16 v[180:181], v243 offset:16384\n"
      "v_mfma_f32_16x16x32_bf16 a[76:79], v[108:111], v[144:147], a[76:79]\n"
      "v_mfma_f32_16x16x32_bf16 a[144:147], v[144:147], v[112:115], a[144:147]\n"
      "ds_read_b64_tr_b16 v[182:183], v243 offset:16640\n"
      "v_mfma_f32_16x16x32_bf16 a[176:179], v[144:147], v[116:119], a[176:179]\n"
      "v_mfma_f32_16x16x32_bf16 a[208:211], v[144:147], v[120:123], a[208:211]\n"
      "v_mfma_f32_16x16x32_bf16 a[240:243], v[144:147], v[124:127], a[240:243]\n"
      "ds_read_b128 v[144:147], v240 offset:8192\n"
      "ds_read_b64_tr_b16 v[184:185], v242 offset:16896\n"
      "s_add_u32 s80, s64, 3145728\n"
      "s_addc_u32 s81, s65, 0\n"
      "s_add_u32 m0, s82, 81920\n"
      "s_nop 0\n"
      "global_load_lds_dwordx4 v232, s[80:81] offset:0\n"
      "v_mfma_f32_16x16x32_bf16 a[80:83], v[96:99], v[148:151], a[80:83]\n"
      "v_mfma_f32_16x16x32_bf16 a[84:87], v[100:103], v[148:151], a[84:87]\n"
      "v_mfma_f32_16x16x32_bf16 a[88:91], v[104:107], v[148:151], a[88:91]\n"
      "ds_read_b64_tr_b16 v[186:187], v242 offset:17152\n"
      "v_mfma_f32_16x16x32_bf16 a[92:95], v[108:111], v[148:151], a[92:95]\n"
      "v_mfma_f32_16x16x32_bf16 a[148:151], v[148:151], v[112:115], a[148:151]\n"
      "ds_read_b64_tr_b16 v[188:189], v243 offset:16896\n"
      "v_mfma_f32_16x16x32_bf16 a[180:183], v[148:151], v[116:119], a[180:183]\n"
      "v_mfma_f32_16x16x32_bf16 a[212:215], v[148:151], v[120:123], a[212:215]\n"
      "global_load_lds_dwordx4 v233, s[80:81] offset:1024\n"
      "v_mfma_f32_16x16x32_bf16 a[244:247], v[148:151], v[124:127], a[244:247]\n"
      "ds_read_b128 v[148:151], v240 offset:10240\n"
      "ds_read_b64_tr_b16 v[190:191], v243 offset:17152\n"
      "v_mfma_f32_16x16x32_bf16 a[96:99], v[96:99], v[152:155], a[96:99]\n"
      "v_mfma_f32_16x16x32_bf16 a[100:103], v[100:103], v[152:155], a[100:103]\n"
      "v_mfma_f32_16x16x32_bf16 a[104:107], v[104:107], v[152:155], a[104:107]\n"
      "v_mfma_f32_16x16x32_bf16 a[108:111], v[108:111], v[152:155], a[108:111]\n"
      "v_mfma_f32_16x16x32_bf16 a[152:155], v[152:155], v[112:115], a[152:155]\n"
      "v_mfma_f32_16x16x32_bf16 a[184:187], v[152:155], v[116:119], a[184:187]\n"
      "global_load_lds_dwordx4 v234, s[80:81] offset:2048\n"
      "v_mfma_f32_16x16x32_bf16 a[216:219], v[152:155], v[120:123], a[216:219]\n"
      "v_mfma_f32_16x16x32_bf16 a[248:251], v[152:155], v[124:127], a[248:251]\n"
      "ds_read_b128 v[152:155], v240 offset:12288\n"
      "v_mfma_f32_16x16x32_bf16 a[112:115], v[96:99], v[156:159], a[112:115]\n"
      "v_mfma_f32_16x16x32_bf16 a[116:119], v[100:103], v[156:159], a[116:119]\n"
      "v_mfma_f32_16x16x32_bf16 a[120:123], v[104:107], v[156:159], a[120:123]\n"
      "v_mfma_f32_16x16x32_bf16 a[124:127], v[108:111], v[156:159], a[124:127]\n"
      "v_mfma_f32_16x16x32_bf16 a[156:159], v[156:159], v[112:115], a[156:159]\n"
      "global_load_lds_dwordx4 v235, s[80:81] offset:3072\n"
      "v_mfma_f32_16x16x32_bf16 a[188:191], v[156:159], v[116:119], a[188:191]\n"
      "v_mfma_f32_16x16x32_bf16 a[220:223], v[156:159], v[120:123], a[220:223]\n"
      "v_mfma_f32_16x16x32_bf16 a[252:255], v[156:159], v[124:127], a[252:255]\n"
      "ds_read_b128 v[156:159], v240 offset:14336\n"
      "s_waitcnt lgkmcnt(15)\n"
      "v_mfma_f32_16x16x32_bf16 a[0:3], v[160:163], v[128:131], a[0:3]\n"
      "ds_read_b64_tr_b16 v[96:97], v242 offset:8192\n"
      "v_mfma_f32_16x16x32_bf16 a[4:7], v[164:167], v[128:131], a[4:7]\n"
      "v_mfma_f32_16x16x32_bf16 a[8:11], v[168:171], v[128:131], a[8:11]\n"
      "s_waitcnt lgkmcnt(8)\n"
      "v_mfma_f32_16x16x32_bf16 a[12:15], v[172:175], v[128:131], a[12:15]\n"
      "ds_read_b64_tr_b16 v[98:99], v242 offset:8448\n"
      "v_mfma_f32_16x16x32_bf16 a[128:131], v[128:131], v[176:179], a[128:131]\n"
      "s_add_u32 s80, s56, 896\n"
      "s_addc_u32 s81, s57, 0\n"
      "s_add_u32 m0, s82, 98304\n"
      "s_nop 0\n"
      "global_load_lds_dwordx4 v224, s[80:81] offset:0\n"
      "v_mfma_f32_16x16x32_bf16 a[160:163], v[128:131], v[180:183], a[160:163]\n"
      "s_waitcnt lgkmcnt(1)\n"
      "v_mfma_f32_16x16x32_bf16 a[192:195], v[128:131], v[184:187], a[192:195]\n"
      "ds_read_b64_tr_b16 v[100:101], v243 offset:8192\n"
      "v_mfma_f32_16x16x32_bf16 a[224:227], v[128:131], v[188:191], a[224:227]\n"
      "ds_read_b128 v[128:131], v241 offset:0\n"
      "v_mfma_f32_16x16x32_bf16 a[16:19], v[160:163], v[132:135], a[16:19]\n"
      "v_mfma_f32_16x16x32_bf16 a[20:23], v[164:167], v[132:135], a[20:23]\n"
      "ds_read_b64_tr_b16 v[102:103], v243 offset:8448\n"
      "v_mfma_f32_16x16x32_bf16 a[24:27], v[168:171], v[132:135], a[24:27]\n"
      "v_mfma_f32_16x16x32_bf16 a[28:31], v[172:175], v[132:135], a[28:31]\n"
      "v_mfma_f32_16x16x32_bf16 a[132:135], v[132:135], v[176:179], a[132:135]\n"
      "ds_read_b64_tr_b16 v[104:105], v242 offset:8704\n"
      "global_load_lds_dwordx4 v225, s[80:81] offset:1024\n"
      "v_mfma_f32_16x16x32_bf16 a[164:167], v[132:135], v[180:183], a[164:167]\n"
      "v_mfma_f32_16x16x32_bf16 a[196:199], v[132:135], v[184:187], a[196:199]\n"
      "v_mfma_f32_16x16x32_bf16 a[228:231], v[132:135], v[188:191], a[228:231]\n"
      "ds_read_b128 v[132:135], v241 offset:2048\n"
      "ds_read_b64_tr_b16 v[106:107], v242 offset:8960\n"
      "v_mfma_f32_16x16x32_bf16 a[32:35], v[160:163], v[136:139], a[32:35]\n"
      "v_mfma_f32_16x16x32_bf16 a[36:39], v[164:167], v[136:139], a[36:39]\n"
      "v_mfma_f32_16x16x32_bf16 a[40:43], v[168:171], v[136:139], a[40:43]\n"
      "ds_read_b64_tr_b16 v[108:109], v243 offset:8704\n"
      "v_mfma_f32_16x16x32_bf16 a[44:47], v[172:175], v[136:139], a[44:47]\n"
      "v_mfma_f32_16x16x32_bf16 a[136:139], v[136:139], v[176:179], a[136:139]\n"
      "global_load_lds_dwordx4 v226, s[80:81] offset:2048\n"
      "v_mfma_f32_16x16x32_bf16 a[168:171], v[136:139], v[180:183], a[168:171]\n"
      "ds_read_b64_tr_b16 v[110:111], v243 offset:8960\n"
      "v_mfma_f32_16x16x32_bf16 a[200:203], v[136:139], v[184:187], a[200:203]\n"
      "v_mfma_f32_16x16x32_bf16 a[232:235], v[136:139], v[188:191], a[232:235]\n"
      "ds_read_b128 v[136:139], v241 offset:4096\n"
      "v_mfma_f32_16x16x32_bf16 a[48:51], v[160:163], v[140:143], a[48:51]\n"
      "ds_read_b64_tr_b16 v[112:113], v242 offset:24576\n"
      "v_mfma_f32_16x16x32_bf16 a[52:55], v[164:167], v[140:143], a[52:55]\n"
      "v_mfma_f32_16x16x32_bf16 a[56:59], v[168:171], v[140:143], a[56:59]\n"
      "v_mfma_f32_16x16x32_bf16 a[60:63], v[172:175], v[140:143], a[60:63]\n"
      "ds_read_b64_tr_b16 v[114:115], v242 offset:24832\n"
      "v_mfma_f32_16x16x32_bf16 a[140:143], v[140:143], v[176:179], a[140:143]\n"
      "global_load_lds_dwordx4 v227, s[80:81] offset:3072\n"
      "v_mfma_f32_16x16x32_bf16 a[172:175], v[140:143], v[180:183], a[172:175]\n"
      "v_mfma_f32_16x16x32_bf16 a[204:207], v[140:143], v[184:187], a[204:207]\n"
      "ds_read_b64_tr_b16 v[116:117], v243 offset:24576\n"
      "v_mfma_f32_16x16x32_bf16 a[236:239], v[140:143], v[188:191], a[236:239]\n"
      "ds_read_b128 v[140:143], v241 offset:6144\n"
      "v_mfma_f32_16x16x32_bf16 a[64:67], v[160:163], v[144:147], a[64:67]\n"
      "v_mfma_f32_16x16x32_bf16 a[68:71], v[164:167], v[144:147], a[68:71]\n"
      "ds_read_b64_tr_b16 v[118:119], v243 offset:24832\n"
      "v_mfma_f32_16x16x32_bf16 a[72:75], v[168:171], v[144:147], a[72:75]\n"
      "v_mfma_f32_16x16x32_bf16 a[76:79], v[172:175], v[144:147], a[76:79]\n"
      "v_mfma_f32_16x16x32_bf16 a[144:147], v[144:147], v[176:179], a[144:147]\n"
      "ds_read_b64_tr_b16 v[120:121], v242 offset:25088\n"
      "s_add_u32 s80, s58, 896\n"
      "s_addc_u32 s81, s59, 0\n"
      "s_add_u32 m0, s82, 114688\n"
      "s_nop 0\n"
      "global_load_lds_dwordx4 v224, s[80:81] offset:0\n"
      "v_mfma_f32_16x16x32_bf16 a[176:179], v[144:147], v[180:183], a[176:179]\n"
      "v_mfma_f32_16x16x32_bf16 a[208:211], v[144:147], v[184:187], a[208:211]\n"
      "v_mfma_f32_16x16x32_bf16 a[240:243], v[144:147], v[188:191], a[240:243]\n"
      "ds_read_b128 v[144:147], v241 offset:8192\n"
      "ds_read_b64_tr_b16 v[122:123], v242 offset:25344\n"
      "v_mfma_f32_16x16x32_bf16 a[80:83], v[160:163], v[148:151], a[80:83]\n"
      "v_mfma_f32_16x16x32_bf16 a[84:87], v[164:167], v[148:151], a[84:87]\n"
      "v_mfma_f32_16x16x32_bf16 a[88:91], v[168:171], v[148:151], a[88:91]\n"
      "ds_read_b64_tr_b16 v[124:125], v243 offset:25088\n"
      "v_mfma_f32_16x16x32_bf16 a[92:95], v[172:175], v[148:151], a[92:95]\n"
      "v_mfma_f32_16x16x32_bf16 a[148:151], v[148:151], v[176:179], a[148:151]\n"
      "global_load_lds_dwordx4 v225, s[80:81] offset:1024\n"
      "v_mfma_f32_16x16x32_bf16 a[180:183], v[148:151], v[180:183], a[180:183]\n"
      "ds_read_b64_tr_b16 v[126:127], v243 offset:25344\n"
      "v_mfma_f32_16x16x32_bf16 a[212:215], v[148:151], v[184:187], a[212:215]\n"
      "v_mfma_f32_16x16x32_bf16 a[244:247], v[148:151], v[188:191], a[244:247]\n"
      "ds_read_b128 v[148:151], v241 offset:10240\n"
      "v_mfma_f32_16x16x32_bf16 a[96:99], v[160:163], v[152:155], a[96:99]\n"
      "v_mfma_f32_16x16x32_bf16 a[100:103], v[164:167], v[152:155], a[100:103]\n"
      "v_mfma_f32_16x16x32_bf16 a[104:107], v[168:171], v[152:155], a[104:107]\n"
      "v_mfma_f32_16x16x32_bf16 a[108:111], v[172:175], v[152:155], a[108:111]\n"
      "v_mfma_f32_16x16x32_bf16 a[152:155], v[152:155], v[176:179], a[152:155]\n"
      "global_load_lds_dwordx4 v226, s[80:81] offset:2048\n"
      "v_mfma_f32_16x16x32_bf16 a[184:187], v[152:155], v[180:183], a[184:187]\n"
      "v_mfma_f32_16x16x32_bf16 a[216:219], v[152:155], v[184:187], a[216:219]\n"
      "v_mfma_f32_16x16x32_bf16 a[248:251], v[152:155], v[188:191], a[248:251]\n"
      "ds_read_b128 v[152:155], v241 offset:12288\n"
      "v_mfma_f32_16x16x32_bf16 a[112:115], v[160:163], v[156:159], a[112:115]\n"
      "v_mfma_f32_16x16x32_bf16 a[116:119], v[164:167], v[156:159], a[116:119]\n"
      "v_mfma_f32_16x16x32_bf16 a[120:123], v[168:171], v[156:159], a[120:123]\n"
      "v_mfma_f32_16x16x32_bf16 a[124:127], v[172:175], v[156:159], a[124:127]\n"
      "v_mfma_f32_16x16x32_bf16 a[156:159], v[156:159], v[176:179], a[156:159]\n"
      "global_load_lds_dwordx4 v227, s[80:81] offset:3072\n"
      "v_mfma_f32_16x16x32_bf16 a[188:191], v[156:159], v[180:183], a[188:191]\n"
      "v_mfma_f32_16x16x32_bf16 a[220:223], v[156:159], v[184:187], a[220:223]\n"
      "v_mfma_f32_16x16x32_bf16 a[252:255], v[156:159], v[188:191], a[252:255]\n"
      "ds_read_b128 v[156:159], v241 offset:14336\n"
      "s_waitcnt lgkmcnt(14)\n"
      "v_mfma_f32_16x16x32_bf16 a[0:3], v[96:99], v[128:131], a[0:3]\n"
      "v_mfma_f32_16x16x32_bf16 a[4:7], v[100:103], v[128:131], a[4:7]\n"
      "v_mfma_f32_16x16x32_bf16 a[8:11], v[104:107], v[128:131], a[8:11]\n"
      "v_mfma_f32_16x16x32_bf16 a[12:15], v[108:111], v[128:131], a[12:15]\n"
      "s_waitcnt lgkmcnt(5)\n"
      "v_mfma_f32_16x16x32_bf16 a[128:131], v[128:131], v[112:115], a[128:131]\n"
      "v_mfma_f32_16x16x32_bf16 a[160:163], v[128:131], v[116:119], a[160:163]\n"
      "v_mfma_f32_16x16x32_bf16 a[192:195], v[128:131], v[120:123], a[192:195]\n"
      "s_waitcnt lgkmcnt(0)\n"
      "v_mfma_f32_16x16x32_bf16 a[224:227], v[128:131], v[124:127], a[224:227]\n"
      "s_waitcnt vmcnt(8) lgkmcnt(0)\n"
      "s_barrier\n"
      "v_add_u32 v240, 32768, v236\n"
      "v_add_u32 v241, 32768, v237\n"
      "v_add_u32 v242, 65536, v238\n"
      "v_add_u32 v243, 65536, v239\n"
      "ds_read_b128 v[128:131], v240 offset:0\n"
      "v_mfma_f32_16x16x32_bf16 a[16:19], v[96:99], v[132:135], a[16:19]\n"
      "ds_read_b64_tr_b16 v[160:161], v242 offset:0\n"
      "v_mfma_f32_16x16x32_bf16 a[20:23], v[100:103], v[132:135], a[20:23]\n"
      "v_mfma_f32_16x16x32_bf16 a[24:27], v[104:107], v[132:135], a[24:27]\n"
      "ds_read_b64_tr_b16 v[162:163], v242 offset:256\n"
      "v_mfma_f32_16x16x32_bf16 a[28:31], v[108:111], v[132:135], a[28:31]\n"
      "s_add_u32 s80, s62, 3670016\n"
      "s_addc_u32 s81, s63, 0\n"
      "s_add_u32 m0, s82, 131072\n"
      "s_nop 0\n"
      "global_load_lds_dwordx4 v232, s[80:81] offset:0\n"
      "v_mfma_f32_16x16x32_bf16 a[132:135], v[132:135], v[112:115], a[132:135]\n"
      "v_mfma_f32_16x16x32_bf16 a[164:167], v[132:135], v[116:119], a[164:167]\n"
      "ds_read_b64_tr_b16 v[164:165], v243 offset:0\n"
      "v_mfma_f32_16x16x32_bf16 a[196:199], v[132:135], v[120:123], a[196:199]\n"
      "v_mfma_f32_16x16x32_bf16 a[228:231], v[132:135], v[124:127], a[228:231]\n"
      "ds_read_b128 v[132:135], v240 offset:2048\n"
      "ds_read_b64_tr_b16 v[166:167], v243 offset:256\n"
      "v_mfma_f32_16x16x32_bf16 a[32:35], v[96:99], v[136:139], a[32:35]\n"
      "v_mfma_f32_16x16x32_bf16 a[36:39], v[100:103], v[136:139], a[36:39]\n"
      "v_mfma_f32_16x16x32_bf16 a[40:43], v[104:107], v[136:139], a[40:43]\n"
      "ds_read_b64_tr_b16 v[168:169], v242 offset:512\n"
      "global_load_lds_dwordx4 v233, s[80:81] offset:1024\n"
      "v_mfma_f32_16x16x32_bf16 a[44:47], v[108:111], v[136:139], a[44:47]\n"
      "v_mfma_f32_16x16x32_bf16 a[136:139], v[136:139], v[112:115], a[136:139]\n"
      "v_mfma_f32_16x16x32_bf16 a[168:171], v[136:139], v[116:119], a[168:171]\n"
      "ds_read_b64_tr_b16 v[170:171], v242 offset:768\n"
      "v_mfma_f32_16x16x32_bf16 a[200:203], v[136:139], v[120:123], a[200:203]\n"
      "v_mfma_f32_16x16x32_bf16 a[232:235], v[136:139], v[124:127], a[232:235]\n"
      "ds_read_b128 v[136:139], v240 offset:4096\n"
      "ds_read_b64_tr_b16 v[172:173], v243 offset:512\n"
      "v_mfma_f32_16x16x32_bf16 a[48:51], v[96:99], v[140:143], a[48:51]\n"
      "v_mfma_f32_16x16x32_bf16 a[52:55], v[100:103], v[140:143], a[52:55]\n"
      "global_load_lds_dwordx4 v234, s[80:81] offset:2048\n"
      "v_mfma_f32_16x16x32_bf16 a[56:59], v[104:107], v[140:143], a[56:59]\n"
      "ds_read_b64_tr_b16 v[174:175], v243 offset:768\n"
      "v_mfma_f32_16x16x32_bf16 a[60:63], v[108:111], v[140:143], a[60:63]\n"
      "v_mfma_f32_16x16x32_bf16 a[140:143], v[140:143], v[112:115], a[140:143]\n"
      "v_mfma_f32_16x16x32_bf16 a[172:175], v[140:143], v[116:119], a[172:175]\n"
      "ds_read_b64_tr_b16 v[176:177], v242 offset:16384\n"
      "v_mfma_f32_16x16x32_bf16 a[204:207], v[140:143], v[120:123], a[204:207]\n"
      "v_mfma_f32_16x16x32_bf16 a[236:239], v[140:143], v[124:127], a[236:239]\n"
      "ds_read_b128 v[140:143], v240 offset:6144\n"
      "ds_read_b64_tr_b16 v[178:179], v242 offset:16640\n"
      "v_mfma_f32_16x16x32_bf16 a[64:67], v[96:99], v[144:147], a[64:67]\n"
      "global_load_lds_dwordx4 v235, s[80:81] offset:3072\n"
      "v_mfma_f32_16x16x32_bf16 a[68:71], v[100:103], v[144:147], a[68:71]\n"
      "v_mfma_f32_16x16x32_bf16 a[72:75], v[104:107], v[144:147], a[72:75]\n"
      "ds_read_b64_tr_b16 v[180:181], v243 offset:16384\n"
      "v_mfma_f32_16x16x32_bf16 a[76:79], v[108:111], v[144:147], a[76:79]\n"
      "v_mfma_f32_16x16x32_bf16 a[144:147], v[144:147], v[112:115], a[144:147]\n"
      "ds_read_b64_tr_b16 v[182:183], v243 offset:16640\n"
      "v_mfma_f32_16x16x32_bf16 a[176:179], v[144:147], v[116:119], a[176:179]\n"
      "v_mfma_f32_16x16x32_bf16 a[208:211], v[144:147], v[120:123], a[208:211]\n"
      "v_mfma_f32_16x16x32_bf16 a[240:243], v[144:147], v[124:127], a[240:243]\n"
      "ds_read_b128 v[144:147], v240 offset:8192\n"
      "ds_read_b64_tr_b16 v[184:185], v242 offset:16896\n"
      "s_add_u32 s80, s64, 3670016\n"
      "s_addc_u32 s81, s65, 0\n"
      "s_add_u32 m0, s82, 147456\n"
      "s_nop 0\n"
      "global_load_lds_dwordx4 v232, s[80:81] offset:0\n"
      "v_mfma_f32_16x16x32_bf16 a[80:83], v[96:99], v[148:151], a[80:83]\n"
      "v_mfma_f32_16x16x32_bf16 a[84:87], v[100:103], v[148:151], a[84:87]\n"
      "v_mfma_f32_16x16x32_bf16 a[88:91], v[104:107], v[148:151], a[88:91]\n"
      "ds_read_b64_tr_b16 v[186:187], v242 offset:17152\n"
      "v_mfma_f32_16x16x32_bf16 a[92:95], v[108:111], v[148:151], a[92:95]\n"
      "v_mfma_f32_16x16x32_bf16 a[148:151], v[148:151], v[112:115], a[148:151]\n"
      "ds_read_b64_tr_b16 v[188:189], v243 offset:16896\n"
      "v_mfma_f32_16x16x32_bf16 a[180:183], v[148:151], v[116:119], a[180:183]\n"
      "v_mfma_f32_16x16x32_bf16 a[212:215], v[148:151], v[120:123], a[212:215]\n"
      "global_load_lds_dwordx4 v233, s[80:81] offset:1024\n"
      "v_mfma_f32_16x16x32_bf16 a[244:247], v[148:151], v[124:127], a[244:247]\n"
      "ds_read_b128 v[148:151], v240 offset:10240\n"
      "ds_read_b64_tr_b16 v[190:191], v243 offset:17152\n"
      "v_mfma_f32_16x16x32_bf16 a[96:99], v[96:99], v[152:155], a[96:99]\n"
      "v_mfma_f32_16x16x32_bf16 a[100:103], v[100:103], v[152:155], a[100:103]\n"
      "v_mfma_f32_16x16x32_bf16 a[104:107], v[104:107], v[152:155], a[104:107]\n"
      "v_mfma_f32_16x16x32_bf16 a[108:111], v[108:111], v[152:155], a[108:111]\n"
      "v_mfma_f32_16x16x32_bf16 a[152:155], v[152:155], v[112:115], a[152:155]\n"
      "v_mfma_f32_16x16x32_bf16 a[184:187], v[152:155], v[116:119], a[184:187]\n"
      "global_load_lds_dwordx4 v234, s[80:81] offset:2048\n"
      "v_mfma_f32_16x16x32_bf16 a[216:219], v[152:155], v[120:123], a[216:219]\n"
      "v_mfma_f32_16x16x32_bf16 a[248:251], v[152:155], v[124:127], a[248:251]\n"
      "ds_read_b128 v[152:155], v240 offset:12288\n"
      "v_mfma_f32_16x16x32_bf16 a[112:115], v[96:99], v[156:159], a[112:115]\n"
      "v_mfma_f32_16x16x32_bf16 a[116:119], v[100:103], v[156:159], a[116:119]\n"
      "v_mfma_f32_16x16x32_bf16 a[120:123], v[104:107], v[156:159], a[120:123]\n"
      "v_mfma_f32_16x16x32_bf16 a[124:127], v[108:111], v[156:159], a[124:127]\n"
      "v_mfma_f32_16x16x32_bf16 a[156:159], v[156:159], v[112:115], a[156:159]\n"
      "global_load_lds_dwordx4 v235, s[80:81] offset:3072\n"
      "v_mfma_f32_16x16x32_bf16 a[188:191], v[156:159], v[116:119], a[188:191]\n"
      "v_mfma_f32_16x16x32_bf16 a[220:223], v[156:159], v[120:123], a[220:223]\n"
      "v_mfma_f32_16x16x32_bf16 a[252:255], v[156:159], v[124:127], a[252:255]\n"
      "ds_read_b128 v[156:159], v240 offset:14336\n"
      "s_waitcnt lgkmcnt(15)\n"
      "v_mfma_f32_16x16x32_bf16 a[0:3], v[160:163], v[128:131], a[0:3]\n"
      "ds_read_b64_tr_b16 v[96:97], v242 offset:8192\n"
      "v_mfma_f32_16x16x32_bf16 a[4:7], v[164:167], v[128:131], a[4:7]\n"
      "v_mfma_f32_16x16x32_bf16 a[8:11], v[168:171], v[128:131], a[8:11]\n"
      "s_waitcnt lgkmcnt(8)\n"
      "v_mfma_f32_16x16x32_bf16 a[12:15], v[172:175], v[128:131], a[12:15]\n"
      "ds_read_b64_tr_b16 v[98:99], v242 offset:8448\n"
      "v_mfma_f32_16x16x32_bf16 a[128:131], v[128:131], v[176:179], a[128:131]\n"
      "s_add_u32 s80, s66, 0\n"
      "s_addc_u32 s81, s67, 0\n"
      "s_add_u32 m0, s82, 0\n"
      "s_nop 0\n"
      "global_load_lds_dwordx4 v228, s[80:81] offset:0\n"
      "v_mfma_f32_16x16x32_bf16 a[160:163], v[128:131], v[180:183], a[160:163]\n"
      "s_waitcnt lgkmcnt(1)\n"
      "v_mfma_f32_16x16x32_bf16 a[192:195], v[128:131], v[184:187], a[192:195]\n"
      "ds_read_b64_tr_b16 v[100:101], v243 offset:8192\n"
      "v_mfma_f32_16x16x32_bf16 a[224:227], v[128:131], v[188:191], a[224:227]\n"
      "ds_read_b128 v[128:131], v241 offset:0\n"
      "v_mfma_f32_16x16x32_bf16 a[16:19], v[160:163], v[132:135], a[16:19]\n"
      "v_mfma_f32_16x16x32_bf16 a[20:23], v[164:167], v[132:135], a[20:23]\n"
      "ds_read_b64_tr_b16 v[102:103], v243 offset:8448\n"
      "v_mfma_f32_16x16x32_bf16 a[24:27], v[168:171], v[132:135], a[24:27]\n"
      "v_mfma_f32_16x16x32_bf16 a[28:31], v[172:175], v[132:135], a[28:31]\n"
      "v_mfma_f32_16x16x32_bf16 a[132:135], v[132:135], v[176:179], a[132:135]\n"
      "ds_read_b64_tr_b16 v[104:105], v242 offset:8704\n"
      "global_load_lds_dwordx4 v229, s[80:81] offset:1024\n"
      "v_mfma_f32_16x16x32_bf16 a[164:167], v[132:135], v[180:183], a[164:167]\n"
      "v_mfma_f32_16x16x32_bf16 a[196:199], v[132:135], v[184:187], a[196:199]\n"
      "v_mfma_f32_16x16x32_bf16 a[228:231], v[132:135], v[188:191], a[228:231]\n"
      "ds_read_b128 v[132:135], v241 offset:2048\n"
      "ds_read_b64_tr_b16 v[106:107], v242 offset:8960\n"
      "v_mfma_f32_16x16x32_bf16 a[32:35], v[160:163], v[136:139], a[32:35]\n"
      "v_mfma_f32_16x16x32_bf16 a[36:39], v[164:167], v[136:139], a[36:39]\n"
      "v_mfma_f32_16x16x32_bf16 a[40:43], v[168:171], v[136:139], a[40:43]\n"
      "ds_read_b64_tr_b16 v[108:109], v243 offset:8704\n"
      "v_mfma_f32_16x16x32_bf16 a[44:47], v[172:175], v[136:139], a[44:47]\n"
      "v_mfma_f32_16x16x32_bf16 a[136:139], v[136:139], v[176:179], a[136:139]\n"
      "global_load_lds_dwordx4 v230, s[80:81] offset:2048\n"
      "v_mfma_f32_16x16x32_bf16 a[168:171], v[136:139], v[180:183], a[168:171]\n"
      "ds_read_b64_tr_b16 v[110:111], v243 offset:8960\n"
      "v_mfma_f32_16x16x32_bf16 a[200:203], v[136:139], v[184:187], a[200:203]\n"
      "v_mfma_f32_16x16x32_bf16 a[232:235], v[136:139], v[188:191], a[232:235]\n"
      "ds_read_b128 v[136:139], v241 offset:4096\n"
      "v_mfma_f32_16x16x32_bf16 a[48:51], v[160:163], v[140:143], a[48:51]\n"
      "ds_read_b64_tr_b16 v[112:113], v242 offset:24576\n"
      "v_mfma_f32_16x16x32_bf16 a[52:55], v[164:167], v[140:143], a[52:55]\n"
      "v_mfma_f32_16x16x32_bf16 a[56:59], v[168:171], v[140:143], a[56:59]\n"
      "v_mfma_f32_16x16x32_bf16 a[60:63], v[172:175], v[140:143], a[60:63]\n"
      "ds_read_b64_tr_b16 v[114:115], v242 offset:24832\n"
      "v_mfma_f32_16x16x32_bf16 a[140:143], v[140:143], v[176:179], a[140:143]\n"
      "global_load_lds_dwordx4 v231, s[80:81] offset:3072\n"
      "v_mfma_f32_16x16x32_bf16 a[172:175], v[140:143], v[180:183], a[172:175]\n"
      "v_mfma_f32_16x16x32_bf16 a[204:207], v[140:143], v[184:187], a[204:207]\n"
      "ds_read_b64_tr_b16 v[116:117], v243 offset:24576\n"
      "v_mfma_f32_16x16x32_bf16 a[236:239], v[140:143], v[188:191], a[236:239]\n"
      "ds_read_b128 v[140:143], v241 offset:6144\n"
      "v_mfma_f32_16x16x32_bf16 a[64:67], v[160:163], v[144:147], a[64:67]\n"
      "v_mfma_f32_16x16x32_bf16 a[68:71], v[164:167], v[144:147], a[68:71]\n"
      "ds_read_b64_tr_b16 v[118:119], v243 offset:24832\n"
      "v_mfma_f32_16x16x32_bf16 a[72:75], v[168:171], v[144:147], a[72:75]\n"
      "v_mfma_f32_16x16x32_bf16 a[76:79], v[172:175], v[144:147], a[76:79]\n"
      "v_mfma_f32_16x16x32_bf16 a[144:147], v[144:147], v[176:179], a[144:147]\n"
      "ds_read_b64_tr_b16 v[120:121], v242 offset:25088\n"
      "s_add_u32 s80, s68, 0\n"
      "s_addc_u32 s81, s69, 0\n"
      "s_add_u32 m0, s82, 16384\n"
      "s_nop 0\n"
      "global_load_lds_dwordx4 v228, s[80:81] offset:0\n"
      "v_mfma_f32_16x16x32_bf16 a[176:179], v[144:147], v[180:183], a[176:179]\n"
      "v_mfma_f32_16x16x32_bf16 a[208:211], v[144:147], v[184:187], a[208:211]\n"
      "v_mfma_f32_16x16x32_bf16 a[240:243], v[144:147], v[188:191], a[240:243]\n"
      "ds_read_b128 v[144:147], v241 offset:8192\n"
      "ds_read_b64_tr_b16 v[122:123], v242 offset:25344\n"
      "v_mfma_f32_16x16x32_bf16 a[80:83], v[160:163], v[148:151], a[80:83]\n"
      "v_mfma_f32_16x16x32_bf16 a[84:87], v[164:167], v[148:151], a[84:87]\n"
      "v_mfma_f32_16x16x32_bf16 a[88:91], v[168:171], v[148:151], a[88:91]\n"
      "ds_read_b64_tr_b16 v[124:125], v243 offset:25088\n"
      "v_mfma_f32_16x16x32_bf16 a[92:95], v[172:175], v[148:151], a[92:95]\n"
      "v_mfma_f32_16x16x32_bf16 a[148:151], v[148:151], v[176:179], a[148:151]\n"
      "global_load_lds_dwordx4 v229, s[80:81] offset:1024\n"
      "v_mfma_f32_16x16x32_bf16 a[180:183], v[148:151], v[180:183], a[180:183]\n"
      "ds_read_b64_tr_b16 v[126:127], v243 offset:25344\n"
      "v_mfma_f32_16x16x32_bf16 a[212:215], v[148:151], v[184:187], a[212:215]\n"
      "v_mfma_f32_16x16x32_bf16 a[244:247], v[148:151], v[188:191], a[244:247]\n"
      "ds_read_b128 v[148:151], v241 offset:10240\n"
      "v_mfma_f32_16x16x32_bf16 a[96:99], v[160:163], v[152:155], a[96:99]\n"
      "v_mfma_f32_16x16x32_bf16 a[100:103], v[164:167], v[152:155], a[100:103]\n"
      "v_mfma_f32_16x16x32_bf16 a[104:107], v[168:171], v[152:155], a[104:107]\n"
      "v_mfma_f32_16x16x32_bf16 a[108:111], v[172:175], v[152:155], a[108:111]\n"
      "v_mfma_f32_16x16x32_bf16 a[152:155], v[152:155], v[176:179], a[152:155]\n"
      "global_load_lds_dwordx4 v230, s[80:81] offset:2048\n"
      "v_mfma_f32_16x16x32_bf16 a[184:187], v[152:155], v[180:183], a[184:187]\n"
      "v_mfma_f32_16x16x32_bf16 a[216:219], v[152:155], v[184:187], a[216:219]\n"
      "v_mfma_f32_16x16x32_bf16 a[248:251], v[152:155], v[188:191], a[248:251]\n"
      "ds_read_b128 v[152:155], v241 offset:12288\n"
      "v_mfma_f32_16x16x32_bf16 a[112:115], v[160:163], v[156:159], a[112:115]\n"
      "v_mfma_f32_16x16x32_bf16 a[116:119], v[164:167], v[156:159], a[116:119]\n"
      "v_mfma_f32_16x16x32_bf16 a[120:123], v[168:171], v[156:159], a[120:123]\n"
      "v_mfma_f32_16x16x32_bf16 a[124:127], v[172:175], v[156:159], a[124:127]\n"
      "v_mfma_f32_16x16x32_bf16 a[156:159], v[156:159], v[176:179], a[156:159]\n"
      "global_load_lds_dwordx4 v231, s[80:81] offset:3072\n"
      "v_mfma_f32_16x16x32_bf16 a[188:191], v[156:159], v[180:183], a[188:191]\n"
      "v_mfma_f32_16x16x32_bf16 a[220:223], v[156:159], v[184:187], a[220:223]\n"
      "v_mfma_f32_16x16x32_bf16 a[252:255], v[156:159], v[188:191], a[252:255]\n"
      "ds_read_b128 v[156:159], v241 offset:14336\n"
      "s_waitcnt lgkmcnt(14)\n"
      "v_mfma_f32_16x16x32_bf16 a[0:3], v[96:99], v[128:131], a[0:3]\n"
      "v_mfma_f32_16x16x32_bf16 a[4:7], v[100:103], v[128:131], a[4:7]\n"
      "v_mfma_f32_16x16x32_bf16 a[8:11], v[104:107], v[128:131], a[8:11]\n"
      "v_mfma_f32_16x16x32_bf16 a[12:15], v[108:111], v[128:131], a[12:15]\n"
      "s_waitcnt lgkmcnt(5)\n"
      "v_mfma_f32_16x16x32_bf16 a[128:131], v[128:131], v[112:115], a[128:131]\n"
      "v_mfma_f32_16x16x32_bf16 a[160:163], v[128:131], v[116:119], a[160:163]\n"
      "v_mfma_f32_16x16x32_bf16 a[192:195], v[128:131], v[120:123], a[192:195]\n"
      "s_waitcnt lgkmcnt(0)\n"
      "v_mfma_f32_16x16x32_bf16 a[224:227], v[128:131], v[124:127], a[224:227]\n"
      "s_waitcnt vmcnt(8) lgkmcnt(0)\n"
      "s_barrier\n"
      "v_add_u32 v240, 98304, v236\n"
      "v_add_u32 v241, 98304, v237\n"
      "v_add_u32 v242, 131072, v238\n"
      "v_add_u32 v243, 131072, v239\n"
      "ds_read_b128 v[128:131], v240 offset:0\n"
      "v_mfma_f32_16x16x32_bf16 a[16:19], v[96:99], v[132:135], a[16:19]\n"
      "ds_read_b64_tr_b16 v[160:161], v242 offset:0\n"
      "v_mfma_f32_16x16x32_bf16 a[20:23], v[100:103], v[132:135], a[20:23]\n"
      "v_mfma_f32_16x16x32_bf16 a[24:27], v[104:107], v[132:135], a[24:27]\n"
      "ds_read_b64_tr_b16 v[162:163], v242 offset:256\n"
      "v_mfma_f32_16x16x32_bf16 a[28:31], v[108:111], v[132:135], a[28:31]\n"
      "s_add_u32 s80, s70, 0\n"
      "s_addc_u32 s81, s71, 0\n"
      "s_add_u32 m0, s82, 32768\n"
      "s_nop 0\n"
      "global_load_lds_dwordx4 v232, s[80:81] offset:0\n"
      "v_mfma_f32_16x16x32_bf16 a[132:135], v[132:135], v[112:115], a[132:135]\n"
      "v_mfma_f32_16x16x32_bf16 a[164:167], v[132:135], v[116:119], a[164:167]\n"
      "ds_read_b64_tr_b16 v[164:165], v243 offset:0\n"
      "v_mfma_f32_16x16x32_bf16 a[196:199], v[132:135], v[120:123], a[196:199]\n"
      "v_mfma_f32_16x16x32_bf16 a[228:231], v[132:135], v[124:127], a[228:231]\n"
      "ds_read_b128 v[132:135], v240 offset:2048\n"
      "ds_read_b64_tr_b16 v[166:167], v243 offset:256\n"
      "v_mfma_f32_16x16x32_bf16 a[32:35], v[96:99], v[136:139], a[32:35]\n"
      "v_mfma_f32_16x16x32_bf16 a[36:39], v[100:103], v[136:139], a[36:39]\n"
      "v_mfma_f32_16x16x32_bf16 a[40:43], v[104:107], v[136:139], a[40:43]\n"
      "ds_read_b64_tr_b16 v[168:169], v242 offset:512\n"
      "global_load_lds_dwordx4 v233, s[80:81] offset:1024\n"
      "v_mfma_f32_16x16x32_bf16 a[44:47], v[108:111], v[136:139], a[44:47]\n"
      "v_mfma_f32_16x16x32_bf16 a[136:139], v[136:139], v[112:115], a[136:139]\n"
      "v_mfma_f32_16x16x32_bf16 a[168:171], v[136:139], v[116:119], a[168:171]\n"
      "ds_read_b64_tr_b16 v[170:171], v242 offset:768\n"
      "v_mfma_f32_16x16x32_bf16 a[200:203], v[136:139], v[120:123], a[200:203]\n"
      "v_mfma_f32_16x16x32_bf16 a[232:235], v[136:139], v[124:127], a[232:235]\n"
      "ds_read_b128 v[136:139], v240 offset:4096\n"
      "ds_read_b64_tr_b16 v[172:173], v243 offset:512\n"
      "v_mfma_f32_16x16x32_bf16 a[48:51], v[96:99], v[140:143], a[48:51]\n"
      "v_mfma_f32_16x16x32_bf16 a[52:55], v[100:103], v[140:143], a[52:55]\n"
      "global_load_lds_dwordx4 v234, s[80:81] offset:2048\n"
      "v_mfma_f32_16x16x32_bf16 a[56:59], v[104:107], v[140:143], a[56:59]\n"
      "ds_read_b64_tr_b16 v[174:175], v243 offset:768\n"
      "v_mfma_f32_16x16x32_bf16 a[60:63], v[108:111], v[140:143], a[60:63]\n"
      "v_mfma_f32_16x16x32_bf16 a[140:143], v[140:143], v[112:115], a[140:143]\n"
      "v_mfma_f32_16x16x32_bf16 a[172:175], v[140:143], v[116:119], a[172:175]\n"
      "ds_read_b64_tr_b16 v[176:177], v242 offset:16384\n"
      "v_mfma_f32_16x16x32_bf16 a[204:207], v[140:143], v[120:123], a[204:207]\n"
      "v_mfma_f32_16x16x32_bf16 a[236:239], v[140:143], v[124:127], a[236:239]\n"
      "ds_read_b128 v[140:143], v240 offset:6144\n"
      "ds_read_b64_tr_b16 v[178:179], v242 offset:16640\n"
      "v_mfma_f32_16x16x32_bf16 a[64:67], v[96:99], v[144:147], a[64:67]\n"
      "global_load_lds_dwordx4 v235, s[80:81] offset:3072\n"
      "v_mfma_f32_16x16x32_bf16 a[68:71], v[100:103], v[144:147], a[68:71]\n"
      "v_mfma_f32_16x16x32_bf16 a[72:75], v[104:107], v[144:147], a[72:75]\n"
      "ds_read_b64_tr_b16 v[180:181], v243 offset:16384\n"
      "v_mfma_f32_16x16x32_bf16 a[76:79], v[108:111], v[144:147], a[76:79]\n"
      "v_mfma_f32_16x16x32_bf16 a[144:147], v[144:147], v[112:115], a[144:147]\n"
      "ds_read_b64_tr_b16 v[182:183], v243 offset:16640\n"
      "v_mfma_f32_16x16x32_bf16 a[176:179], v[144:147], v[116:119], a[176:179]\n"
      "v_mfma_f32_16x16x32_bf16 a[208:211], v[144:147], v[120:123], a[208:211]\n"
      "v_mfma_f32_16x16x32_bf16 a[240:243], v[144:147], v[124:127], a[240:243]\n"
      "ds_read_b128 v[144:147], v240 offset:8192\n"
      "ds_read_b64_tr_b16 v[184:185], v242 offset:16896\n"
      "s_add_u32 s80, s66, 128\n"
      "s_addc_u32 s81, s67, 0\n"
      "s_add_u32 m0, s82, 49152\n"
      "s_nop 0\n"
      "global_load_lds_dwordx4 v228, s[80:81] offset:0\n"
      "v_mfma_f32_16x16x32_bf16 a[80:83], v[96:99], v[148:151], a[80:83]\n"
      "v_mfma_f32_16x16x32_bf16 a[84:87], v[100:103], v[148:151], a[84:87]\n"
      "v_mfma_f32_16x16x32_bf16 a[88:91], v[104:107], v[148:151], a[88:91]\n"
      "ds_read_b64_tr_b16 v[186:187], v242 offset:17152\n"
      "v_mfma_f32_16x16x32_bf16 a[92:95], v[108:111], v[148:151], a[92:95]\n"
      "v_mfma_f32_16x16x32_bf16 a[148:151], v[148:151], v[112:115], a[148:151]\n"
      "ds_read_b64_tr_b16 v[188:189], v243 offset:16896\n"
      "v_mfma_f32_16x16x32_bf16 a[180:183], v[148:151], v[116:119], a[180:183]\n"
      "v_mfma_f32_16x16x32_bf16 a[212:215], v[148:151], v[120:123], a[212:215]\n"
      "global_load_lds_dwordx4 v229, s[80:81] offset:1024\n"
      "v_mfma_f32_16x16x32_bf16 a[244:247], v[148:151], v[124:127], a[244:247]\n"
      "ds_read_b128 v[148:151], v240 offset:10240\n"
      "ds_read_b64_tr_b16 v[190:191], v243 offset:17152\n"
      "v_mfma_f32_16x16x32_bf16 a[96:99], v[96:99], v[152:155], a[96:99]\n"
      "v_mfma_f32_16x16x32_bf16 a[100:103], v[100:103], v[152:155], a[100:103]\n"
      "v_mfma_f32_16x16x32_bf16 a[104:107], v[104:107], v[152:155], a[104:107]\n"
      "v_mfma_f32_16x16x32_bf16 a[108:111], v[108:111], v[152:155], a[108:111]\n"
      "v_mfma_f32_16x16x32_bf16 a[152:155], v[152:155], v[112:115], a[152:155]\n"
      "v_mfma_f32_16x16x32_bf16 a[184:187], v[152:155], v[116:119], a[184:187]\n"
      "global_load_lds_dwordx4 v230, s[80:81] offset:2048\n"
      "v_mfma_f32_16x16x32_bf16 a[216:219], v[152:155], v[120:123], a[216:219]\n"
      "v_mfma_f32_16x16x32_bf16 a[248:251], v[152:155], v[124:127], a[248:251]\n"
      "ds_read_b128 v[152:155], v240 offset:12288\n"
      "v_mfma_f32_16x16x32_bf16 a[112:115], v[96:99], v[156:159], a[112:115]\n"
      "v_mfma_f32_16x16x32_bf16 a[116:119], v[100:103], v[156:159], a[116:119]\n"
      "v_mfma_f32_16x16x32_bf16 a[120:123], v[104:107], v[156:159], a[120:123]\n"
      "v_mfma_f32_16x16x32_bf16 a[124:127], v[108:111], v[156:159], a[124:127]\n"
      "v_mfma_f32_16x16x32_bf16 a[156:159], v[156:159], v[112:115], a[156:159]\n"
      "global_load_lds_dwordx4 v231, s[80:81] offset:3072\n"
      "v_mfma_f32_16x16x32_bf16 a[188:191], v[156:159], v[116:119], a[188:191]\n"
      "v_mfma_f32_16x16x32_bf16 a[220:223], v[156:159], v[120:123], a[220:223]\n"
      "v_mfma_f32_16x16x32_bf16 a[252:255], v[156:159], v[124:127], a[252:255]\n"
      "ds_read_b128 v[156:159], v240 offset:14336\n"
      "s_waitcnt lgkmcnt(15)\n"
      "v_mfma_f32_16x16x32_bf16 a[0:3], v[160:163], v[128:131], a[0:3]\n"
      "ds_read_b64_tr_b16 v[96:97], v242 offset:8192\n"
      "v_mfma_f32_16x16x32_bf16 a[4:7], v[164:167], v[128:131], a[4:7]\n"
      "v_mfma_f32_16x16x32_bf16 a[8:11], v[168:171], v[128:131], a[8:11]\n"
      "s_waitcnt lgkmcnt(8)\n"
      "v_mfma_f32_16x16x32_bf16 a[12:15], v[172:175], v[128:131], a[12:15]\n"
      "ds_read_b64_tr_b16 v[98:99], v242 offset:8448\n"
      "v_mfma_f32_16x16x32_bf16 a[128:131], v[128:131], v[176:179], a[128:131]\n"
      "s_add_u32 s80, s68, 128\n"
      "s_addc_u32 s81, s69, 0\n"
      "s_add_u32 m0, s82, 65536\n"
      "s_nop 0\n"
      "global_load_lds_dwordx4 v228, s[80:81] offset:0\n"
      "v_mfma_f32_16x16x32_bf16 a[160:163], v[128:131], v[180:183], a[160:163]\n"
      "s_waitcnt lgkmcnt(1)\n"
      "v_mfma_f32_16x16x32_bf16 a[192:195], v[128:131], v[184:187], a[192:195]\n"
      "ds_read_b64_tr_b16 v[100:101], v243 offset:8192\n"
      "v_mfma_f32_16x16x32_bf16 a[224:227], v[128:131], v[188:191], a[224:227]\n"
      "ds_read_b128 v[128:131], v241 offset:0\n"
      "v_mfma_f32_16x16x32_bf16 a[16:19], v[160:163], v[132:135], a[16:19]\n"
      "v_mfma_f32_16x16x32_bf16 a[20:23], v[164:167], v[132:135], a[20:23]\n"
      "ds_read_b64_tr_b16 v[102:103], v243 offset:8448\n"
      "v_mfma_f32_16x16x32_bf16 a[24:27], v[168:171], v[132:135], a[24:27]\n"
      "v_mfma_f32_16x16x32_bf16 a[28:31], v[172:175], v[132:135], a[28:31]\n"
      "v_mfma_f32_16x16x32_bf16 a[132:135], v[132:135], v[176:179], a[132:135]\n"
      "ds_read_b64_tr_b16 v[104:105], v242 offset:8704\n"
      "global_load_lds_dwordx4 v229, s[80:81] offset:1024\n"
      "v_mfma_f32_16x16x32_bf16 a[164:167], v[132:135], v[180:183], a[164:167]\n"
      "v_mfma_f32_16x16x32_bf16 a[196:199], v[132:135], v[184:187], a[196:199]\n"
      "v_mfma_f32_16x16x32_bf16 a[228:231], v[132:135], v[188:191], a[228:231]\n"
      "ds_read_b128 v[132:135], v241 offset:2048\n"
      "ds_read_b64_tr_b16 v[106:107], v242 offset:8960\n"
      "v_mfma_f32_16x16x32_bf16 a[32:35], v[160:163], v[136:139], a[32:35]\n"
      "v_mfma_f32_16x16x32_bf16 a[36:39], v[164:167], v[136:139], a[36:39]\n"
      "v_mfma_f32_16x16x32_bf16 a[40:43], v[168:171], v[136:139], a[40:43]\n"
      "ds_read_b64_tr_b16 v[108:109], v243 offset:8704\n"
      "v_mfma_f32_16x16x32_bf16 a[44:47], v[172:175], v[136:139], a[44:47]\n"
      "v_mfma_f32_16x16x32_bf16 a[136:139], v[136:139], v[176:179], a[136:139]\n"
      "global_load_lds_dwordx4 v230, s[80:81] offset:2048\n"
      "v_mfma_f32_16x16x32_bf16 a[168:171], v[136:139], v[180:183], a[168:171]\n"
      "ds_read_b64_tr_b16 v[110:111], v243 offset:8960\n"
      "v_mfma_f32_16x16x32_bf16 a[200:203], v[136:139], v[184:187], a[200:203]\n"
      "v_mfma_f32_16x16x32_bf16 a[232:235], v[136:139], v[188:191], a[232:235]\n"
      "ds_read_b128 v[136:139], v241 offset:4096\n"
      "v_mfma_f32_16x16x32_bf16 a[48:51], v[160:163], v[140:143], a[48:51]\n"
      "ds_read_b64_tr_b16 v[112:113], v242 offset:24576\n"
      "v_mfma_f32_16x16x32_bf16 a[52:55], v[164:167], v[140:143], a[52:55]\n"
      "v_mfma_f32_16x16x32_bf16 a[56:59], v[168:171], v[140:143], a[56:59]\n"
      "v_mfma_f32_16x16x32_bf16 a[60:63], v[172:175], v[140:143], a[60:63]\n"
      "ds_read_b64_tr_b16 v[114:115], v242 offset:24832\n"
      "v_mfma_f32_16x16x32_bf16 a[140:143], v[140:143], v[176:179], a[140:143]\n"
      "global_load_lds_dwordx4 v231, s[80:81] offset:3072\n"
      "v_mfma_f32_16x16x32_bf16 a[172:175], v[140:143], v[180:183], a[172:175]\n"
      "v_mfma_f32_16x16x32_bf16 a[204:207], v[140:143], v[184:187], a[204:207]\n"
      "ds_read_b64_tr_b16 v[116:117], v243 offset:24576\n"
      "v_mfma_f32_16x16x32_bf16 a[236:239], v[140:143], v[188:191], a[236:239]\n"
      "ds_read_b128 v[140:143], v241 offset:6144\n"
      "v_mfma_f32_16x16x32_bf16 a[64:67], v[160:163], v[144:147], a[64:67]\n"
      "v_mfma_f32_16x16x32_bf16 a[68:71], v[164:167], v[144:147], a[68:71]\n"
      "ds_read_b64_tr_b16 v[118:119], v243 offset:24832\n"
      "v_mfma_f32_16x16x32_bf16 a[72:75], v[168:171], v[144:147], a[72:75]\n"
      "v_mfma_f32_16x16x32_bf16 a[76:79], v[172:175], v[144:147], a[76:79]\n"
      "v_mfma_f32_16x16x32_bf16 a[144:147], v[144:147], v[176:179], a[144:147]\n"
      "ds_read_b64_tr_b16 v[120:121], v242 offset:25088\n"
      "s_add_u32 s80, s70, 524288\n"
      "s_addc_u32 s81, s71, 0\n"
      "s_add_u32 m0, s82, 81920\n"
      "s_nop 0\n"
      "global_load_lds_dwordx4 v232, s[80:81] offset:0\n"
      "v_mfma_f32_16x16x32_bf16 a[176:179], v[144:147], v[180:183], a[176:179]\n"
      "v_mfma_f32_16x16x32_bf16 a[208:211], v[144:147], v[184:187], a[208:211]\n"
      "v_mfma_f32_16x16x32_bf16 a[240:243], v[144:147], v[188:191], a[240:243]\n"
      "ds_read_b128 v[144:147], v241 offset:8192\n"
      "ds_read_b64_tr_b16 v[122:123], v242 offset:25344\n"
      "v_mfma_f32_16x16x32_bf16 a[80:83], v[160:163], v[148:151], a[80:83]\n"
      "v_mfma_f32_16x16x32_bf16 a[84:87], v[164:167], v[148:151], a[84:87]\n"
      "v_mfma_f32_16x16x32_bf16 a[88:91], v[168:171], v[148:151], a[88:91]\n"
      "ds_read_b64_tr_b16 v[124:125], v243 offset:25088\n"
      "v_mfma_f32_16x16x32_bf16 a[92:95], v[172:175], v[148:151], a[92:95]\n"
      "v_mfma_f32_16x16x32_bf16 a[148:151], v[148:151], v[176:179], a[148:151]\n"
      "global_load_lds_dwordx4 v233, s[80:81] offset:1024\n"
      "v_mfma_f32_16x16x32_bf16 a[180:183], v[148:151], v[180:183], a[180:183]\n"
      "ds_read_b64_tr_b16 v[126:127], v243 offset:25344\n"
      "v_mfma_f32_16x16x32_bf16 a[212:215], v[148:151], v[184:187], a[212:215]\n"
      "v_mfma_f32_16x16x32_bf16 a[244:247], v[148:151], v[188:191], a[244:247]\n"
      "ds_read_b128 v[148:151], v241 offset:10240\n"
      "v_mfma_f32_16x16x32_bf16 a[96:99], v[160:163], v[152:155], a[96:99]\n"
      "v_mfma_f32_16x16x32_bf16 a[100:103], v[164:167], v[152:155], a[100:103]\n"
      "v_mfma_f32_16x16x32_bf16 a[104:107], v[168:171], v[152:155], a[104:107]\n"
      "v_mfma_f32_16x16x32_bf16 a[108:111], v[172:175], v[152:155], a[108:111]\n"
      "v_mfma_f32_16x16x32_bf16 a[152:155], v[152:155], v[176:179], a[152:155]\n"
      "global_load_lds_dwordx4 v234, s[80:81] offset:2048\n"
      "v_mfma_f32_16x16x32_bf16 a[184:187], v[152:155], v[180:183], a[184:187]\n"
      "v_mfma_f32_16x16x32_bf16 a[216:219], v[152:155], v[184:187], a[216:219]\n"
      "v_mfma_f32_16x16x32_bf16 a[248:251], v[152:155], v[188:191], a[248:251]\n"
      "ds_read_b128 v[152:155], v241 offset:12288\n"
      "v_mfma_f32_16x16x32_bf16 a[112:115], v[160:163], v[156:159], a[112:115]\n"
      "v_mfma_f32_16x16x32_bf16 a[116:119], v[164:167], v[156:159], a[116:119]\n"
      "v_mfma_f32_16x16x32_bf16 a[120:123], v[168:171], v[156:159], a[120:123]\n"
      "v_mfma_f32_16x16x32_bf16 a[124:127], v[172:175], v[156:159], a[124:127]\n"
      "v_mfma_f32_16x16x32_bf16 a[156:159], v[156:159], v[176:179], a[156:159]\n"
      "global_load_lds_dwordx4 v235, s[80:81] offset:3072\n"
      "v_mfma_f32_16x16x32_bf16 a[188:191], v[156:159], v[180:183], a[188:191]\n"
      "v_mfma_f32_16x16x32_bf16 a[220:223], v[156:159], v[184:187], a[220:223]\n"
      "v_mfma_f32_16x16x32_bf16 a[252:255], v[156:159], v[188:191], a[252:255]\n"
      "ds_read_b128 v[156:159], v241 offset:14336\n"
      "s_waitcnt lgkmcnt(14)\n"
      "v_mfma_f32_16x16x32_bf16 a[0:3], v[96:99], v[128:131], a[0:3]\n"
      "v_mfma_f32_16x16x32_bf16 a[4:7], v[100:103], v[128:131], a[4:7]\n"
      "v_mfma_f32_16x16x32_bf16 a[8:11], v[104:107], v[128:131], a[8:11]\n"
      "v_mfma_f32_16x16x32_bf16 a[12:15], v[108:111], v[128:131], a[12:15]\n"
      "s_waitcnt lgkmcnt(5)\n"
      "v_mfma_f32_16x16x32_bf16 a[128:131], v[128:131], v[112:115], a[128:131]\n"
      "v_mfma_f32_16x16x32_bf16 a[160:163], v[128:131], v[116:119], a[160:163]\n"
      "v_mfma_f32_16x16x32_bf16 a[192:195], v[128:131], v[120:123], a[192:195]\n"
      "s_waitcnt lgkmcnt(0)\n"
      "v_mfma_f32_16x16x32_bf16 a[224:227], v[128:131], v[124:127], a[224:227]\n"
      "s_waitcnt vmcnt(12) lgkmcnt(0)\n"
      "s_barrier\n"
      "v_add_u32 v240, 0, v236\n"
      "v_add_u32 v241, 0, v237\n"
      "v_add_u32 v242, 32768, v238\n"
      "v_add_u32 v243, 32768, v239\n"
      "ds_read_b128 v[128:131], v240 offset:0\n"
      "v_mfma_f32_16x16x32_bf16 a[16:19], v[96:99], v[132:135], a[16:19]\n"
      "ds_read_b64_tr_b16 v[160:161], v242 offset:0\n"
      "v_mfma_f32_16x16x32_bf16 a[20:23], v[100:103], v[132:135], a[20:23]\n"
      "v_mfma_f32_16x16x32_bf16 a[24:27], v[104:107], v[132:135], a[24:27]\n"
      "v_mfma_f32_16x16x32_bf16 a[28:31], v[108:111], v[132:135], a[28:31]\n"
      "s_add_u32 s80, s66, 256\n"
      "s_addc_u32 s81, s67, 0\n"
      "s_add_u32 m0, s82, 98304\n"
      "s_nop 0\n"
      "global_load_lds_dwordx4 v228, s[80:81] offset:0\n"
      "v_mfma_f32_16x16x32_bf16 a[132:135], v[132:135], v[112:115], a[132:135]\n"
      "v_mfma_f32_16x16x32_bf16 a[164:167], v[132:135], v[116:119], a[164:167]\n"
      "ds_read_b64_tr_b16 v[162:163], v242 offset:256\n"
      "v_mfma_f32_16x16x32_bf16 a[196:199], v[132:135], v[120:123], a[196:199]\n"
      "v_mfma_f32_16x16x32_bf16 a[228:231], v[132:135], v[124:127], a[228:231]\n"
      "ds_read_b128 v[132:135], v240 offset:2048\n"
      "v_mfma_f32_16x16x32_bf16 a[32:35], v[96:99], v[136:139], a[32:35]\n"
      "v_mfma_f32_16x16x32_bf16 a[36:39], v[100:103], v[136:139], a[36:39]\n"
      "v_mfma_f32_16x16x32_bf16 a[40:43], v[104:107], v[136:139], a[40:43]\n"
      "ds_read_b64_tr_b16 v[164:165], v243 offset:0\n"
      "global_load_lds_dwordx4 v229, s[80:81] offset:1024\n"
      "v_mfma_f32_16x16x32_bf16 a[44:47], v[108:111], v[136:139], a[44:47]\n"
      "v_mfma_f32_16x16x32_bf16 a[136:139], v[136:139], v[112:115], a[136:139]\n"
      "v_mfma_f32_16x16x32_bf16 a[168:171], v[136:139], v[116:119], a[168:171]\n"
      "v_mfma_f32_16x16x32_bf16 a[200:203], v[136:139], v[120:123], a[200:203]\n"
      "v_mfma_f32_16x16x32_bf16 a[232:235], v[136:139], v[124:127], a[232:235]\n"
      "ds_read_b128 v[136:139], v240 offset:4096\n"
      "ds_read_b64_tr_b16 v[166:167], v243 offset:256\n"
      "v_mfma_f32_16x16x32_bf16 a[48:51], v[96:99], v[140:143], a[48:51]\n"
      "v_mfma_f32_16x16x32_bf16 a[52:55], v[100:103], v[140:143], a[52:55]\n"
      "global_load_lds_dwordx4 v230, s[80:81] offset:2048\n"
      "v_mfma_f32_16x16x32_bf16 a[56:59], v[104:107], v[140:143], a[56:59]\n"
      "v_mfma_f32_16x16x32_bf16 a[60:63], v[108:111], v[140:143], a[60:63]\n"
      "v_mfma_f32_16x16x32_bf16 a[140:143], v[140:143], v[112:115], a[140:143]\n"
      "v_mfma_f32_16x16x32_bf16 a[172:175], v[140:143], v[116:119], a[172:175]\n"
      "ds_read_b64_tr_b16 v[168:169], v242 offset:512\n"
      "v_mfma_f32_16x16x32_bf16 a[204:207], v[140:143], v[120:123], a[204:207]\n"
      "v_mfma_f32_16x16x32_bf16 a[236:239], v[140:143], v[124:127], a[236:239]\n"
      "ds_read_b128 v[140:143], v240 offset:6144\n"
      "v_mfma_f32_16x16x32_bf16 a[64:67], v[96:99], v[144:147], a[64:67]\n"
      "global_load_lds_dwordx4 v231, s[80:81] offset:3072\n"
      "v_mfma_f32_16x16x32_bf16 a[68:71], v[100:103], v[144:147], a[68:71]\n"
      "v_mfma_f32_16x16x32_bf16 a[72:75], v[104:107], v[144:147], a[72:75]\n"
      "ds_read_b64_tr_b16 v[170:171], v242 offset:768\n"
      "v_mfma_f32_16x16x32_bf16 a[76:79], v[108:111], v[144:147], a[76:79]\n"
      "v_mfma_f32_16x16x32_bf16 a[144:147], v[144:147], v[112:115], a[144:147]\n"
      "v_mfma_f32_16x16x32_bf16 a[176:179], v[144:147], v[116:119], a[176:179]\n"
      "v_mfma_f32_16x16x32_bf16 a[208:211], v[144:147], v[120:123], a[208:211]\n"
      "v_mfma_f32_16x16x32_bf16 a[240:243], v[144:147], v[124:127], a[240:243]\n"
      "ds_read_b128 v[144:147], v240 offset:8192\n"
      "ds_read_b64_tr_b16 v[172:173], v243 offset:512\n"
      "s_add_u32 s80, s68, 256\n"
      "s_addc_u32 s81, s69, 0\n"
      "s_add_u32 m0, s82, 114688\n"
      "s_nop 0\n"
      "global_load_lds_dwordx4 v228, s[80:81] offset:0\n"
      "v_mfma_f32_16x16x32_bf16 a[80:83], v[96:99], v[148:151], a[80:83]\n"
      "v_mfma_f32_16x16x32_bf16 a[84:87], v[100:103], v[148:151], a[84:87]\n"
      "v_mfma_f32_16x16x32_bf16 a[88:91], v[104:107], v[148:151], a[88:91]\n"
      "v_mfma_f32_16x16x32_bf16 a[92:95], v[108:111], v[148:151], a[92:95]\n"
      "v_mfma_f32_16x16x32_bf16 a[148:151], v[148:151], v[112:115], a[148:151]\n"
      "ds_read_b64_tr_b16 v[174:175], v243 offset:768\n"
      "v_mfma_f32_16x16x32_bf16 a[180:183], v[148:151], v[116:119], a[180:183]\n"
      "v_mfma_f32_16x16x32_bf16 a[212:215], v[148:151], v[120:123], a[212:215]\n"
      "global_load_lds_dwordx4 v229, s[80:81] offset:1024\n"
      "v_mfma_f32_16x16x32_bf16 a[244:247], v[148:151], v[124:127], a[244:247]\n"
      "ds_read_b128 v[148:151], v240 offset:10240\n"
      "v_mfma_f32_16x16x32_bf16 a[96:99], v[96:99], v[152:155], a[96:99]\n"
      "v_mfma_f32_16x16x32_bf16 a[100:103], v[100:103], v[152:155], a[100:103]\n"
      "v_mfma_f32_16x16x32_bf16 a[104:107], v[104:107], v[152:155], a[104:107]\n"
      "v_mfma_f32_16x16x32_bf16 a[108:111], v[108:111], v[152:155], a[108:111]\n"
      "v_mfma_f32_16x16x32_bf16 a[152:155], v[152:155], v[112:115], a[152:155]\n"
      "v_mfma_f32_16x16x32_bf16 a[184:187], v[152:155], v[116:119], a[184:187]\n"
      "global_load_lds_dwordx4 v230, s[80:81] offset:2048\n"
      "v_mfma_f32_16x16x32_bf16 a[216:219], v[152:155], v[120:123], a[216:219]\n"
      "v_mfma_f32_16x16x32_bf16 a[248:251], v[152:155], v[124:127], a[248:251]\n"
      "ds_read_b128 v[152:155], v240 offset:12288\n"
      "v_mfma_f32_16x16x32_bf16 a[112:115], v[96:99], v[156:159], a[112:115]\n"
      "v_mfma_f32_16x16x32_bf16 a[116:119], v[100:103], v[156:159], a[116:119]\n"
      "v_mfma_f32_16x16x32_bf16 a[120:123], v[104:107], v[156:159], a[120:123]\n"
      "v_mfma_f32_16x16x32_bf16 a[124:127], v[108:111], v[156:159], a[124:127]\n"
      "v_mfma_f32_16x16x32_bf16 a[156:159], v[156:159], v[112:115], a[156:159]\n"
      "global_load_lds_dwordx4 v231, s[80:81] offset:3072\n"
      "v_mfma_f32_16x16x32_bf16 a[188:191], v[156:159], v[116:119], a[188:191]\n"
      "v_mfma_f32_16x16x32_bf16 a[220:223], v[156:159], v[120:123], a[220:223]\n"
      "v_mfma_f32_16x16x32_bf16 a[252:255], v[156:159], v[124:127], a[252:255]\n"
      "ds_read_b128 v[156:159], v240 offset:14336\n"
      "s_waitcnt lgkmcnt(7)\n"
      "v_mfma_f32_16x16x32_bf16 v[0:3], v[160:163], v[128:131], 0\n"
      "ds_read_b64_tr_b16 v[176:177], v242 offset:8192\n"
      "v_mfma_f32_16x16x32_bf16 v[4:7], v[164:167], v[128:131], 0\n"
      "s_waitcnt lgkmcnt(1)\n"
      "v_mfma_f32_16x16x32_bf16 v[8:11], v[168:171], v[128:131], 0\n"
      "s_add_u32 s80, s70, 1048576\n"
      "s_addc_u32 s81, s71, 0\n"
      "s_add_u32 m0, s82, 131072\n"
      "s_nop 0\n"
      "global_load_lds_dwordx4 v232, s[80:81] offset:0\n"
      "v_mfma_f32_16x16x32_bf16 v[12:15], v[172:175], v[128:131], 0\n"
      "ds_read_b128 v[128:131], v241 offset:0\n"
      "ds_read_b64_tr_b16 v[178:179], v242 offset:8448\n"
      "v_mfma_f32_16x16x32_bf16 v[16:19], v[160:163], v[132:135], 0\n"
      "v_mfma_f32_16x16x32_bf16 v[20:23], v[164:167], v[132:135], 0\n"
      "v_mfma_f32_16x16x32_bf16 v[24:27], v[168:171], v[132:135], 0\n"
      "ds_read_b64_tr_b16 v[180:181], v243 offset:8192\n"
      "global_load_lds_dwordx4 v233, s[80:81] offset:1024\n"
      "v_mfma_f32_16x16x32_bf16 v[28:31], v[172:175], v[132:135], 0\n"
      "v_mfma_f32_16x16x32_bf16 v[208:211], v[160:163], v[250:253], 0\n"
      "ds_read_b128 v[132:135], v241 offset:2048\n"
      "v_mfma_f32_16x16x32_bf16 v[32:35], v[160:163], v[136:139], 0\n"
      "v_mfma_f32_16x16x32_bf16 v[36:39], v[164:167], v[136:139], 0\n"
      "ds_read_b64_tr_b16 v[182:183], v243 offset:8448\n"
      "v_mfma_f32_16x16x32_bf16 v[40:43], v[168:171], v[136:139], 0\n"
      "global_load_lds_dwordx4 v234, s[80:81] offset:2048\n"
      "v_mfma_f32_16x16x32_bf16 v[44:47], v[172:175], v[136:139], 0\n"
      "ds_read_b128 v[136:139], v241 offset:4096\n"
      "v_mfma_f32_16x16x32_bf16 v[48:51], v[160:163], v[140:143], 0\n"
      "ds_read_b64_tr_b16 v[184:185], v242 offset:8704\n"
      "v_mfma_f32_16x16x32_bf16 v[52:55], v[164:167], v[140:143], 0\n"
      "v_mfma_f32_16x16x32_bf16 v[56:59], v[168:171], v[140:143], 0\n"
      "global_load_lds_dwordx4 v235, s[80:81] offset:3072\n"
      "v_mfma_f32_16x16x32_bf16 v[60:63], v[172:175], v[140:143], 0\n"
      "ds_read_b64_tr_b16 v[186:187], v242 offset:8960\n"
      "v_mfma_f32_16x16x32_bf16 v[212:215], v[164:167], v[250:253], 0\n"
      "ds_read_b128 v[140:143], v241 offset:6144\n"
      "v_mfma_f32_16x16x32_bf16 v[64:67], v[160:163], v[144:147], 0\n"
      "v_mfma_f32_16x16x32_bf16 v[68:71], v[164:167], v[144:147], 0\n"
      "v_mfma_f32_16x16x32_bf16 v[72:75], v[168:171], v[144:147], 0\n"
      "ds_read_b64_tr_b16 v[188:189], v243 offset:8704\n"
      "s_add_u32 s80, s70, 1572864\n"
      "s_addc_u32 s81, s71, 0\n"
      "s_add_u32 m0, s82, 147456\n"
      "s_nop 0\n"
      "global_load_lds_dwordx4 v232, s[80:81] offset:0\n"
      "v_mfma_f32_16x16x32_bf16 v[76:79], v[172:175], v[144:147], 0\n"
      "ds_read_b128 v[144:147], v241 offset:8192\n"
      "v_mfma_f32_16x16x32_bf16 v[80:83], v[160:163], v[148:151], 0\n"
      "v_mfma_f32_16x16x32_bf16 v[84:87], v[164:167], v[148:151], 0\n"
      "ds_read_b64_tr_b16 v[190:191], v243 offset:8960\n"
      "v_mfma_f32_16x16x32_bf16 v[88:91], v[168:171], v[148:151], 0\n"
      "global_load_lds_dwordx4 v233, s[80:81] offset:1024\n"
      "v_mfma_f32_16x16x32_bf16 v[92:95], v[172:175], v[148:151], 0\n"
      "v_mfma_f32_16x16x32_bf16 v[216:219], v[168:171], v[250:253], 0\n"
      "ds_read_b128 v[148:151], v241 offset:10240\n"
      "v_mfma_f32_16x16x32_bf16 v[96:99], v[160:163], v[152:155], 0\n"
      "v_mfma_f32_16x16x32_bf16 v[100:103], v[164:167], v[152:155], 0\n"
      "v_mfma_f32_16x16x32_bf16 v[104:107], v[168:171], v[152:155], 0\n"
      "global_load_lds_dwordx4 v234, s[80:81] offset:2048\n"
      "v_mfma_f32_16x16x32_bf16 v[108:111], v[172:175], v[152:155], 0\n"
      "ds_read_b128 v[152:155], v241 offset:12288\n"
      "v_mfma_f32_16x16x32_bf16 v[112:115], v[160:163], v[156:159], 0\n"
      "v_mfma_f32_16x16x32_bf16 v[116:119], v[164:167], v[156:159], 0\n"
      "v_mfma_f32_16x16x32_bf16 v[120:123], v[168:171], v[156:159], 0\n"
      "global_load_lds_dwordx4 v235, s[80:81] offset:3072\n"
      "v_mfma_f32_16x16x32_bf16 v[124:127], v[172:175], v[156:159], 0\n"
      "v_mfma_f32_16x16x32_bf16 v[220:223], v[172:175], v[250:253], 0\n"
      "ds_read_b128 v[156:159], v241 offset:14336\n"
      "s_waitcnt lgkmcnt(7)\n"
      "v_mfma_f32_16x16x32_bf16 v[0:3], v[176:179], v[128:131], v[0:3]\n"
      "v_mfma_f32_16x16x32_bf16 v[4:7], v[180:183], v[128:131], v[4:7]\n"
      "v_mfma_f32_16x16x32_bf16 v[8:11], v[184:187], v[128:131], v[8:11]\n"
      "s_waitcnt lgkmcnt(0)\n"
      "v_mfma_f32_16x16x32_bf16 v[12:15], v[188:191], v[128:131], v[12:15]\n"
      "s_waitcnt vmcnt(16) lgkmcnt(0)\n"
      "s_barrier\n"
      "v_add_u32 v240, 49152, v236\n"
      "v_add_u32 v241, 49152, v237\n"
      "v_add_u32 v242, 81920, v238\n"
      "v_add_u32 v243, 81920, v239\n"
      "ds_read_b128 v[128:131], v240 offset:0\n"
      "v_mfma_f32_16x16x32_bf16 v[16:19], v[176:179], v[132:135], v[16:19]\n"
      "ds_read_b64_tr_b16 v[160:161], v242 offset:0\n"
      "v_mfma_f32_16x16x32_bf16 v[20:23], v[180:183], v[132:135], v[20:23]\n"
      "s_add_u32 s84, s72, 0\n s_addc_u32 s85, s73, 0\n"
      "v_mfma_f32_16x16x32_bf16 v[24:27], v[184:187], v[132:135], v[24:27]\n"
      "s_add_u32 s80, s66, 384\n"
      "s_addc_u32 s81, s67, 0\n"
      "s_add_u32 m0, s82, 0\n"
      "s_nop 0\n"
      "global_load_lds_dwordx4 v228, s[80:81] offset:0\n"
      "v_mfma_f32_16x16x32_bf16 v[28:31], v[188:191], v[132:135], v[28:31]\n"
      "ds_read_b64_tr_b16 v[162:163], v242 offset:256\n"
      "v_accvgpr_read_b32 v192, a0\n"
      "v_mfma_f32_16x16x32_bf16 v[208:211], v[176:179], v[250:253], v[208:211]\n"
      "ds_read_b128 v[132:135], v240 offset:2048\n"
      "v_accvgpr_read_b32 v193, a1\n"
      "v_mfma_f32_16x16x32_bf16 v[32:35], v[176:179], v[136:139], v[32:35]\n"
      "v_accvgpr_read_b32 v194, a2\n"
      "v_accvgpr_read_b32 v195, a3\n"
      "v_mfma_f32_16x16x32_bf16 v[36:39], v[180:183], v[136:139], v[36:39]\n"
      "ds_read_b64_tr_b16 v[164:165], v243 offset:0\n"
      "v_accvgpr_read_b32 v196, a4\n"
      "v_mfma_f32_16x16x32_bf16 v[40:43], v[184:187], v[136:139], v[40:43]\n"
      "v_accvgpr_read_b32 v197, a5\n"
      "v_accvgpr_read_b32 v198, a6\n"
      "v_mfma_f32_16x16x32_bf16 v[44:47], v[188:191], v[136:139], v[44:47]\n"
      "ds_read_b128 v[136:139], v240 offset:4096\n"
      "global_load_lds_dwordx4 v229, s[80:81] offset:1024\n"
      "v_mfma_f32_16x16x32_bf16 v[48:51], v[176:179], v[140:143], v[48:51]\n"
      "ds_read_b64_tr_b16 v[166:167], v243 offset:256\n"
      "v_accvgpr_read_b32 v199, a7\n"
      "v_mfma_f32_16x16x32_bf16 v[52:55], v[180:183], v[140:143], v[52:55]\n"
      "v_cvt_pk_bf16_f32 v192, v192, v193\n"
      "v_cvt_pk_bf16_f32 v193, v194, v195\n"
      "v_mfma_f32_16x16x32_bf16 v[56:59], v[184:187], v[140:143], v[56:59]\n"
      "v_cvt_pk_bf16_f32 v194, v196, v197\n"
      "v_cvt_pk_bf16_f32 v195, v198, v199\n"
      "v_mfma_f32_16x16x32_bf16 v[60:63], v[188:191], v[140:143], v[60:63]\n"
      "ds_read_b64_tr_b16 v[168:169], v242 offset:512\n"
      "v_mfma_f32_16x16x32_bf16 v[212:215], v[180:183], v[250:253], v[212:215]\n"
      "ds_read_b128 v[140:143], v240 offset:6144\n"
      "global_load_lds_dwordx4 v230, s[80:81] offset:2048\n"
      "v_mfma_f32_16x16x32_bf16 v[64:67], v[176:179], v[144:147], v[64:67]\n"
      "global_store_dwordx4 v246, v[192:195], s[84:85] offset:0 sc1\n"
      "v_mfma_f32_16x16x32_bf16 v[68:71], v[180:183], v[144:147], v[68:71]\n"
      "ds_read_b64_tr_b16 v[170:171], v242 offset:768\n"
      "v_accvgpr_write_b32 a0, v192\n"
      "v_mfma_f32_16x16x32_bf16 v[72:75], v[184:187], v[144:147], v[72:75]\n"
      "v_accvgpr_write_b32 a1, v193\n"
      "v_accvgpr_write_b32 a2, v194\n"
      "v_mfma_f32_16x16x32_bf16 v[76:79], v[188:191], v[144:147], v[76:79]\n"
      "ds_read_b128 v[144:147], v240 offset:8192\n"
      "v_accvgpr_write_b32 a3, v195\n"
      "v_mfma_f32_16x16x32_bf16 v[80:83], v[176:179], v[148:151], v[80:83]\n"
      "ds_read_b64_tr_b16 v[172:173], v243 offset:512\n"
      "global_load_lds_dwordx4 v231, s[80:81] offset:3072\n"
      "v_mfma_f32_16x16x32_bf16 v[84:87], v[180:183], v[148:151], v[84:87]\n"
      "v_accvgpr_read_b32 v200, a8\n"
      "v_accvgpr_read_b32 v201, a9\n"
      "v_mfma_f32_16x16x32_bf16 v[88:91], v[184:187], v[148:151], v[88:91]\n"
      "v_accvgpr_read_b32 v202, a10\n"
      "v_accvgpr_read_b32 v203, a11\n"
      "v_mfma_f32_16x16x32_bf16 v[92:95], v[188:191], v[148:151], v[92:95]\n"
      "ds_read_b64_tr_b16 v[174:175], v243 offset:768\n"
      "v_accvgpr_read_b32 v204, a12\n"
      "v_mfma_f32_16x16x32_bf16 v[216:219], v[184:187], v[250:253], v[216:219]\n"
      "ds_read_b128 v[148:151], v240 offset:10240\n"
      "v_accvgpr_read_b32 v205, a13\n"
      "v_mfma_f32_16x16x32_bf16 v[96:99], v[176:179], v[152:155], v[96:99]\n"
      "v_accvgpr_read_b32 v206, a14\n"
      "v_accvgpr_read_b32 v207, a15\n"
      "v_mfma_f32_16x16x32_bf16 v[100:103], v[180:183], v[152:155], v[100:103]\n"
      "s_add_u32 s80, s68, 384\n"
      "s_addc_u32 s81, s69, 0\n"
      "s_add_u32 m0, s82, 16384\n"
      "s_nop 0\n"
      "global_load_lds_dwordx4 v228, s[80:81] offset:0\n"
      "v_mfma_f32_16x16x32_bf16 v[104:107], v[184:187], v[152:155], v[104:107]\n"
      "v_cvt_pk_bf16_f32 v200, v200, v201\n"
      "v_mfma_f32_16x16x32_bf16 v[108:111], v[188:191], v[152:155], v[108:111]\n"
      "ds_read_b128 v[152:155], v240 offset:12288\n"
      "v_cvt_pk_bf16_f32 v201, v202, v203\n"
      "v_mfma_f32_16x16x32_bf16 v[112:115], v[176:179], v[156:159], v[112:115]\n"
      "v_cvt_pk_bf16_f32 v202, v204, v205\n"
      "v_mfma_f32_16x16x32_bf16 v[116:119], v[180:183], v[156:159], v[116:119]\n"
      "v_cvt_pk_bf16_f32 v203, v206, v207\n"
      "v_mfma_f32_16x16x32_bf16 v[120:123], v[184:187], v[156:159], v[120:123]\n"
      "global_load_lds_dwordx4 v229, s[80:81] offset:1024\n"
      "v_mfma_f32_16x16x32_bf16 v[124:127], v[188:191], v[156:159], v[124:127]\n"
      "global_store_dwordx4 v246, v[200:203], s[84:85] offset:1024 sc1\n"
      "v_accvgpr_write_b32 a8, v200\n"
      "v_mfma_f32_16x16x32_bf16 v[220:223], v[188:191], v[250:253], v[220:223]\n"
      "ds_read_b128 v[156:159], v240 offset:14336\n"
      "v_accvgpr_write_b32 a9, v201\n"
      "s_waitcnt lgkmcnt(7)\n"
      "v_mfma_f32_16x16x32_bf16 v[0:3], v[160:163], v[128:131], v[0:3]\n"
      "ds_read_b64_tr_b16 v[176:177], v242 offset:8192\n"
      "v_accvgpr_write_b32 a10, v202\n"
      "v_mfma_f32_16x16x32_bf16 v[4:7], v[164:167], v[128:131], v[4:7]\n"
      "v_accvgpr_write_b32 a11, v203\n"
      "s_waitcnt lgkmcnt(1)\n"
      "v_mfma_f32_16x16x32_bf16 v[8:11], v[168:171], v[128:131], v[8:11]\n"
      "s_add_u32 s84, s72, 4096\n s_addc_u32 s85, s73, 0\n"
      "v_mfma_f32_16x16x32_bf16 v[12:15], v[172:175], v[128:131], v[12:15]\n"
      "ds_read_b128 v[128:131], v241 offset:0\n"
      "ds_read_b64_tr_b16 v[178:179], v242 offset:8448\n"
      "global_load_lds_dwordx4 v230, s[80:81] offset:2048\n"
      "v_mfma_f32_16x16x32_bf16 v[16:19], v[160:163], v[132:135], v[16:19]\n"
      "v_accvgpr_read_b32 v192, a16\n"
      "v_accvgpr_read_b32 v193, a17\n"
      "v_mfma_f32_16x16x32_bf16 v[20:23], v[164:167], v[132:135], v[20:23]\n"
      "v_accvgpr_read_b32 v194, a18\n"
      "v_accvgpr_read_b32 v195, a19\n"
      "v_mfma_f32_16x16x32_bf16 v[24:27], v[168:171], v[132:135], v[24:27]\n"
      "ds_read_b64_tr_b16 v[180:181], v243 offset:8192\n"
      "v_accvgpr_read_b32 v196, a20\n"
      "v_mfma_f32_16x16x32_bf16 v[28:31], v[172:175], v[132:135], v[28:31]\n"
      "v_accvgpr_read_b32 v197, a21\n"
      "v_accvgpr_read_b32 v198, a22\n"
      "v_mfma_f32_16x16x32_bf16 v[208:211], v[160:163], v[250:253], v[208:211]\n"
      "ds_read_b128 v[132:135], v241 offset:2048\n"
      "v_accvgpr_read_b32 v199, a23\n"
      "v_mfma_f32_16x16x32_bf16 v[32:35], v[160:163], v[136:139], v[32:35]\n"
      "global_load_lds_dwordx4 v231, s[80:81] offset:3072\n"
      "v_mfma_f32_16x16x32_bf16 v[36:39], v[164:167], v[136:139], v[36:39]\n"
      "ds_read_b64_tr_b16 v[182:183], v243 offset:8448\n"
      "v_cvt_pk_bf16_f32 v192, v192, v193\n"
      "v_mfma_f32_16x16x32_bf16 v[40:43], v[168:171], v[136:139], v[40:43]\n"
      "v_cvt_pk_bf16_f32 v193, v194, v195\n"
      "v_mfma_f32_16x16x32_bf16 v[44:47], v[172:175], v[136:139], v[44:47]\n"
      "ds_read_b128 v[136:139], v241 offset:4096\n"
      "v_cvt_pk_bf16_f32 v194, v196, v197\n"
      "v_mfma_f32_16x16x32_bf16 v[48:51], v[160:163], v[140:143], v[48:51]\n"
      "ds_read_b64_tr_b16 v[184:185], v242 offset:8704\n"
      "v_cvt_pk_bf16_f32 v195, v198, v199\n"
      "v_mfma_f32_16x16x32_bf16 v[52:55], v[164:167], v[140:143], v[52:55]\n"
      "global_store_dwordx4 v246, v[192:195], s[84:85] offset:0 sc1\n"
      "v_mfma_f32_16x16x32_bf16 v[56:59], v[168:171], v[140:143], v[56:59]\n"
      "s_add_u32 s80, s66, 512\n"
      "s_addc_u32 s81, s67, 0\n"
      "s_add_u32 m0, s82, 32768\n"
      "s_nop 0\n"
      "global_load_lds_dwordx4 v228, s[80:81] offset:0\n"
      "v_mfma_f32_16x16x32_bf16 v[60:63], v[172:175], v[140:143], v[60:63]\n"
      "ds_read_b64_tr_b16 v[186:187], v242 offset:8960\n"
      "v_accvgpr_write_b32 a16, v192\n"
      "v_mfma_f32_16x16x32_bf16 v[212:215], v[164:167], v[250:253], v[212:215]\n"
      "ds_read_b128 v[140:143], v241 offset:6144\n"
      "v_accvgpr_write_b32 a17, v193\n"
      "v_mfma_f32_16x16x32_bf16 v[64:67], v[160:163], v[144:147], v[64:67]\n"
      "v_accvgpr_write_b32 a18, v194\n"
      "v_mfma_f32_16x16x32_bf16 v[68:71], v[164:167], v[144:147], v[68:71]\n"
      "v_accvgpr_write_b32 a19, v195\n"
      "v_mfma_f32_16x16x32_bf16 v[72:75], v[168:171], v[144:147], v[72:75]\n"
      "ds_read_b64_tr_b16 v[188:189], v243 offset:8704\n"
      "v_accvgpr_read_b32 v200, a24\n"
      "v_mfma_f32_16x16x32_bf16 v[76:79], v[172:175], v[144:147], v[76:79]\n"
      "ds_read_b128 v[144:147], v241 offset:8192\n"
      "global_load_lds_dwordx4 v229, s[80:81] offset:1024\n"
      "v_mfma_f32_16x16x32_bf16 v[80:83], v[160:163], v[148:151], v[80:83]\n"
      "v_accvgpr_read_b32 v201, a25\n"
      "v_mfma_f32_16x16x32_bf16 v[84:87], v[164:167], v[148:151], v[84:87]\n"
      "ds_read_b64_tr_b16 v[190:191], v243 offset:8960\n"
      "v_accvgpr_read_b32 v202, a26\n"
      "v_mfma_f32_16x16x32_bf16 v[88:91], v[168:171], v[148:151], v[88:91]\n"
      "v_accvgpr_read_b32 v203, a27\n"
      "v_mfma_f32_16x16x32_bf16 v[92:95], v[172:175], v[148:151], v[92:95]\n"
      "v_accvgpr_read_b32 v204, a28\n"
      "v_mfma_f32_16x16x32_bf16 v[216:219], v[168:171], v[250:253], v[216:219]\n"
      "ds_read_b128 v[148:151], v241 offset:10240\n"
      "v_accvgpr_read_b32 v205, a29\n"
      "v_mfma_f32_16x16x32_bf16 v[96:99], v[160:163], v[152:155], v[96:99]\n"
      "global_load_lds_dwordx4 v230, s[80:81] offset:2048\n"
      "v_mfma_f32_16x16x32_bf16 v[100:103], v[164:167], v[152:155], v[100:103]\n"
      "v_accvgpr_read_b32 v206, a30\n"
      "v_accvgpr_read_b32 v207, a31\n"
      "v_mfma_f32_16x16x32_bf16 v[104:107], v[168:171], v[152:155], v[104:107]\n"
      "v_cvt_pk_bf16_f32 v200, v200, v201\n"
      "v_mfma_f32_16x16x32_bf16 v[108:111], v[172:175], v[152:155], v[108:111]\n"
      "ds_read_b128 v[152:155], v241 offset:12288\n"
      "v_cvt_pk_bf16_f32 v201, v202, v203\n"
      "v_mfma_f32_16x16x32_bf16 v[112:115], v[160:163], v[156:159], v[112:115]\n"
      "v_cvt_pk_bf16_f32 v202, v204, v205\n"
      "v_mfma_f32_16x16x32_bf16 v[116:119], v[164:167], v[156:159], v[116:119]\n"
      "v_cvt_pk_bf16_f32 v203, v206, v207\n"
      "v_mfma_f32_16x16x32_bf16 v[120:123], v[168:171], v[156:159], v[120:123]\n"
      "global_load_lds_dwordx4 v231, s[80:81] offset:3072\n"
      "v_mfma_f32_16x16x32_bf16 v[124:127], v[172:175], v[156:159], v[124:127]\n"
      "global_store_dwordx4 v246, v[200:203], s[84:85] offset:1024 sc1\n"
      "v_mfma_f32_16x16x32_bf16 v[220:223], v[172:175], v[250:253], v[220:223]\n"
      "ds_read_b128 v[156:159], v241 offset:14336\n"
      "s_waitcnt lgkmcnt(7)\n"
      "v_mfma_f32_16x16x32_bf16 v[0:3], v[176:179], v[128:131], v[0:3]\n"
      "v_mfma_f32_16x16x32_bf16 v[4:7], v[180:183], v[128:131], v[4:7]\n"
      "v_mfma_f32_16x16x32_bf16 v[8:11], v[184:187], v[128:131], v[8:11]\n"
      "s_waitcnt lgkmcnt(0)\n"
      "v_mfma_f32_16x16x32_bf16 v[12:15], v[188:191], v[128:131], v[12:15]\n"
      "s_waitcnt vmcnt(20) lgkmcnt(0)\n"
      "s_barrier\n"
      "v_add_u32 v240, 98304, v236\n"
      "v_add_u32 v241, 98304, v237\n"
      "v_add_u32 v242, 131072, v238\n"
      "v_add_u32 v243, 131072, v239\n"
      "ds_read_b128 v[128:131], v240 offset:0\n"
      "v_mfma_f32_16x16x32_bf16 v[16:19], v[176:179], v[132:135], v[16:19]\n"
      "ds_read_b64_tr_b16 v[160:161], v242 offset:0\n"
      "v_accvgpr_write_b32 a24, v200\n"
      "v_mfma_f32_16x16x32_bf16 v[20:23], v[180:183], v[132:135], v[20:23]\n"
      "v_accvgpr_write_b32 a25, v201\n"
      "v_accvgpr_write_b32 a26, v202\n"
      "v_mfma_f32_16x16x32_bf16 v[24:27], v[184:187], v[132:135], v[24:27]\n"
      "s_add_u32 s80, s68, 512\n"
      "s_addc_u32 s81, s69, 0\n"
      "s_add_u32 m0, s82, 49152\n"
      "s_nop 0\n"
      "global_load_lds_dwordx4 v228, s[80:81] offset:0\n"
      "v_mfma_f32_16x16x32_bf16 v[28:31], v[188:191], v[132:135], v[28:31]\n"
      "ds_read_b64_tr_b16 v[162:163], v242 offset:256\n"
      "v_accvgpr_write_b32 a27, v203\n"
      "v_mfma_f32_16x16x32_bf16 v[208:211], v[176:179], v[250:253], v[208:211]\n"
      "ds_read_b128 v[132:135], v240 offset:2048\n"
      "v_mfma_f32_16x16x32_bf16 v[32:35], v[176:179], v[136:139], v[32:35]\n"
      "s_add_u32 s84, s72, 8192\n s_addc_u32 s85, s73, 0\n"
      "v_mfma_f32_16x16x32_bf16 v[36:39], v[180:183], v[136:139], v[36:39]\n"
      "ds_read_b64_tr_b16 v[164:165], v243 offset:0\n"
      "v_accvgpr_read_b32 v192, a32\n"
      "v_mfma_f32_16x16x32_bf16 v[40:43], v[184:187], v[136:139], v[40:43]\n"
      "v_accvgpr_read_b32 v193, a33\n"
      "v_accvgpr_read_b32 v194, a34\n"
      "v_mfma_f32_16x16x32_bf16 v[44:47], v[188:191], v[136:139], v[44:47]\n"
      "ds_read_b128 v[136:139], v240 offset:4096\n"
      "global_load_lds_dwordx4 v229, s[80:81] offset:1024\n"
      "v_mfma_f32_16x16x32_bf16 v[48:51], v[176:179], v[140:143], v[48:51]\n"
      "ds_read_b64_tr_b16 v[166:167], v243 offset:256\n"
      "v_accvgpr_read_b32 v195, a35\n"
      "v_mfma_f32_16x16x32_bf16 v[52:55], v[180:183], v[140:143], v[52:55]\n"
      "v_accvgpr_read_b32 v196, a36\n"
      "v_accvgpr_read_b32 v197, a37\n"
      "v_mfma_f32_16x16x32_bf16 v[56:59], v[184:187], v[140:143], v[56:59]\n"
      "v_accvgpr_read_b32 v198, a38\n"
      "v_accvgpr_read_b32 v199, a39\n"
      "v_mfma_f32_16x16x32_bf16 v[60:63], v[188:191], v[140:143], v[60:63]\n"
      "ds_read_b64_tr_b16 v[168:169], v242 offset:512\n"
      "v_cvt_pk_bf16_f32 v192, v192, v193\n"
      "v_mfma_f32_16x16x32_bf16 v[212:215], v[180:183], v[250:253], v[212:215]\n"
      "ds_read_b128 v[140:143], v240 offset:6144\n"
      "global_load_lds_dwordx4 v230, s[80:81] offset:2048\n"
      "v_mfma_f32_16x16x32_bf16 v[64:67], v[176:179], v[144:147], v[64:67]\n"
      "v_cvt_pk_bf16_f32 v193, v194, v195\n"
      "v_cvt_pk_bf16_f32 v194, v196, v197\n"
      "v_mfma_f32_16x16x32_bf16 v[68:71], v[180:183], v[144:147], v[68:71]\n"
      "ds_read_b64_tr_b16 v[170:171], v242 offset:768\n"
      "v_cvt_pk_bf16_f32 v195, v198, v199\n"
      "v_mfma_f32_16x16x32_bf16 v[72:75], v[184:187], v[144:147], v[72:75]\n"
      "global_store_dwordx4 v246, v[192:195], s[84:85] offset:0 sc1\n"
      "v_mfma_f32_16x16x32_bf16 v[76:79], v[188:191], v[144:147], v[76:79]\n"
      "ds_read_b128 v[144:147], v240 offset:8192\n"
      "v_accvgpr_write_b32 a32, v192\n"
      "v_mfma_f32_16x16x32_bf16 v[80:83], v[176:179], v[148:151], v[80:83]\n"
      "ds_read_b64_tr_b16 v[172:173], v243 offset:512\n"
      "global_load_lds_dwordx4 v231, s[80:81] offset:3072\n"
      "v_mfma_f32_16x16x32_bf16 v[84:87], v[180:183], v[148:151], v[84:87]\n"
      "v_accvgpr_write_b32 a33, v193\n"
      "v_accvgpr_write_b32 a34, v194\n"
      "v_mfma_f32_16x16x32_bf16 v[88:91], v[184:187], v[148:151], v[88:91]\n"
      "v_accvgpr_write_b32 a35, v195\n"
      "v_accvgpr_read_b32 v200, a40\n"
      "v_mfma_f32_16x16x32_bf16 v[92:95], v[188:191], v[148:151], v[92:95]\n"
      "ds_read_b64_tr_b16 v[174:175], v243 offset:768\n"
      "v_accvgpr_read_b32 v201, a41\n"
      "v_mfma_f32_16x16x32_bf16 v[216:219], v[184:187], v[250:253], v[216:219]\n"
      "ds_read_b128 v[148:151], v240 offset:10240\n"
      "v_accvgpr_read_b32 v202, a42\n"
      "v_mfma_f32_16x16x32_bf16 v[96:99], v[176:179], v[152:155], v[96:99]\n"
      "v_accvgpr_read_b32 v203, a43\n"
      "v_accvgpr_read_b32 v204, a44\n"
      "v_mfma_f32_16x16x32_bf16 v[100:103], v[180:183], v[152:155], v[100:103]\n"
      "s_add_u32 s80, s70, 2097152\n"
      "s_addc_u32 s81, s71, 0\n"
      "s_add_u32 m0, s82, 65536\n"
      "s_nop 0\n"
      "global_load_lds_dwordx4 v232, s[80:81] offset:0\n"
      "v_mfma_f32_16x16x32_bf16 v[104:107], v[184:187], v[152:155], v[104:107]\n"
      "v_accvgpr_read_b32 v205, a45\n"
      "v_accvgpr_read_b32 v206, a46\n"
      "v_mfma_f32_16x16x32_bf16 v[108:111], v[188:191], v[152:155], v[108:111]\n"
      "ds_read_b128 v[152:155], v240 offset:12288\n"
      "v_accvgpr_read_b32 v207, a47\n"
      "v_mfma_f32_16x16x32_bf16 v[112:115], v[176:179], v[156:159], v[112:115]\n"
      "v_cvt_pk_bf16_f32 v200, v200, v201\n"
      "v_mfma_f32_16x16x32_bf16 v[116:119], v[180:183], v[156:159], v[116:119]\n"
      "v_cvt_pk_bf16_f32 v201, v202, v203\n"
      "v_mfma_f32_16x16x32_bf16 v[120:123], v[184:187], v[156:159], v[120:123]\n"
      "global_load_lds_dwordx4 v233, s[80:81] offset:1024\n"
      "v_mfma_f32_16x16x32_bf16 v[124:127], v[188:191], v[156:159], v[124:127]\n"
      "v_cvt_pk_bf16_f32 v202, v204, v205\n"
      "v_mfma_f32_16x16x32_bf16 v[220:223], v[188:191], v[250:253], v[220:223]\n"
      "ds_read_b128 v[156:159], v240 offset:14336\n"
      "v_cvt_pk_bf16_f32 v203, v206, v207\n"
      "s_waitcnt lgkmcnt(7)\n"
      "v_mfma_f32_16x16x32_bf16 v[0:3], v[160:163], v[128:131], v[0:3]\n"
      "ds_read_b64_tr_b16 v[176:177], v242 offset:8192\n"
      "v_mfma_f32_16x16x32_bf16 v[4:7], v[164:167], v[128:131], v[4:7]\n"
      "global_store_dwordx4 v246, v[200:203], s[84:85] offset:1024 sc1\n"
      "s_waitcnt lgkmcnt(1)\n"
      "v_mfma_f32_16x16x32_bf16 v[8:11], v[168:171], v[128:131], v[8:11]\n"
      "v_accvgpr_write_b32 a40, v200\n"
      "v_accvgpr_write_b32 a41, v201\n"
      "v_mfma_f32_16x16x32_bf16 v[12:15], v[172:175], v[128:131], v[12:15]\n"
      "ds_read_b128 v[128:131], v241 offset:0\n"
      "ds_read_b64_tr_b16 v[178:179], v242 offset:8448\n"
      "global_load_lds_dwordx4 v234, s[80:81] offset:2048\n"
      "v_mfma_f32_16x16x32_bf16 v[16:19], v[160:163], v[132:135], v[16:19]\n"
      "v_accvgpr_write_b32 a42, v202\n"
      "v_accvgpr_write_b32 a43, v203\n"
      "v_mfma_f32_16x16x32_bf16 v[20:23], v[164:167], v[132:135], v[20:23]\n"
      "s_add_u32 s84, s72, 12288\n s_addc_u32 s85, s73, 0\n"
      "v_mfma_f32_16x16x32_bf16 v[24:27], v[168:171], v[132:135], v[24:27]\n"
      "ds_read_b64_tr_b16 v[180:181], v243 offset:8192\n"
      "v_accvgpr_read_b32 v192, a48\n"
      "v_mfma_f32_16x16x32_bf16 v[28:31], v[172:175], v[132:135], v[28:31]\n"
      "v_accvgpr_read_b32 v193, a49\n"
      "v_accvgpr_read_b32 v194, a50\n"
      "v_mfma_f32_16x16x32_bf16 v[208:211], v[160:163], v[250:253], v[208:211]\n"
      "ds_read_b128 v[132:135], v241 offset:2048\n"
      "v_accvgpr_read_b32 v195, a51\n"
      "v_mfma_f32_16x16x32_bf16 v[32:35], v[160:163], v[136:139], v[32:35]\n"
      "global_load_lds_dwordx4 v235, s[80:81] offset:3072\n"
      "v_mfma_f32_16x16x32_bf16 v[36:39], v[164:167], v[136:139], v[36:39]\n"
      "ds_read_b64_tr_b16 v[182:183], v243 offset:8448\n"
      "v_accvgpr_read_b32 v196, a52\n"
      "v_mfma_f32_16x16x32_bf16 v[40:43], v[168:171], v[136:139], v[40:43]\n"
      "v_accvgpr_read_b32 v197, a53\n"
      "v_mfma_f32_16x16x32_bf16 v[44:47], v[172:175], v[136:139], v[44:47]\n"
      "ds_read_b128 v[136:139], v241 offset:4096\n"
      "v_accvgpr_read_b32 v198, a54\n"
      "v_mfma_f32_16x16x32_bf16 v[48:51], v[160:163], v[140:143], v[48:51]\n"
      "ds_read_b64_tr_b16 v[184:185], v242 offset:8704\n"
      "v_accvgpr_read_b32 v199, a55\n"
      "v_mfma_f32_16x16x32_bf16 v[52:55], v[164:167], v[140:143], v[52:55]\n"
      "v_cvt_pk_bf16_f32 v192, v192, v193\n"
      "v_mfma_f32_16x16x32_bf16 v[56:59], v[168:171], v[140:143], v[56:59]\n"
      "s_add_u32 s80, s66, 640\n"
      "s_addc_u32 s81, s67, 0\n"
      "s_add_u32 m0, s82, 81920\n"
      "s_nop 0\n"
      "global_load_lds_dwordx4 v228, s[80:81] offset:0\n"
      "v_mfma_f32_16x16x32_bf16 v[60:63], v[172:175], v[140:143], v[60:63]\n"
      "ds_read_b64_tr_b16 v[186:187], v242 offset:8960\n"
      "v_cvt_pk_bf16_f32 v193, v194, v195\n"
      "v_mfma_f32_16x16x32_bf16 v[212:215], v[164:167], v[250:253], v[212:215]\n"
      "ds_read_b128 v[140:143], v241 offset:6144\n"
      "v_cvt_pk_bf16_f32 v194, v196, v197\n"
      "v_mfma_f32_16x16x32_bf16 v[64:67], v[160:163], v[144:147], v[64:67]\n"
      "v_cvt_pk_bf16_f32 v195, v198, v199\n"
      "v_mfma_f32_16x16x32_bf16 v[68:71], v[164:167], v[144:147], v[68:71]\n"
      "global_store_dwordx4 v246, v[192:195], s[84:85] offset:0 sc1\n"
      "v_mfma_f32_16x16x32_bf16 v[72:75], v[168:171], v[144:147], v[72:75]\n"
      "ds_read_b64_tr_b16 v[188:189], v243 offset:8704\n"
      "v_accvgpr_write_b32 a48, v192\n"
      "v_mfma_f32_16x16x32_bf16 v[76:79], v[172:175], v[144:147], v[76:79]\n"
      "ds_read_b128 v[144:147], v241 offset:8192\n"
      "global_load_lds_dwordx4 v229, s[80:81] offset:1024\n"
      "v_mfma_f32_16x16x32_bf16 v[80:83], v[160:163], v[148:151], v[80:83]\n"
      "v_accvgpr_write_b32 a49, v193\n"
      "v_mfma_f32_16x16x32_bf16 v[84:87], v[164:167], v[148:151], v[84:87]\n"
      "ds_read_b64_tr_b16 v[190:191], v243 offset:8960\n"
      "v_accvgpr_write_b32 a50, v194\n"
      "v_mfma_f32_16x16x32_bf16 v[88:91], v[168:171], v[148:151], v[88:91]\n"
      "v_accvgpr_write_b32 a51, v195\n"
      "v_mfma_f32_16x16x32_bf16 v[92:95], v[172:175], v[148:151], v[92:95]\n"
      "v_accvgpr_read_b32 v200, a56\n"
      "v_mfma_f32_16x16x32_bf16 v[216:219], v[168:171], v[250:253], v[216:219]\n"
      "ds_read_b128 v[148:151], v241 offset:10240\n"
      "v_accvgpr_read_b32 v201, a57\n"
      "v_mfma_f32_16x16x32_bf16 v[96:99], v[160:163], v[152:155], v[96:99]\n"
      "global_load_lds_dwordx4 v230, s[80:81] offset:2048\n"
      "v_mfma_f32_16x16x32_bf16 v[100:103], v[164:167], v[152:155], v[100:103]\n"
      "v_accvgpr_read_b32 v202, a58\n"
      "v_mfma_f32_16x16x32_bf16 v[104:107], v[168:171], v[152:155], v[104:107]\n"
      "v_accvgpr_read_b32 v203, a59\n"
      "v_accvgpr_read_b32 v204, a60\n"
      "v_mfma_f32_16x16x32_bf16 v[108:111], v[172:175], v[152:155], v[108:111]\n"
      "ds_read_b128 v[152:155], v241 offset:12288\n"
      "v_accvgpr_read_b32 v205, a61\n"
      "v_mfma_f32_16x16x32_bf16 v[112:115], v[160:163], v[156:159], v[112:115]\n"
      "v_accvgpr_read_b32 v206, a62\n"
      "v_mfma_f32_16x16x32_bf16 v[116:119], v[164:167], v[156:159], v[116:119]\n"
      "v_accvgpr_read_b32 v207, a63\n"
      "v_mfma_f32_16x16x32_bf16 v[120:123], v[168:171], v[156:159], v[120:123]\n"
      "global_load_lds_dwordx4 v231, s[80:81] offset:3072\n"
      "v_mfma_f32_16x16x32_bf16 v[124:127], v[172:175], v[156:159], v[124:127]\n"
      "v_cvt_pk_bf16_f32 v200, v200, v201\n"
      "v_mfma_f32_16x16x32_bf16 v[220:223], v[172:175], v[250:253], v[220:223]\n"
      "ds_read_b128 v[156:159], v241 offset:14336\n"
      "v_cvt_pk_bf16_f32 v201, v202, v203\n"
      "s_waitcnt lgkmcnt(7)\n"
      "v_mfma_f32_16x16x32_bf16 v[0:3], v[176:179], v[128:131], v[0:3]\n"
      "v_mfma_f32_16x16x32_bf16 v[4:7], v[180:183], v[128:131], v[4:7]\n"
      "v_mfma_f32_16x16x32_bf16 v[8:11], v[184:187], v[128:131], v[8:11]\n"
      "s_waitcnt lgkmcnt(0)\n"
      "v_mfma_f32_16x16x32_bf16 v[12:15], v[188:191], v[128:131], v[12:15]\n"
      "s_waitcnt vmcnt(21) lgkmcnt(0)\n"
      "s_barrier\n"
      "v_add_u32 v240, 0, v236\n"
      "v_add_u32 v241, 0, v237\n"
      "v_add_u32 v242, 147456, v238\n"
      "v_add_u32 v243, 147456, v239\n"
      "ds_read_b128 v[128:131], v240 offset:0\n"
      "v_mfma_f32_16x16x32_bf16 v[16:19], v[176:179], v[132:135], v[16:19]\n"
      "ds_read_b64_tr_b16 v[160:161], v242 offset:0\n"
      "v_cvt_pk_bf16_f32 v202, v204, v205\n"
      "v_mfma_f32_16x16x32_bf16 v[20:23], v[180:183], v[132:135], v[20:23]\n"
      "v_cvt_pk_bf16_f32 v203, v206, v207\n"
      "v_mfma_f32_16x16x32_bf16 v[24:27], v[184:187], v[132:135], v[24:27]\n"
      "s_add_u32 s80, s68, 640\n"
      "s_addc_u32 s81, s69, 0\n"
      "s_add_u32 m0, s82, 98304\n"
      "s_nop 0\n"
      "global_load_lds_dwordx4 v228, s[80:81] offset:0\n"
      "v_mfma_f32_16x16x32_bf16 v[28:31], v[188:191], v[132:135], v[28:31]\n"
      "ds_read_b64_tr_b16 v[162:163], v242 offset:256\n"
      "v_mfma_f32_16x16x32_bf16 v[208:211], v[176:179], v[250:253], v[208:211]\n"
      "ds_read_b128 v[132:135], v240 offset:2048\n"
      "v_mfma_f32_16x16x32_bf16 v[32:35], v[176:179], v[136:139], v[32:35]\n"
      "global_store_dwordx4 v246, v[200:203], s[84:85] offset:1024 sc1\n"
      "v_mfma_f32_16x16x32_bf16 v[36:39], v[180:183], v[136:139], v[36:39]\n"
      "ds_read_b64_tr_b16 v[164:165], v243 offset:0\n"
      "v_accvgpr_write_b32 a56, v200\n"
      "v_mfma_f32_16x16x32_bf16 v[40:43], v[184:187], v[136:139], v[40:43]\n"
      "v_accvgpr_write_b32 a57, v201\n"
      "v_accvgpr_write_b32 a58, v202\n"
      "v_mfma_f32_16x16x32_bf16 v[44:47], v[188:191], v[136:139], v[44:47]\n"
      "ds_read_b128 v[136:139], v240 offset:4096\n"
      "global_load_lds_dwordx4 v229, s[80:81] offset:1024\n"
      "v_mfma_f32_16x16x32_bf16 v[48:51], v[176:179], v[140:143], v[48:51]\n"
      "ds_read_b64_tr_b16 v[166:167], v243 offset:256\n"
      "v_accvgpr_write_b32 a59, v203\n"
      "v_mfma_f32_16x16x32_bf16 v[52:55], v[180:183], v[140:143], v[52:55]\n"
      "s_add_u32 s84, s72, 16384\n s_addc_u32 s85, s73, 0\n"
      "v_mfma_f32_16x16x32_bf16 v[56:59], v[184:187], v[140:143], v[56:59]\n"
      "v_accvgpr_read_b32 v192, a64\n"
      "v_accvgpr_read_b32 v193, a65\n"
      "v_mfma_f32_16x16x32_bf16 v[60:63], v[188:191], v[140:143], v[60:63]\n"
      "ds_read_b64_tr_b16 v[168:169], v242 offset:512\n"
      "v_accvgpr_read_b32 v194, a66\n"
      "v_mfma_f32_16x16x32_bf16 v[212:215], v[180:183], v[250:253], v[212:215]\n"
      "ds_read_b128 v[140:143], v240 offset:6144\n"
      "global_load_lds_dwordx4 v230, s[80:81] offset:2048\n"
      "v_mfma_f32_16x16x32_bf16 v[64:67], v[176:179], v[144:147], v[64:67]\n"
      "v_accvgpr_read_b32 v195, a67\n"
      "v_accvgpr_read_b32 v196, a68\n"
      "v_mfma_f32_16x16x32_bf16 v[68:71], v[180:183], v[144:147], v[68:71]\n"
      "ds_read_b64_tr_b16 v[170:171], v242 offset:768\n"
      "v_accvgpr_read_b32 v197, a69\n"
      "v_mfma_f32_16x16x32_bf16 v[72:75], v[184:187], v[144:147], v[72:75]\n"
      "v_accvgpr_read_b32 v198, a70\n"
      "v_accvgpr_read_b32 v199, a71\n"
      "v_mfma_f32_16x16x32_bf16 v[76:79], v[188:191], v[144:147], v[76:79]\n"
      "ds_read_b128 v[144:147], v240 offset:8192\n"
      "v_cvt_pk_bf16_f32 v192, v192, v193\n"
      "v_mfma_f32_16x16x32_bf16 v[80:83], v[176:179], v[148:151], v[80:83]\n"
      "ds_read_b64_tr_b16 v[172:173], v243 offset:512\n"
      "global_load_lds_dwordx4 v231, s[80:81] offset:3072\n"
      "v_mfma_f32_16x16x32_bf16 v[84:87], v[180:183], v[148:151], v[84:87]\n"
      "v_cvt_pk_bf16_f32 v193, v194, v195\n"
      "v_cvt_pk_bf16_f32 v194, v196, v197\n"
      "v_mfma_f32_16x16x32_bf16 v[88:91], v[184:187], v[148:151], v[88:91]\n"
      "v_cvt_pk_bf16_f32 v195, v198, v199\n"
      "v_mfma_f32_16x16x32_bf16 v[92:95], v[188:191], v[148:151], v[92:95]\n"
      "ds_read_b64_tr_b16 v[174:175], v243 offset:768\n"
      "global_store_dwordx4 v246, v[192:195], s[84:85] offset:0 sc1\n"
      "v_mfma_f32_16x16x32_bf16 v[216:219], v[184:187], v[250:253], v[216:219]\n"
      "ds_read_b128 v[148:151], v240 offset:10240\n"
      "v_accvgpr_write_b32 a64, v192\n"
      "v_mfma_f32_16x16x32_bf16 v[96:99], v[176:179], v[152:155], v[96:99]\n"
      "v_accvgpr_write_b32 a65, v193\n"
      "v_accvgpr_write_b32 a66, v194\n"
      "v_mfma_f32_16x16x32_bf16 v[100:103], v[180:183], v[152:155], v[100:103]\n"
      "s_add_u32 s80, s70, 2621440\n"
      "s_addc_u32 s81, s71, 0\n"
      "s_add_u32 m0, s82, 114688\n"
      "s_nop 0\n"
      "global_load_lds_dwordx4 v232, s[80:81] offset:0\n"
      "v_mfma_f32_16x16x32_bf16 v[104:107], v[184:187], v[152:155], v[104:107]\n"
      "v_accvgpr_write_b32 a67, v195\n"
      "v_accvgpr_read_b32 v200, a72\n"
      "v_mfma_f32_16x16x32_bf16 v[108:111], v[188:191], v[152:155], v[108:111]\n"
      "ds_read_b128 v[152:155], v240 offset:12288\n"
      "v_accvgpr_read_b32 v201, a73\n"
      "v_mfma_f32_16x16x32_bf16 v[112:115], v[176:179], v[156:159], v[112:115]\n"
      "v_accvgpr_read_b32 v202, a74\n"
      "v_accvgpr_read_b32 v203, a75\n"
      "v_mfma_f32_16x16x32_bf16 v[116:119], v[180:183], v[156:159], v[116:119]\n"
      "v_accvgpr_read_b32 v204, a76\n"
      "v_accvgpr_read_b32 v205, a77\n"
      "v_mfma_f32_16x16x32_bf16 v[120:123], v[184:187], v[156:159], v[120:123]\n"
      "global_load_lds_dwordx4 v233, s[80:81] offset:1024\n"
      "v_mfma_f32_16x16x32_bf16 v[124:127], v[188:191], v[156:159], v[124:127]\n"
      "v_accvgpr_read_b32 v206, a78\n"
      "v_mfma_f32_16x16x32_bf16 v[220:223], v[188:191], v[250:253], v[220:223]\n"
      "ds_read_b128 v[156:159], v240 offset:14336\n"
      "v_accvgpr_read_b32 v207, a79\n"
      "s_waitcnt lgkmcnt(7)\n"
      "v_mfma_f32_16x16x32_bf16 v[0:3], v[160:163], v[128:131], v[0:3]\n"
      "ds_read_b64_tr_b16 v[176:177], v242 offset:8192\n"
      "v_cvt_pk_bf16_f32 v200, v200, v201\n"
      "v_mfma_f32_16x16x32_bf16 v[4:7], v[164:167], v[128:131], v[4:7]\n"
      "v_cvt_pk_bf16_f32 v201, v202, v203\n"
      "s_waitcnt lgkmcnt(1)\n"
      "v_mfma_f32_16x16x32_bf16 v[8:11], v[168:171], v[128:131], v[8:11]\n"
      "v_cvt_pk_bf16_f32 v202, v204, v205\n"
      "v_mfma_f32_16x16x32_bf16 v[12:15], v[172:175], v[128:131], v[12:15]\n"
      "ds_read_b128 v[128:131], v241 offset:0\n"
      "ds_read_b64_tr_b16 v[178:179], v242 offset:8448\n"
      "global_load_lds_dwordx4 v234, s[80:81] offset:2048\n"
      "v_mfma_f32_16x16x32_bf16 v[16:19], v[160:163], v[132:135], v[16:19]\n"
      "v_cvt_pk_bf16_f32 v203, v206, v207\n"
      "v_mfma_f32_16x16x32_bf16 v[20:23], v[164:167], v[132:135], v[20:23]\n"
      "global_store_dwordx4 v246, v[200:203], s[84:85] offset:1024 sc1\n"
      "v_mfma_f32_16x16x32_bf16 v[24:27], v[168:171], v[132:135], v[24:27]\n"
      "ds_read_b64_tr_b16 v[180:181], v243 offset:8192\n"
      "v_accvgpr_write_b32 a72, v200\n"
      "v_mfma_f32_16x16x32_bf16 v[28:31], v[172:175], v[132:135], v[28:31]\n"
      "v_accvgpr_write_b32 a73, v201\n"
      "v_accvgpr_write_b32 a74, v202\n"
      "v_mfma_f32_16x16x32_bf16 v[208:211], v[160:163], v[250:253], v[208:211]\n"
      "ds_read_b128 v[132:135], v241 offset:2048\n"
      "v_accvgpr_write_b32 a75, v203\n"
      "v_mfma_f32_16x16x32_bf16 v[32:35], v[160:163], v[136:139], v[32:35]\n"
      "global_load_lds_dwordx4 v235, s[80:81] offset:3072\n"
      "v_mfma_f32_16x16x32_bf16 v[36:39], v[164:167], v[136:139], v[36:39]\n"
      "ds_read_b64_tr_b16 v[182:183], v243 offset:8448\n"
      "v_mfma_f32_16x16x32_bf16 v[40:43], v[168:171], v[136:139], v[40:43]\n"
      "s_add_u32 s84, s72, 20480\n s_addc_u32 s85, s73, 0\n"
      "v_mfma_f32_16x16x32_bf16 v[44:47], v[172:175], v[136:139], v[44:47]\n"
      "ds_read_b128 v[136:139], v241 offset:4096\n"
      "v_accvgpr_read_b32 v192, a80\n"
      "v_mfma_f32_16x16x32_bf16 v[48:51], v[160:163], v[140:143], v[48:51]\n"
      "ds_read_b64_tr_b16 v[184:185], v242 offset:8704\n"
      "v_accvgpr_read_b32 v193, a81\n"
      "v_mfma_f32_16x16x32_bf16 v[52:55], v[164:167], v[140:143], v[52:55]\n"
      "v_accvgpr_read_b32 v194, a82\n"
      "v_accvgpr_read_b32 v195, a83\n"
      "v_mfma_f32_16x16x32_bf16 v[56:59], v[168:171], v[140:143], v[56:59]\n"
      "s_add_u32 s80, s66, 768\n"
      "s_addc_u32 s81, s67, 0\n"
      "s_add_u32 m0, s82, 131072\n"
      "s_nop 0\n"
      "global_load_lds_dwordx4 v228, s[80:81] offset:0\n"
      "v_mfma_f32_16x16x32_bf16 v[60:63], v[172:175], v[140:143], v[60:63]\n"
      "ds_read_b64_tr_b16 v[186:187], v242 offset:8960\n"
      "v_accvgpr_read_b32 v196, a84\n"
      "v_mfma_f32_16x16x32_bf16 v[212:215], v[164:167], v[250:253], v[212:215]\n"
      "ds_read_b128 v[140:143], v241 offset:6144\n"
      "v_accvgpr_read_b32 v197, a85\n"
      "v_mfma_f32_16x16x32_bf16 v[64:67], v[160:163], v[144:147], v[64:67]\n"
      "v_accvgpr_read_b32 v198, a86\n"
      "v_mfma_f32_16x16x32_bf16 v[68:71], v[164:167], v[144:147], v[68:71]\n"
      "v_accvgpr_read_b32 v199, a87\n"
      "v_mfma_f32_16x16x32_bf16 v[72:75], v[168:171], v[144:147], v[72:75]\n"
      "ds_read_b64_tr_b16 v[188:189], v243 offset:8704\n"
      "v_cvt_pk_bf16_f32 v192, v192, v193\n"
      "v_mfma_f32_16x16x32_bf16 v[76:79], v[172:175], v[144:147], v[76:79]\n"
      "ds_read_b128 v[144:147], v241 offset:8192\n"
      "global_load_lds_dwordx4 v229, s[80:81] offset:1024\n"
      "v_mfma_f32_16x16x32_bf16 v[80:83], v[160:163], v[148:151], v[80:83]\n"
      "v_cvt_pk_bf16_f32 v193, v194, v195\n"
      "v_mfma_f32_16x16x32_bf16 v[84:87], v[164:167], v[148:151], v[84:87]\n"
      "ds_read_b64_tr_b16 v[190:191], v243 offset:8960\n"
      "v_cvt_pk_bf16_f32 v194, v196, v197\n"
      "v_mfma_f32_16x16x32_bf16 v[88:91], v[168:171], v[148:151], v[88:91]\n"
      "v_cvt_pk_bf16_f32 v195, v198, v199\n"
      "v_mfma_f32_16x16x32_bf16 v[92:95], v[172:175], v[148:151], v[92:95]\n"
      "global_store_dwordx4 v246, v[192:195], s[84:85] offset:0 sc1\n"
      "v_mfma_f32_16x16x32_bf16 v[216:219], v[168:171], v[250:253], v[216:219]\n"
      "ds_read_b128 v[148:151], v241 offset:10240\n"
      "v_accvgpr_write_b32 a80, v192\n"
      "v_mfma_f32_16x16x32_bf16 v[96:99], v[160:163], v[152:155], v[96:99]\n"
      "global_load_lds_dwordx4 v230, s[80:81] offset:2048\n"
      "v_mfma_f32_16x16x32_bf16 v[100:103], v[164:167], v[152:155], v[100:103]\n"
      "v_accvgpr_write_b32 a81, v193\n"
      "v_mfma_f32_16x16x32_bf16 v[104:107], v[168:171], v[152:155], v[104:107]\n"
      "v_accvgpr_write_b32 a82, v194\n"
      "v_mfma_f32_16x16x32_bf16 v[108:111], v[172:175], v[152:155], v[108:111]\n"
      "ds_read_b128 v[152:155], v241 offset:12288\n"
      "v_accvgpr_write_b32 a83, v195\n"
      "v_mfma_f32_16x16x32_bf16 v[112:115], v[160:163], v[156:159], v[112:115]\n"
      "v_accvgpr_read_b32 v200, a88\n"
      "v_mfma_f32_16x16x32_bf16 v[116:119], v[164:167], v[156:159], v[116:119]\n"
      "v_accvgpr_read_b32 v201, a89\n"
      "v_accvgpr_read_b32 v202, a90\n"
      "v_mfma_f32_16x16x32_bf16 v[120:123], v[168:171], v[156:159], v[120:123]\n"
      "global_load_lds_dwordx4 v231, s[80:81] offset:3072\n"
      "v_mfma_f32_16x16x32_bf16 v[124:127], v[172:175], v[156:159], v[124:127]\n"
      "v_accvgpr_read_b32 v203, a91\n"
      "v_mfma_f32_16x16x32_bf16 v[220:223], v[172:175], v[250:253], v[220:223]\n"
      "ds_read_b128 v[156:159], v241 offset:14336\n"
      "v_accvgpr_read_b32 v204, a92\n"
      "s_waitcnt lgkmcnt(7)\n"
      "v_mfma_f32_16x16x32_bf16 v[0:3], v[176:179], v[128:131], v[0:3]\n"
      "v_mfma_f32_16x16x32_bf16 v[4:7], v[180:183], v[128:131], v[4:7]\n"
      "v_mfma_f32_16x16x32_bf16 v[8:11], v[184:187], v[128:131], v[8:11]\n"
      "s_waitcnt lgkmcnt(0)\n"
      "v_mfma_f32_16x16x32_bf16 v[12:15], v[188:191], v[128:131], v[12:15]\n"
      "s_waitcnt vmcnt(21) lgkmcnt(0)\n"
      "s_barrier\n"
      "v_add_u32 v240, 32768, v236\n"
      "v_add_u32 v241, 32768, v237\n"
      "v_add_u32 v242, 65536, v238\n"
      "v_add_u32 v243, 65536, v239\n"
      "ds_read_b128 v[128:131], v240 offset:0\n"
      "v_mfma_f32_16x16x32_bf16 v[16:19], v[176:179], v[132:135], v[16:19]\n"
      "ds_read_b64_tr_b16 v[160:161], v242 offset:0\n"
      "v_accvgpr_read_b32 v205, a93\n"
      "v_mfma_f32_16x16x32_bf16 v[20:23], v[180:183], v[132:135], v[20:23]\n"
      "v_accvgpr_read_b32 v206, a94\n"
      "v_accvgpr_read_b32 v207, a95\n"
      "v_mfma_f32_16x16x32_bf16 v[24:27], v[184:187], v[132:135], v[24:27]\n"
      "s_add_u32 s80, s68, 768\n"
      "s_addc_u32 s81, s69, 0\n"
      "s_add_u32 m0, s82, 147456\n"
      "s_nop 0\n"
      "global_load_lds_dwordx4 v228, s[80:81] offset:0\n"
      "v_mfma_f32_16x16x32_bf16 v[28:31], v[188:191], v[132:135], v[28:31]\n"
      "ds_read_b64_tr_b16 v[162:163], v242 offset:256\n"
      "v_cvt_pk_bf16_f32 v200, v200, v201\n"
      "v_mfma_f32_16x16x32_bf16 v[208:211], v[176:179], v[250:253], v[208:211]\n"
      "ds_read_b128 v[132:135], v240 offset:2048\n"
      "v_cvt_pk_bf16_f32 v201, v202, v203\n"
      "v_mfma_f32_16x16x32_bf16 v[32:35], v[176:179], v[136:139], v[32:35]\n"
      "v_cvt_pk_bf16_f32 v202, v204, v205\n"
      "v_mfma_f32_16x16x32_bf16 v[36:39], v[180:183], v[136:139], v[36:39]\n"
      "ds_read_b64_tr_b16 v[164:165], v243 offset:0\n"
      "v_cvt_pk_bf16_f32 v203, v206, v207\n"
      "v_mfma_f32_16x16x32_bf16 v[40:43], v[184:187], v[136:139], v[40:43]\n"
      "global_store_dwordx4 v246, v[200:203], s[84:85] offset:1024 sc1\n"
      "v_mfma_f32_16x16x32_bf16 v[44:47], v[188:191], v[136:139], v[44:47]\n"
      "ds_read_b128 v[136:139], v240 offset:4096\n"
      "global_load_lds_dwordx4 v229, s[80:81] offset:1024\n"
      "v_mfma_f32_16x16x32_bf16 v[48:51], v[176:179], v[140:143], v[48:51]\n"
      "ds_read_b64_tr_b16 v[166:167], v243 offset:256\n"
      "v_accvgpr_write_b32 a88, v200\n"
      "v_mfma_f32_16x16x32_bf16 v[52:55], v[180:183], v[140:143], v[52:55]\n"
      "v_accvgpr_write_b32 a89, v201\n"
      "v_accvgpr_write_b32 a90, v202\n"
      "v_mfma_f32_16x16x32_bf16 v[56:59], v[184:187], v[140:143], v[56:59]\n"
      "v_accvgpr_write_b32 a91, v203\n"
      "v_mfma_f32_16x16x32_bf16 v[60:63], v[188:191], v[140:143], v[60:63]\n"
      "ds_read_b64_tr_b16 v[168:169], v242 offset:512\n"
      "v_mfma_f32_16x16x32_bf16 v[212:215], v[180:183], v[250:253], v[212:215]\n"
      "ds_read_b128 v[140:143], v240 offset:6144\n"
      "global_load_lds_dwordx4 v230, s[80:81] offset:2048\n"
      "v_mfma_f32_16x16x32_bf16 v[64:67], v[176:179], v[144:147], v[64:67]\n"
      "s_add_u32 s84, s72, 24576\n s_addc_u32 s85, s73, 0\n"
      "v_mfma_f32_16x16x32_bf16 v[68:71], v[180:183], v[144:147], v[68:71]\n"
      "ds_read_b64_tr_b16 v[170:171], v242 offset:768\n"
      "v_accvgpr_read_b32 v192, a96\n"
      "v_mfma_f32_16x16x32_bf16 v[72:75], v[184:187], v[144:147], v[72:75]\n"
      "v_accvgpr_read_b32 v193, a97\n"
      "v_accvgpr_read_b32 v194, a98\n"
      "v_mfma_f32_16x16x32_bf16 v[76:79], v[188:191], v[144:147], v[76:79]\n"
      "ds_read_b128 v[144:147], v240 offset:8192\n"
      "v_accvgpr_read_b32 v195, a99\n"
      "v_mfma_f32_16x16x32_bf16 v[80:83], v[176:179], v[148:151], v[80:83]\n"
      "ds_read_b64_tr_b16 v[172:173], v243 offset:512\n"
      "global_load_lds_dwordx4 v231, s[80:81] offset:3072\n"
      "v_mfma_f32_16x16x32_bf16 v[84:87], v[180:183], v[148:151], v[84:87]\n"
      "v_accvgpr_read_b32 v196, a100\n"
      "v_accvgpr_read_b32 v197, a101\n"
      "v_mfma_f32_16x16x32_bf16 v[88:91], v[184:187], v[148:151], v[88:91]\n"
      "v_accvgpr_read_b32 v198, a102\n"
      "v_accvgpr_read_b32 v199, a103\n"
      "v_mfma_f32_16x16x32_bf16 v[92:95], v[188:191], v[148:151], v[92:95]\n"
      "ds_read_b64_tr_b16 v[174:175], v243 offset:768\n"
      "v_cvt_pk_bf16_f32 v192, v192, v193\n"
      "v_mfma_f32_16x16x32_bf16 v[216:219], v[184:187], v[250:253], v[216:219]\n"
      "ds_read_b128 v[148:151], v240 offset:10240\n"
      "v_cvt_pk_bf16_f32 v193, v194, v195\n"
      "v_mfma_f32_16x16x32_bf16 v[96:99], v[176:179], v[152:155], v[96:99]\n"
      "v_cvt_pk_bf16_f32 v194, v196, v197\n"
      "v_mfma_f32_16x16x32_bf16 v[100:103], v[180:183], v[152:155], v[100:103]\n"
      "s_add_u32 s80, s70, 3145728\n"
      "s_addc_u32 s81, s71, 0\n"
      "s_add_u32 m0, s82, 0\n"
      "s_nop 0\n"
      "global_load_lds_dwordx4 v232, s[80:81] offset:0\n"
      "v_mfma_f32_16x16x32_bf16 v[104:107], v[184:187], v[152:155], v[104:107]\n"
      "v_cvt_pk_bf16_f32 v195, v198, v199\n"
      "v_mfma_f32_16x16x32_bf16 v[108:111], v[188:191], v[152:155], v[108:111]\n"
      "ds_read_b128 v[152:155], v240 offset:12288\n"
      "global_store_dwordx4 v246, v[192:195], s[84:85] offset:0 sc1\n"
      "v_mfma_f32_16x16x32_bf16 v[112:115], v[176:179], v[156:159], v[112:115]\n"
      "v_accvgpr_write_b32 a96, v192\n"
      "v_accvgpr_write_b32 a97, v193\n"
      "v_mfma_f32_16x16x32_bf16 v[116:119], v[180:183], v[156:159], v[116:119]\n"
      "v_accvgpr_write_b32 a98, v194\n"
      "v_accvgpr_write_b32 a99, v195\n"
      "v_mfma_f32_16x16x32_bf16 v[120:123], v[184:187], v[156:159], v[120:123]\n"
      "global_load_lds_dwordx4 v233, s[80:81] offset:1024\n"
      "v_mfma_f32_16x16x32_bf16 v[124:127], v[188:191], v[156:159], v[124:127]\n"
      "v_accvgpr_read_b32 v200, a104\n"
      "v_mfma_f32_16x16x32_bf16 v[220:223], v[188:191], v[250:253], v[220:223]\n"
      "ds_read_b128 v[156:159], v240 offset:14336\n"
      "v_accvgpr_read_b32 v201, a105\n"
      "s_waitcnt lgkmcnt(7)\n"
      "v_mfma_f32_16x16x32_bf16 v[0:3], v[160:163], v[128:131], v[0:3]\n"
      "ds_read_b64_tr_b16 v[176:177], v242 offset:8192\n"
      "v_accvgpr_read_b32 v202, a106\n"
      "v_mfma_f32_16x16x32_bf16 v[4:7], v[164:167], v[128:131], v[4:7]\n"
      "v_accvgpr_read_b32 v203, a107\n"
      "v_accvgpr_read_b32 v204, a108\n"
      "s_waitcnt lgkmcnt(1)\n"
      "v_mfma_f32_16x16x32_bf16 v[8:11], v[168:171], v[128:131], v[8:11]\n"
      "v_accvgpr_read_b32 v205, a109\n"
      "v_accvgpr_read_b32 v206, a110\n"
      "v_mfma_f32_16x16x32_bf16 v[12:15], v[172:175], v[128:131], v[12:15]\n"
      "ds_read_b128 v[128:131], v241 offset:0\n"
      "ds_read_b64_tr_b16 v[178:179], v242 offset:8448\n"
      "global_load_lds_dwordx4 v234, s[80:81] offset:2048\n"
      "v_mfma_f32_16x16x32_bf16 v[16:19], v[160:163], v[132:135], v[16:19]\n"
      "v_accvgpr_read_b32 v207, a111\n"
      "v_mfma_f32_16x16x32_bf16 v[20:23], v[164:167], v[132:135], v[20:23]\n"
      "v_cvt_pk_bf16_f32 v200, v200, v201\n"
      "v_mfma_f32_16x16x32_bf16 v[24:27], v[168:171], v[132:135], v[24:27]\n"
      "ds_read_b64_tr_b16 v[180:181], v243 offset:8192\n"
      "v_cvt_pk_bf16_f32 v201, v202, v203\n"
      "v_mfma_f32_16x16x32_bf16 v[28:31], v[172:175], v[132:135], v[28:31]\n"
      "v_cvt_pk_bf16_f32 v202, v204, v205\n"
      "v_mfma_f32_16x16x32_bf16 v[208:211], v[160:163], v[250:253], v[208:211]\n"
      "ds_read_b128 v[132:135], v241 offset:2048\n"
      "v_cvt_pk_bf16_f32 v203, v206, v207\n"
      "v_mfma_f32_16x16x32_bf16 v[32:35], v[160:163], v[136:139], v[32:35]\n"
      "global_load_lds_dwordx4 v235, s[80:81] offset:3072\n"
      "v_mfma_f32_16x16x32_bf16 v[36:39], v[164:167], v[136:139], v[36:39]\n"
      "ds_read_b64_tr_b16 v[182:183], v243 offset:8448\n"
      "v_mfma_f32_16x16x32_bf16 v[40:43], v[168:171], v[136:139], v[40:43]\n"
      "global_store_dwordx4 v246, v[200:203], s[84:85] offset:1024 sc1\n"
      "v_mfma_f32_16x16x32_bf16 v[44:47], v[172:175], v[136:139], v[44:47]\n"
      "ds_read_b128 v[136:139], v241 offset:4096\n"
      "v_accvgpr_write_b32 a104, v200\n"
      "v_mfma_f32_16x16x32_bf16 v[48:51], v[160:163], v[140:143], v[48:51]\n"
      "ds_read_b64_tr_b16 v[184:185], v242 offset:8704\n"
      "v_accvgpr_write_b32 a105, v201\n"
      "v_mfma_f32_16x16x32_bf16 v[52:55], v[164:167], v[140:143], v[52:55]\n"
      "v_accvgpr_write_b32 a106, v202\n"
      "v_accvgpr_write_b32 a107, v203\n"
      "v_mfma_f32_16x16x32_bf16 v[56:59], v[168:171], v[140:143], v[56:59]\n"
      "s_add_u32 s80, s66, 896\n"
      "s_addc_u32 s81, s67, 0\n"
      "s_add_u32 m0, s82, 16384\n"
      "s_nop 0\n"
      "global_load_lds_dwordx4 v228, s[80:81] offset:0\n"
      "v_mfma_f32_16x16x32_bf16 v[60:63], v[172:175], v[140:143], v[60:63]\n"
      "ds_read_b64_tr_b16 v[186:187], v242 offset:8960\n"
      "v_mfma_f32_16x16x32_bf16 v[212:215], v[164:167], v[250:253], v[212:215]\n"
      "ds_read_b128 v[140:143], v241 offset:6144\n"
      "v_mfma_f32_16x16x32_bf16 v[64:67], v[160:163], v[144:147], v[64:67]\n"
      "s_add_u32 s84, s72, 28672\n s_addc_u32 s85, s73, 0\n"
      "v_mfma_f32_16x16x32_bf16 v[68:71], v[164:167], v[144:147], v[68:71]\n"
      "v_accvgpr_read_b32 v192, a112\n"
      "v_accvgpr_read_b32 v193, a113\n"
      "v_mfma_f32_16x16x32_bf16 v[72:75], v[168:171], v[144:147], v[72:75]\n"
      "ds_read_b64_tr_b16 v[188:189], v243 offset:8704\n"
      "v_accvgpr_read_b32 v194, a114\n"
      "v_mfma_f32_16x16x32_bf16 v[76:79], v[172:175], v[144:147], v[76:79]\n"
      "ds_read_b128 v[144:147], v241 offset:8192\n"
      "global_load_lds_dwordx4 v229, s[80:81] offset:1024\n"
      "v_mfma_f32_16x16x32_bf16 v[80:83], v[160:163], v[148:151], v[80:83]\n"
      "v_accvgpr_read_b32 v195, a115\n"
      "v_accvgpr_read_b32 v196, a116\n"
      "v_mfma_f32_16x16x32_bf16 v[84:87], v[164:167], v[148:151], v[84:87]\n"
      "ds_read_b64_tr_b16 v[190:191], v243 offset:8960\n"
      "v_accvgpr_read_b32 v197, a117\n"
      "v_mfma_f32_16x16x32_bf16 v[88:91], v[168:171], v[148:151], v[88:91]\n"
      "v_accvgpr_read_b32 v198, a118\n"
      "v_mfma_f32_16x16x32_bf16 v[92:95], v[172:175], v[148:151], v[92:95]\n"
      "v_accvgpr_read_b32 v199, a119\n"
      "v_mfma_f32_16x16x32_bf16 v[216:219], v[168:171], v[250:253], v[216:219]\n"
      "ds_read_b128 v[148:151], v241 offset:10240\n"
      "v_cvt_pk_bf16_f32 v192, v192, v193\n"
      "v_mfma_f32_16x16x32_bf16 v[96:99], v[160:163], v[152:155], v[96:99]\n"
      "global_load_lds_dwordx4 v230, s[80:81] offset:2048\n"
      "v_mfma_f32_16x16x32_bf16 v[100:103], v[164:167], v[152:155], v[100:103]\n"
      "v_cvt_pk_bf16_f32 v193, v194, v195\n"
      "v_mfma_f32_16x16x32_bf16 v[104:107], v[168:171], v[152:155], v[104:107]\n"
      "v_cvt_pk_bf16_f32 v194, v196, v197\n"
      "v_mfma_f32_16x16x32_bf16 v[108:111], v[172:175], v[152:155], v[108:111]\n"
      "ds_read_b128 v[152:155], v241 offset:12288\n"
      "v_cvt_pk_bf16_f32 v195, v198, v199\n"
      "v_mfma_f32_16x16x32_bf16 v[112:115], v[160:163], v[156:159], v[112:115]\n"
      "global_store_dwordx4 v246, v[192:195], s[84:85] offset:0 sc1\n"
      "v_mfma_f32_16x16x32_bf16 v[116:119], v[164:167], v[156:159], v[116:119]\n"
      "v_accvgpr_write_b32 a112, v192\n"
      "v_mfma_f32_16x16x32_bf16 v[120:123], v[168:171], v[156:159], v[120:123]\n"
      "global_load_lds_dwordx4 v231, s[80:81] offset:3072\n"
      "v_mfma_f32_16x16x32_bf16 v[124:127], v[172:175], v[156:159], v[124:127]\n"
      "v_accvgpr_write_b32 a113, v193\n"
      "v_mfma_f32_16x16x32_bf16 v[220:223], v[172:175], v[250:253], v[220:223]\n"
      "ds_read_b128 v[156:159], v241 offset:14336\n"
      "v_accvgpr_write_b32 a114, v194\n"
      "s_waitcnt lgkmcnt(7)\n"
      "v_mfma_f32_16x16x32_bf16 v[0:3], v[176:179], v[128:131], v[0:3]\n"
      "v_mfma_f32_16x16x32_bf16 v[4:7], v[180:183], v[128:131], v[4:7]\n"
      "v_mfma_f32_16x16x32_bf16 v[8:11], v[184:187], v[128:131], v[8:11]\n"
      "s_waitcnt lgkmcnt(0)\n"
      "v_mfma_f32_16x16x32_bf16 v[12:15], v[188:191], v[128:131], v[12:15]\n"
      "s_waitcnt vmcnt(21) lgkmcnt(0)\n"
      "s_barrier\n"
      "v_add_u32 v240, 81920, v236\n"
      "v_add_u32 v241, 81920, v237\n"
      "v_add_u32 v242, 114688, v238\n"
      "v_add_u32 v243, 114688, v239\n"
      "ds_read_b128 v[128:131], v240 offset:0\n"
      "v_mfma_f32_16x16x32_bf16 v[16:19], v[176:179], v[132:135], v[16:19]\n"
      "ds_read_b64_tr_b16 v[160:161], v242 offset:0\n"
      "v_accvgpr_write_b32 a115, v195\n"
      "v_mfma_f32_16x16x32_bf16 v[20:23], v[180:183], v[132:135], v[20:23]\n"
      "v_accvgpr_read_b32 v200, a120\n"
      "v_accvgpr_read_b32 v201, a121\n"
      "v_mfma_f32_16x16x32_bf16 v[24:27], v[184:187], v[132:135], v[24:27]\n"
      "s_add_u32 s80, s68, 896\n"
      "s_addc_u32 s81, s69, 0\n"
      "s_add_u32 m0, s82, 32768\n"
      "s_nop 0\n"
      "global_load_lds_dwordx4 v228, s[80:81] offset:0\n"
      "v_mfma_f32_16x16x32_bf16 v[28:31], v[188:191], v[132:135], v[28:31]\n"
      "ds_read_b64_tr_b16 v[162:163], v242 offset:256\n"
      "v_accvgpr_read_b32 v202, a122\n"
      "v_mfma_f32_16x16x32_bf16 v[208:211], v[176:179], v[250:253], v[208:211]\n"
      "ds_read_b128 v[132:135], v240 offset:2048\n"
      "v_accvgpr_read_b32 v203, a123\n"
      "v_mfma_f32_16x16x32_bf16 v[32:35], v[176:179], v[136:139], v[32:35]\n"
      "v_accvgpr_read_b32 v204, a124\n"
      "v_accvgpr_read_b32 v205, a125\n"
      "v_mfma_f32_16x16x32_bf16 v[36:39], v[180:183], v[136:139], v[36:39]\n"
      "ds_read_b64_tr_b16 v[164:165], v243 offset:0\n"
      "v_accvgpr_read_b32 v206, a126\n"
      "v_mfma_f32_16x16x32_bf16 v[40:43], v[184:187], v[136:139], v[40:43]\n"
      "v_accvgpr_read_b32 v207, a127\n"
      "v_cvt_pk_bf16_f32 v200, v200, v201\n"
      "v_mfma_f32_16x16x32_bf16 v[44:47], v[188:191], v[136:139], v[44:47]\n"
      "ds_read_b128 v[136:139], v240 offset:4096\n"
      "global_load_lds_dwordx4 v229, s[80:81] offset:1024\n"
      "v_mfma_f32_16x16x32_bf16 v[48:51], v[176:179], v[140:143], v[48:51]\n"
      "ds_read_b64_tr_b16 v[166:167], v243 offset:256\n"
      "v_cvt_pk_bf16_f32 v201, v202, v203\n"
      "v_mfma_f32_16x16x32_bf16 v[52:55], v[180:183], v[140:143], v[52:55]\n"
      "v_cvt_pk_bf16_f32 v202, v204, v205\n"
      "v_mfma_f32_16x16x32_bf16 v[56:59], v[184:187], v[140:143], v[56:59]\n"
      "v_cvt_pk_bf16_f32 v203, v206, v207\n"
      "v_mfma_f32_16x16x32_bf16 v[60:63], v[188:191], v[140:143], v[60:63]\n"
      "ds_read_b64_tr_b16 v[168:169], v242 offset:512\n"
      "v_mfma_f32_16x16x32_bf16 v[212:215], v[180:183], v[250:253], v[212:215]\n"
      "ds_read_b128 v[140:143], v240 offset:6144\n"
      "global_load_lds_dwordx4 v230, s[80:81] offset:2048\n"
      "v_mfma_f32_16x16x32_bf16 v[64:67], v[176:179], v[144:147], v[64:67]\n"
      "global_store_dwordx4 v246, v[200:203], s[84:85] offset:1024 sc1\n"
      "v_mfma_f32_16x16x32_bf16 v[68:71], v[180:183], v[144:147], v[68:71]\n"
      "ds_read_b64_tr_b16 v[170:171], v242 offset:768\n"
      "v_accvgpr_write_b32 a120, v200\n"
      "v_mfma_f32_16x16x32_bf16 v[72:75], v[184:187], v[144:147], v[72:75]\n"
      "v_accvgpr_write_b32 a121, v201\n"
      "v_accvgpr_write_b32 a122, v202\n"
      "v_mfma_f32_16x16x32_bf16 v[76:79], v[188:191], v[144:147], v[76:79]\n"
      "ds_read_b128 v[144:147], v240 offset:8192\n"
      "v_accvgpr_write_b32 a123, v203\n"
      "v_mfma_f32_16x16x32_bf16 v[80:83], v[176:179], v[148:151], v[80:83]\n"
      "ds_read_b64_tr_b16 v[172:173], v243 offset:512\n"
      "global_load_lds_dwordx4 v231, s[80:81] offset:3072\n"
      "v_mfma_f32_16x16x32_bf16 v[84:87], v[180:183], v[148:151], v[84:87]\n"
      "s_add_u32 s84, s74, 0\n s_addc_u32 s85, s75, 0\n"
      "v_mfma_f32_16x16x32_bf16 v[88:91], v[184:187], v[148:151], v[88:91]\n"
      "v_accvgpr_read_b32 v192, a128\n"
      "v_accvgpr_read_b32 v193, a129\n"
      "v_mfma_f32_16x16x32_bf16 v[92:95], v[188:191], v[148:151], v[92:95]\n"
      "ds_read_b64_tr_b16 v[174:175], v243 offset:768\n"
      "v_accvgpr_read_b32 v194, a130\n"
      "v_mfma_f32_16x16x32_bf16 v[216:219], v[184:187], v[250:253], v[216:219]\n"
      "ds_read_b128 v[148:151], v240 offset:10240\n"
      "v_accvgpr_read_b32 v195, a131\n"
      "v_mfma_f32_16x16x32_bf16 v[96:99], v[176:179], v[152:155], v[96:99]\n"
      "v_accvgpr_read_b32 v196, a132\n"
      "v_accvgpr_read_b32 v197, a133\n"
      "v_mfma_f32_16x16x32_bf16 v[100:103], v[180:183], v[152:155], v[100:103]\n"
      "s_add_u32 s80, s70, 3670016\n"
      "s_addc_u32 s81, s71, 0\n"
      "s_add_u32 m0, s82, 49152\n"
      "s_nop 0\n"
      "global_load_lds_dwordx4 v232, s[80:81] offset:0\n"
      "v_mfma_f32_16x16x32_bf16 v[104:107], v[184:187], v[152:155], v[104:107]\n"
      "v_accvgpr_read_b32 v198, a134\n"
      "v_accvgpr_read_b32 v199, a135\n"
      "v_mfma_f32_16x16x32_bf16 v[108:111], v[188:191], v[152:155], v[108:111]\n"
      "ds_read_b128 v[152:155], v240 offset:12288\n"
      "v_cvt_pk_bf16_f32 v192, v192, v193\n"
      "v_mfma_f32_16x16x32_bf16 v[112:115], v[176:179], v[156:159], v[112:115]\n"
      "v_cvt_pk_bf16_f32 v193, v194, v195\n"
      "v_mfma_f32_16x16x32_bf16 v[116:119], v[180:183], v[156:159], v[116:119]\n"
      "v_cvt_pk_bf16_f32 v194, v196, v197\n"
      "v_mfma_f32_16x16x32_bf16 v[120:123], v[184:187], v[156:159], v[120:123]\n"
      "global_load_lds_dwordx4 v233, s[80:81] offset:1024\n"
      "v_mfma_f32_16x16x32_bf16 v[124:127], v[188:191], v[156:159], v[124:127]\n"
      "v_cvt_pk_bf16_f32 v195, v198, v199\n"
      "v_mfma_f32_16x16x32_bf16 v[220:223], v[188:191], v[250:253], v[220:223]\n"
      "ds_read_b128 v[156:159], v240 offset:14336\n"
      "global_store_dwordx4 v247, v[192:195], s[84:85] offset:0 sc1\n"
      "s_waitcnt lgkmcnt(7)\n"
      "v_mfma_f32_16x16x32_bf16 v[0:3], v[160:163], v[128:131], v[0:3]\n"
      "ds_read_b64_tr_b16 v[176:177], v242 offset:8192\n"
      "v_accvgpr_write_b32 a128, v192\n"
      "v_mfma_f32_16x16x32_bf16 v[4:7], v[164:167], v[128:131], v[4:7]\n"
      "v_accvgpr_write_b32 a129, v193\n"
      "v_accvgpr_write_b32 a130, v194\n"
      "s_waitcnt lgkmcnt(1)\n"
      "v_mfma_f32_16x16x32_bf16 v[8:11], v[168:171], v[128:131], v[8:11]\n"
      "v_accvgpr_write_b32 a131, v195\n"
      "v_accvgpr_read_b32 v200, a136\n"
      "v_mfma_f32_16x16x32_bf16 v[12:15], v[172:175], v[128:131], v[12:15]\n"
      "ds_read_b128 v[128:131], v241 offset:0\n"
      "ds_read_b64_tr_b16 v[178:179], v242 offset:8448\n"
      "global_load_lds_dwordx4 v234, s[80:81] offset:2048\n"
      "v_mfma_f32_16x16x32_bf16 v[16:19], v[160:163], v[132:135], v[16:19]\n"
      "v_accvgpr_read_b32 v201, a137\n"
      "v_accvgpr_read_b32 v202, a138\n"
      "v_mfma_f32_16x16x32_bf16 v[20:23], v[164:167], v[132:135], v[20:23]\n"
      "v_accvgpr_read_b32 v203, a139\n"
      "v_accvgpr_read_b32 v204, a140\n"
      "v_mfma_f32_16x16x32_bf16 v[24:27], v[168:171], v[132:135], v[24:27]\n"
      "ds_read_b64_tr_b16 v[180:181], v243 offset:8192\n"
      "v_accvgpr_read_b32 v205, a141\n"
      "v_mfma_f32_16x16x32_bf16 v[28:31], v[172:175], v[132:135], v[28:31]\n"
      "v_accvgpr_read_b32 v206, a142\n"
      "v_accvgpr_read_b32 v207, a143\n"
      "v_mfma_f32_16x16x32_bf16 v[208:211], v[160:163], v[250:253], v[208:211]\n"
      "ds_read_b128 v[132:135], v241 offset:2048\n"
      "v_cvt_pk_bf16_f32 v200, v200, v201\n"
      "v_mfma_f32_16x16x32_bf16 v[32:35], v[160:163], v[136:139], v[32:35]\n"
      "global_load_lds_dwordx4 v235, s[80:81] offset:3072\n"
      "v_mfma_f32_16x16x32_bf16 v[36:39], v[164:167], v[136:139], v[36:39]\n"
      "ds_read_b64_tr_b16 v[182:183], v243 offset:8448\n"
      "v_cvt_pk_bf16_f32 v201, v202, v203\n"
      "v_mfma_f32_16x16x32_bf16 v[40:43], v[168:171], v[136:139], v[40:43]\n"
      "v_cvt_pk_bf16_f32 v202, v204, v205\n"
      "v_mfma_f32_16x16x32_bf16 v[44:47], v[172:175], v[136:139], v[44:47]\n"
      "ds_read_b128 v[136:139], v241 offset:4096\n"
      "v_cvt_pk_bf16_f32 v203, v206, v207\n"
      "v_mfma_f32_16x16x32_bf16 v[48:51], v[160:163], v[140:143], v[48:51]\n"
      "ds_read_b64_tr_b16 v[184:185], v242 offset:8704\n"
      "v_mfma_f32_16x16x32_bf16 v[52:55], v[164:167], v[140:143], v[52:55]\n"
      "global_store_dwordx4 v247, v[200:203], s[84:85] offset:1024 sc1\n"
      "v_mfma_f32_16x16x32_bf16 v[56:59], v[168:171], v[140:143], v[56:59]\n"
      "s_add_u32 s80, s66, 1024\n"
      "s_addc_u32 s81, s67, 0\n"
      "s_add_u32 m0, s82, 65536\n"
      "s_nop 0\n"
      "global_load_lds_dwordx4 v228, s[80:81] offset:0\n"
      "v_mfma_f32_16x16x32_bf16 v[60:63], v[172:175], v[140:143], v[60:63]\n"
      "ds_read_b64_tr_b16 v[186:187], v242 offset:8960\n"
      "v_accvgpr_write_b32 a136, v200\n"
      "v_mfma_f32_16x16x32_bf16 v[212:215], v[164:167], v[250:253], v[212:215]\n"
      "ds_read_b128 v[140:143], v241 offset:6144\n"
      "v_accvgpr_write_b32 a137, v201\n"
      "v_mfma_f32_16x16x32_bf16 v[64:67], v[160:163], v[144:147], v[64:67]\n"
      "v_accvgpr_write_b32 a138, v202\n"
      "v_mfma_f32_16x16x32_bf16 v[68:71], v[164:167], v[144:147], v[68:71]\n"
      "v_accvgpr_write_b32 a139, v203\n"
      "v_mfma_f32_16x16x32_bf16 v[72:75], v[168:171], v[144:147], v[72:75]\n"
      "ds_read_b64_tr_b16 v[188:189], v243 offset:8704\n"
      "v_accvgpr_read_b32 v192, a160\n"
      "v_mfma_f32_16x16x32_bf16 v[76:79], v[172:175], v[144:147], v[76:79]\n"
      "ds_read_b128 v[144:147], v241 offset:8192\n"
      "global_load_lds_dwordx4 v229, s[80:81] offset:1024\n"
      "v_mfma_f32_16x16x32_bf16 v[80:83], v[160:163], v[148:151], v[80:83]\n"
      "v_accvgpr_read_b32 v193, a161\n"
      "v_mfma_f32_16x16x32_bf16 v[84:87], v[164:167], v[148:151], v[84:87]\n"
      "ds_read_b64_tr_b16 v[190:191], v243 offset:8960\n"
      "v_accvgpr_read_b32 v194, a162\n"
      "v_mfma_f32_16x16x32_bf16 v[88:91], v[168:171], v[148:151], v[88:91]\n"
      "v_accvgpr_read_b32 v195, a163\n"
      "v_mfma_f32_16x16x32_bf16 v[92:95], v[172:175], v[148:151], v[92:95]\n"
      "v_accvgpr_read_b32 v196, a164\n"
      "v_mfma_f32_16x16x32_bf16 v[216:219], v[168:171], v[250:253], v[216:219]\n"
      "ds_read_b128 v[148:151], v241 offset:10240\n"
      "v_accvgpr_read_b32 v197, a165\n"
      "v_mfma_f32_16x16x32_bf16 v[96:99], v[160:163], v[152:155], v[96:99]\n"
      "global_load_lds_dwordx4 v230, s[80:81] offset:2048\n"
      "v_mfma_f32_16x16x32_bf16 v[100:103], v[164:167], v[152:155], v[100:103]\n"
      "v_accvgpr_read_b32 v198, a166\n"
      "v_accvgpr_read_b32 v199, a167\n"
      "v_mfma_f32_16x16x32_bf16 v[104:107], v[168:171], v[152:155], v[104:107]\n"
      "v_cvt_pk_bf16_f32 v192, v192, v193\n"
      "v_mfma_f32_16x16x32_bf16 v[108:111], v[172:175], v[152:155], v[108:111]\n"
      "ds_read_b128 v[152:155], v241 offset:12288\n"
      "v_cvt_pk_bf16_f32 v193, v194, v195\n"
      "v_mfma_f32_16x16x32_bf16 v[112:115], v[160:163], v[156:159], v[112:115]\n"
      "v_cvt_pk_bf16_f32 v194, v196, v197\n"
      "v_mfma_f32_16x16x32_bf16 v[116:119], v[164:167], v[156:159], v[116:119]\n"
      "v_cvt_pk_bf16_f32 v195, v198, v199\n"
      "v_mfma_f32_16x16x32_bf16 v[120:123], v[168:171], v[156:159], v[120:123]\n"
      "global_load_lds_dwordx4 v231, s[80:81] offset:3072\n"
      "v_mfma_f32_16x16x32_bf16 v[124:127], v[172:175], v[156:159], v[124:127]\n"
      "global_store_dwordx4 v247, v[192:195], s[84:85] offset:2048 sc1\n"
      "v_mfma_f32_16x16x32_bf16 v[220:223], v[172:175], v[250:253], v[220:223]\n"
      "ds_read_b128 v[156:159], v241 offset:14336\n"
      "s_waitcnt lgkmcnt(7)\n"
      "v_mfma_f32_16x16x32_bf16 v[0:3], v[176:179], v[128:131], v[0:3]\n"
      "v_mfma_f32_16x16x32_bf16 v[4:7], v[180:183], v[128:131], v[4:7]\n"
      "v_mfma_f32_16x16x32_bf16 v[8:11], v[184:187], v[128:131], v[8:11]\n"
      "s_waitcnt lgkmcnt(0)\n"
      "v_mfma_f32_16x16x32_bf16 v[12:15], v[188:191], v[128:131], v[12:15]\n"
      "s_waitcnt vmcnt(22) lgkmcnt(0)\n"
      "s_barrier\n"
      "v_add_u32 v240, 131072, v236\n"
      "v_add_u32 v241, 131072, v237\n"
      "v_add_u32 v242, 0, v238\n"
      "v_add_u32 v243, 0, v239\n"
      "ds_read_b128 v[128:131], v240 offset:0\n"
      "v_mfma_f32_16x16x32_bf16 v[16:19], v[176:179], v[132:135], v[16:19]\n"
      "ds_read_b64_tr_b16 v[160:161], v242 offset:0\n"
      "v_accvgpr_write_b32 a160, v192\n"
      "v_mfma_f32_16x16x32_bf16 v[20:23], v[180:183], v[132:135], v[20:23]\n"
      "v_accvgpr_write_b32 a161, v193\n"
      "v_accvgpr_write_b32 a162, v194\n"
      "v_mfma_f32_16x16x32_bf16 v[24:27], v[184:187], v[132:135], v[24:27]\n"
      "s_add_u32 s80, s68, 1024\n"
      "s_addc_u32 s81, s69, 0\n"
      "s_add_u32 m0, s82, 81920\n"
      "s_nop 0\n"
      "global_load_lds_dwordx4 v228, s[80:81] offset:0\n"
      "v_mfma_f32_16x16x32_bf16 v[28:31], v[188:191], v[132:135], v[28:31]\n"
      "ds_read_b64_tr_b16 v[162:163], v242 offset:256\n"
      "v_accvgpr_write_b32 a163, v195\n"
      "v_mfma_f32_16x16x32_bf16 v[208:211], v[176:179], v[250:253], v[208:211]\n"
      "ds_read_b128 v[132:135], v240 offset:2048\n"
      "v_accvgpr_read_b32 v200, a168\n"
      "v_mfma_f32_16x16x32_bf16 v[32:35], v[176:179], v[136:139], v[32:35]\n"
      "v_accvgpr_read_b32 v201, a169\n"
      "v_accvgpr_read_b32 v202, a170\n"
      "v_mfma_f32_16x16x32_bf16 v[36:39], v[180:183], v[136:139], v[36:39]\n"
      "ds_read_b64_tr_b16 v[164:165], v243 offset:0\n"
      "v_accvgpr_read_b32 v203, a171\n"
      "v_mfma_f32_16x16x32_bf16 v[40:43], v[184:187], v[136:139], v[40:43]\n"
      "v_accvgpr_read_b32 v204, a172\n"
      "v_accvgpr_read_b32 v205, a173\n"
      "v_mfma_f32_16x16x32_bf16 v[44:47], v[188:191], v[136:139], v[44:47]\n"
      "ds_read_b128 v[136:139], v240 offset:4096\n"
      "global_load_lds_dwordx4 v229, s[80:81] offset:1024\n"
      "v_mfma_f32_16x16x32_bf16 v[48:51], v[176:179], v[140:143], v[48:51]\n"
      "ds_read_b64_tr_b16 v[166:167], v243 offset:256\n"
      "v_accvgpr_read_b32 v206, a174\n"
      "v_mfma_f32_16x16x32_bf16 v[52:55], v[180:183], v[140:143], v[52:55]\n"
      "v_accvgpr_read_b32 v207, a175\n"
      "v_cvt_pk_bf16_f32 v200, v200, v201\n"
      "v_mfma_f32_16x16x32_bf16 v[56:59], v[184:187], v[140:143], v[56:59]\n"
      "v_cvt_pk_bf16_f32 v201, v202, v203\n"
      "v_mfma_f32_16x16x32_bf16 v[60:63], v[188:191], v[140:143], v[60:63]\n"
      "ds_read_b64_tr_b16 v[168:169], v242 offset:512\n"
      "v_cvt_pk_bf16_f32 v202, v204, v205\n"
      "v_mfma_f32_16x16x32_bf16 v[212:215], v[180:183], v[250:253], v[212:215]\n"
      "ds_read_b128 v[140:143], v240 offset:6144\n"
      "global_load_lds_dwordx4 v230, s[80:81] offset:2048\n"
      "v_mfma_f32_16x16x32_bf16 v[64:67], v[176:179], v[144:147], v[64:67]\n"
      "v_cvt_pk_bf16_f32 v203, v206, v207\n"
      "v_mfma_f32_16x16x32_bf16 v[68:71], v[180:183], v[144:147], v[68:71]\n"
      "ds_read_b64_tr_b16 v[170:171], v242 offset:768\n"
      "v_mfma_f32_16x16x32_bf16 v[72:75], v[184:187], v[144:147], v[72:75]\n"
      "global_store_dwordx4 v247, v[200:203], s[84:85] offset:3072 sc1\n"
      "v_mfma_f32_16x16x32_bf16 v[76:79], v[188:191], v[144:147], v[76:79]\n"
      "ds_read_b128 v[144:147], v240 offset:8192\n"
      "v_accvgpr_write_b32 a168, v200\n"
      "v_mfma_f32_16x16x32_bf16 v[80:83], v[176:179], v[148:151], v[80:83]\n"
      "ds_read_b64_tr_b16 v[172:173], v243 offset:512\n"
      "global_load_lds_dwordx4 v231, s[80:81] offset:3072\n"
      "v_mfma_f32_16x16x32_bf16 v[84:87], v[180:183], v[148:151], v[84:87]\n"
      "v_accvgpr_write_b32 a169, v201\n"
      "v_accvgpr_write_b32 a170, v202\n"
      "v_mfma_f32_16x16x32_bf16 v[88:91], v[184:187], v[148:151], v[88:91]\n"
      "v_accvgpr_write_b32 a171, v203\n"
      "v_mfma_f32_16x16x32_bf16 v[92:95], v[188:191], v[148:151], v[92:95]\n"
      "ds_read_b64_tr_b16 v[174:175], v243 offset:768\n"
      "v_mfma_f32_16x16x32_bf16 v[216:219], v[184:187], v[250:253], v[216:219]\n"
      "ds_read_b128 v[148:151], v240 offset:10240\n"
      "s_add_u32 s84, s74, 4096\n s_addc_u32 s85, s75, 0\n"
      "v_mfma_f32_16x16x32_bf16 v[96:99], v[176:179], v[152:155], v[96:99]\n"
      "v_accvgpr_read_b32 v192, a192\n"
      "v_accvgpr_read_b32 v193, a193\n"
      "v_mfma_f32_16x16x32_bf16 v[100:103], v[180:183], v[152:155], v[100:103]\n"
      "s_add_u32 s80, s70, 4194304\n"
      "s_addc_u32 s81, s71, 0\n"
      "s_add_u32 m0, s82, 98304\n"
      "s_nop 0\n"
      "global_load_lds_dwordx4 v232, s[80:81] offset:0\n"
      "v_mfma_f32_16x16x32_bf16 v[104:107], v[184:187], v[152:155], v[104:107]\n"
      "v_accvgpr_read_b32 v194, a194\n"
      "v_accvgpr_read_b32 v195, a195\n"
      "v_mfma_f32_16x16x32_bf16 v[108:111], v[188:191], v[152:155], v[108:111]\n"
      "ds_read_b128 v[152:155], v240 offset:12288\n"
      "v_accvgpr_read_b32 v196, a196\n"
      "v_mfma_f32_16x16x32_bf16 v[112:115], v[176:179], v[156:159], v[112:115]\n"
      "v_accvgpr_read_b32 v197, a197\n"
      "v_accvgpr_read_b32 v198, a198\n"
      "v_mfma_f32_16x16x32_bf16 v[116:119], v[180:183], v[156:159], v[116:119]\n"
      "v_accvgpr_read_b32 v199, a199\n"
      "v_cvt_pk_bf16_f32 v192, v192, v193\n"
      "v_mfma_f32_16x16x32_bf16 v[120:123], v[184:187], v[156:159], v[120:123]\n"
      "global_load_lds_dwordx4 v233, s[80:81] offset:1024\n"
      "v_mfma_f32_16x16x32_bf16 v[124:127], v[188:191], v[156:159], v[124:127]\n"
      "v_cvt_pk_bf16_f32 v193, v194, v195\n"
      "v_mfma_f32_16x16x32_bf16 v[220:223], v[188:191], v[250:253], v[220:223]\n"
      "ds_read_b128 v[156:159], v240 offset:14336\n"
      "v_cvt_pk_bf16_f32 v194, v196, v197\n"
      "s_waitcnt lgkmcnt(7)\n"
      "v_mfma_f32_16x16x32_bf16 v[0:3], v[160:163], v[128:131], v[0:3]\n"
      "ds_read_b64_tr_b16 v[176:177], v242 offset:8192\n"
      "v_cvt_pk_bf16_f32 v195, v198, v199\n"
      "v_mfma_f32_16x16x32_bf16 v[4:7], v[164:167], v[128:131], v[4:7]\n"
      "global_store_dwordx4 v247, v[192:195], s[84:85] offset:0 sc1\n"
      "s_waitcnt lgkmcnt(1)\n"
      "v_mfma_f32_16x16x32_bf16 v[8:11], v[168:171], v[128:131], v[8:11]\n"
      "v_accvgpr_write_b32 a192, v192\n"
      "v_accvgpr_write_b32 a193, v193\n"
      "v_mfma_f32_16x16x32_bf16 v[12:15], v[172:175], v[128:131], v[12:15]\n"
      "ds_read_b128 v[128:131], v241 offset:0\n"
      "ds_read_b64_tr_b16 v[178:179], v242 offset:8448\n"
      "global_load_lds_dwordx4 v234, s[80:81] offset:2048\n"
      "v_mfma_f32_16x16x32_bf16 v[16:19], v[160:163], v[132:135], v[16:19]\n"
      "v_accvgpr_write_b32 a194, v194\n"
      "v_accvgpr_write_b32 a195, v195\n"
      "v_mfma_f32_16x16x32_bf16 v[20:23], v[164:167], v[132:135], v[20:23]\n"
      "v_accvgpr_read_b32 v200, a200\n"
      "v_mfma_f32_16x16x32_bf16 v[24:27], v[168:171], v[132:135], v[24:27]\n"
      "ds_read_b64_tr_b16 v[180:181], v243 offset:8192\n"
      "v_accvgpr_read_b32 v201, a201\n"
      "v_mfma_f32_16x16x32_bf16 v[28:31], v[172:175], v[132:135], v[28:31]\n"
      "v_accvgpr_read_b32 v202, a202\n"
      "v_mfma_f32_16x16x32_bf16 v[208:211], v[160:163], v[250:253], v[208:211]\n"
      "ds_read_b128 v[132:135], v241 offset:2048\n"
      "v_accvgpr_read_b32 v203, a203\n"
      "v_mfma_f32_16x16x32_bf16 v[32:35], v[160:163], v[136:139], v[32:35]\n"
      "global_load_lds_dwordx4 v235, s[80:81] offset:3072\n"
      "v_mfma_f32_16x16x32_bf16 v[36:39], v[164:167], v[136:139], v[36:39]\n"
      "ds_read_b64_tr_b16 v[182:183], v243 offset:8448\n"
      "v_accvgpr_read_b32 v204, a204\n"
      "v_mfma_f32_16x16x32_bf16 v[40:43], v[168:171], v[136:139], v[40:43]\n"
      "v_accvgpr_read_b32 v205, a205\n"
      "v_mfma_f32_16x16x32_bf16 v[44:47], v[172:175], v[136:139], v[44:47]\n"
      "ds_read_b128 v[136:139], v241 offset:4096\n"
      "v_accvgpr_read_b32 v206, a206\n"
      "v_mfma_f32_16x16x32_bf16 v[48:51], v[160:163], v[140:143], v[48:51]\n"
      "ds_read_b64_tr_b16 v[184:185], v242 offset:8704\n"
      "v_accvgpr_read_b32 v207, a207\n"
      "v_mfma_f32_16x16x32_bf16 v[52:55], v[164:167], v[140:143], v[52:55]\n"
      "v_cvt_pk_bf16_f32 v200, v200, v201\n"
      "v_mfma_f32_16x16x32_bf16 v[56:59], v[168:171], v[140:143], v[56:59]\n"
      "s_add_u32 s80, s66, 1152\n"
      "s_addc_u32 s81, s67, 0\n"
      "s_add_u32 m0, s82, 114688\n"
      "s_nop 0\n"
      "global_load_lds_dwordx4 v228, s[80:81] offset:0\n"
      "v_mfma_f32_16x16x32_bf16 v[60:63], v[172:175], v[140:143], v[60:63]\n"
      "ds_read_b64_tr_b16 v[186:187], v242 offset:8960\n"
      "v_cvt_pk_bf16_f32 v201, v202, v203\n"
      "v_mfma_f32_16x16x32_bf16 v[212:215], v[164:167], v[250:253], v[212:215]\n"
      "ds_read_b128 v[140:143], v241 offset:6144\n"
      "v_cvt_pk_bf16_f32 v202, v204, v205\n"
      "v_mfma_f32_16x16x32_bf16 v[64:67], v[160:163], v[144:147], v[64:67]\n"
      "v_cvt_pk_bf16_f32 v203, v206, v207\n"
      "v_mfma_f32_16x16x32_bf16 v[68:71], v[164:167], v[144:147], v[68:71]\n"
      "global_store_dwordx4 v247, v[200:203], s[84:85] offset:1024 sc1\n"
      "v_mfma_f32_16x16x32_bf16 v[72:75], v[168:171], v[144:147], v[72:75]\n"
      "ds_read_b64_tr_b16 v[188:189], v243 offset:8704\n"
      "v_accvgpr_write_b32 a200, v200\n"
      "v_mfma_f32_16x16x32_bf16 v[76:79], v[172:175], v[144:147], v[76:79]\n"
      "ds_read_b128 v[144:147], v241 offset:8192\n"
      "global_load_lds_dwordx4 v229, s[80:81] offset:1024\n"
      "v_mfma_f32_16x16x32_bf16 v[80:83], v[160:163], v[148:151], v[80:83]\n"
      "v_accvgpr_write_b32 a201, v201\n"
      "v_mfma_f32_16x16x32_bf16 v[84:87], v[164:167], v[148:151], v[84:87]\n"
      "ds_read_b64_tr_b16 v[190:191], v243 offset:8960\n"
      "v_accvgpr_write_b32 a202, v202\n"
      "v_mfma_f32_16x16x32_bf16 v[88:91], v[168:171], v[148:151], v[88:91]\n"
      "v_accvgpr_write_b32 a203, v203\n"
      "v_mfma_f32_16x16x32_bf16 v[92:95], v[172:175], v[148:151], v[92:95]\n"
      "v_accvgpr_read_b32 v192, a224\n"
      "v_mfma_f32_16x16x32_bf16 v[216:219], v[168:171], v[250:253], v[216:219]\n"
      "ds_read_b128 v[148:151], v241 offset:10240\n"
      "v_accvgpr_read_b32 v193, a225\n"
      "v_mfma_f32_16x16x32_bf16 v[96:99], v[160:163], v[152:155], v[96:99]\n"
      "global_load_lds_dwordx4 v230, s[80:81] offset:2048\n"
      "v_mfma_f32_16x16x32_bf16 v[100:103], v[164:167], v[152:155], v[100:103]\n"
      "v_accvgpr_read_b32 v194, a226\n"
      "v_mfma_f32_16x16x32_bf16 v[104:107], v[168:171], v[152:155], v[104:107]\n"
      "v_accvgpr_read_b32 v195, a227\n"
      "v_accvgpr_read_b32 v196, a228\n"
      "v_mfma_f32_16x16x32_bf16 v[108:111], v[172:175], v[152:155], v[108:111]\n"
      "ds_read_b128 v[152:155], v241 offset:12288\n"
      "v_accvgpr_read_b32 v197, a229\n"
      "v_mfma_f32_16x16x32_bf16 v[112:115], v[160:163], v[156:159], v[112:115]\n"
      "v_accvgpr_read_b32 v198, a230\n"
      "v_mfma_f32_16x16x32_bf16 v[116:119], v[164:167], v[156:159], v[116:119]\n"
      "v_accvgpr_read_b32 v199, a231\n"
      "v_mfma_f32_16x16x32_bf16 v[120:123], v[168:171], v[156:159], v[120:123]\n"
      "global_load_lds_dwordx4 v231, s[80:81] offset:3072\n"
      "v_mfma_f32_16x16x32_bf16 v[124:127], v[172:175], v[156:159], v[124:127]\n"
      "v_cvt_pk_bf16_f32 v192, v192, v193\n"
      "v_mfma_f32_16x16x32_bf16 v[220:223], v[172:175], v[250:253], v[220:223]\n"
      "ds_read_b128 v[156:159], v241 offset:14336\n"
      "v_cvt_pk_bf16_f32 v193, v194, v195\n"
      "s_waitcnt lgkmcnt(7)\n"
      "v_mfma_f32_16x16x32_bf16 v[0:3], v[176:179], v[128:131], v[0:3]\n"
      "v_mfma_f32_16x16x32_bf16 v[4:7], v[180:183], v[128:131], v[4:7]\n"
      "v_mfma_f32_16x16x32_bf16 v[8:11], v[184:187], v[128:131], v[8:11]\n"
      "s_waitcnt lgkmcnt(0)\n"
      "v_mfma_f32_16x16x32_bf16 v[12:15], v[188:191], v[128:131], v[12:15]\n"
      "s_waitcnt vmcnt(21) lgkmcnt(0)\n"
      "s_barrier\n"
      "v_add_u32 v240, 16384, v236\n"
      "v_add_u32 v241, 16384, v237\n"
      "v_add_u32 v242, 49152, v238\n"
      "v_add_u32 v243, 49152, v239\n"
      "ds_read_b128 v[128:131], v240 offset:0\n"
      "v_mfma_f32_16x16x32_bf16 v[16:19], v[176:179], v[132:135], v[16:19]\n"
      "ds_read_b64_tr_b16 v[160:161], v242 offset:0\n"
      "v_cvt_pk_bf16_f32 v194, v196, v197\n"
      "v_mfma_f32_16x16x32_bf16 v[20:23], v[180:183], v[132:135], v[20:23]\n"
      "v_cvt_pk_bf16_f32 v195, v198, v199\n"
      "v_mfma_f32_16x16x32_bf16 v[24:27], v[184:187], v[132:135], v[24:27]\n"
      "global_store_dwordx4 v247, v[192:195], s[84:85] offset:2048 sc1\n"
      "v_mfma_f32_16x16x32_bf16 v[28:31], v[188:191], v[132:135], v[28:31]\n"
      "ds_read_b64_tr_b16 v[162:163], v242 offset:256\n"
      "v_accvgpr_write_b32 a224, v192\n"
      "v_mfma_f32_16x16x32_bf16 v[208:211], v[176:179], v[250:253], v[208:211]\n"
      "ds_read_b128 v[132:135], v240 offset:2048\n"
      "s_add_u32 s80, s68, 1152\n"
      "s_addc_u32 s81, s69, 0\n"
      "s_add_u32 m0, s82, 131072\n"
      "s_nop 0\n"
      "global_load_lds_dwordx4 v228, s[80:81] offset:0\n"
      "v_mfma_f32_16x16x32_bf16 v[32:35], v[176:179], v[136:139], v[32:35]\n"
      "v_accvgpr_write_b32 a225, v193\n"
      "v_accvgpr_write_b32 a226, v194\n"
      "v_mfma_f32_16x16x32_bf16 v[36:39], v[180:183], v[136:139], v[36:39]\n"
      "ds_read_b64_tr_b16 v[164:165], v243 offset:0\n"
      "v_accvgpr_write_b32 a227, v195\n"
      "v_mfma_f32_16x16x32_bf16 v[40:43], v[184:187], v[136:139], v[40:43]\n"
      "v_accvgpr_read_b32 v200, a232\n"
      "v_accvgpr_read_b32 v201, a233\n"
      "v_mfma_f32_16x16x32_bf16 v[44:47], v[188:191], v[136:139], v[44:47]\n"
      "ds_read_b128 v[136:139], v240 offset:4096\n"
      "v_accvgpr_read_b32 v202, a234\n"
      "v_mfma_f32_16x16x32_bf16 v[48:51], v[176:179], v[140:143], v[48:51]\n"
      "ds_read_b64_tr_b16 v[166:167], v243 offset:256\n"
      "v_accvgpr_read_b32 v203, a235\n"
      "v_mfma_f32_16x16x32_bf16 v[52:55], v[180:183], v[140:143], v[52:55]\n"
      "v_accvgpr_read_b32 v204, a236\n"
      "v_accvgpr_read_b32 v205, a237\n"
      "v_mfma_f32_16x16x32_bf16 v[56:59], v[184:187], v[140:143], v[56:59]\n"
      "v_accvgpr_read_b32 v206, a238\n"
      "v_accvgpr_read_b32 v207, a239\n"
      "v_mfma_f32_16x16x32_bf16 v[60:63], v[188:191], v[140:143], v[60:63]\n"
      "ds_read_b64_tr_b16 v[168:169], v242 offset:512\n"
      "global_load_lds_dwordx4 v229, s[80:81] offset:1024\n"
      "v_mfma_f32_16x16x32_bf16 v[212:215], v[180:183], v[250:253], v[212:215]\n"
      "ds_read_b128 v[140:143], v240 offset:6144\n"
      "v_cvt_pk_bf16_f32 v200, v200, v201\n"
      "v_mfma_f32_16x16x32_bf16 v[64:67], v[176:179], v[144:147], v[64:67]\n"
      "v_cvt_pk_bf16_f32 v201, v202, v203\n"
      "v_mfma_f32_16x16x32_bf16 v[68:71], v[180:183], v[144:147], v[68:71]\n"
      "ds_read_b64_tr_b16 v[170:171], v242 offset:768\n"
      "v_cvt_pk_bf16_f32 v202, v204, v205\n"
      "v_mfma_f32_16x16x32_bf16 v[72:75], v[184:187], v[144:147], v[72:75]\n"
      "v_cvt_pk_bf16_f32 v203, v206, v207\n"
      "v_mfma_f32_16x16x32_bf16 v[76:79], v[188:191], v[144:147], v[76:79]\n"
      "ds_read_b128 v[144:147], v240 offset:8192\n"
      "v_mfma_f32_16x16x32_bf16 v[80:83], v[176:179], v[148:151], v[80:83]\n"
      "ds_read_b64_tr_b16 v[172:173], v243 offset:512\n"
      "v_mfma_f32_16x16x32_bf16 v[84:87], v[180:183], v[148:151], v[84:87]\n"
      "global_store_dwordx4 v247, v[200:203], s[84:85] offset:3072 sc1\n"
      "v_mfma_f32_16x16x32_bf16 v[88:91], v[184:187], v[148:151], v[88:91]\n"
      "global_load_lds_dwordx4 v230, s[80:81] offset:2048\n"
      "v_mfma_f32_16x16x32_bf16 v[92:95], v[188:191], v[148:151], v[92:95]\n"
      "ds_read_b64_tr_b16 v[174:175], v243 offset:768\n"
      "v_accvgpr_write_b32 a232, v200\n"
      "v_mfma_f32_16x16x32_bf16 v[216:219], v[184:187], v[250:253], v[216:219]\n"
      "ds_read_b128 v[148:151], v240 offset:10240\n"
      "v_accvgpr_write_b32 a233, v201\n"
      "v_mfma_f32_16x16x32_bf16 v[96:99], v[176:179], v[152:155], v[96:99]\n"
      "v_accvgpr_write_b32 a234, v202\n"
      "v_accvgpr_write_b32 a235, v203\n"
      "v_mfma_f32_16x16x32_bf16 v[100:103], v[180:183], v[152:155], v[100:103]\n"
      "s_add_u32 s84, s74, 16384\n s_addc_u32 s85, s75, 0\n"
      "v_mfma_f32_16x16x32_bf16 v[104:107], v[184:187], v[152:155], v[104:107]\n"
      "v_accvgpr_read_b32 v192, a144\n"
      "v_accvgpr_read_b32 v193, a145\n"
      "v_mfma_f32_16x16x32_bf16 v[108:111], v[188:191], v[152:155], v[108:111]\n"
      "ds_read_b128 v[152:155], v240 offset:12288\n"
      "v_accvgpr_read_b32 v194, a146\n"
      "v_mfma_f32_16x16x32_bf16 v[112:115], v[176:179], v[156:159], v[112:115]\n"
      "v_accvgpr_read_b32 v195, a147\n"
      "v_mfma_f32_16x16x32_bf16 v[116:119], v[180:183], v[156:159], v[116:119]\n"
      "global_load_lds_dwordx4 v231, s[80:81] offset:3072\n"
      "v_mfma_f32_16x16x32_bf16 v[120:123], v[184:187], v[156:159], v[120:123]\n"
      "v_accvgpr_read_b32 v196, a148\n"
      "v_accvgpr_read_b32 v197, a149\n"
      "v_mfma_f32_16x16x32_bf16 v[124:127], v[188:191], v[156:159], v[124:127]\n"
      "v_accvgpr_read_b32 v198, a150\n"
      "v_mfma_f32_16x16x32_bf16 v[220:223], v[188:191], v[250:253], v[220:223]\n"
      "ds_read_b128 v[156:159], v240 offset:14336\n"
      "v_accvgpr_read_b32 v199, a151\n"
      "s_waitcnt lgkmcnt(7)\n"
      "v_mfma_f32_16x16x32_bf16 v[0:3], v[160:163], v[128:131], v[0:3]\n"
      "ds_read_b64_tr_b16 v[176:177], v242 offset:8192\n"
      "s_lshl_b32 s53, %20, 8\n s_add_u32 s53, s53, 4096\n v_lshl_add_u32 v254, v248, 2, s53\n global_load_dword v254, v254, %15 sc1\n"
      "v_mfma_f32_16x16x32_bf16 v[4:7], v[164:167], v[128:131], v[4:7]\n"
      "v_cvt_pk_bf16_f32 v192, v192, v193\n"
      "s_waitcnt lgkmcnt(1)\n"
      "v_mfma_f32_16x16x32_bf16 v[8:11], v[168:171], v[128:131], v[8:11]\n"
      "v_cvt_pk_bf16_f32 v193, v194, v195\n"
      "v_mfma_f32_16x16x32_bf16 v[12:15], v[172:175], v[128:131], v[12:15]\n"
      "ds_read_b128 v[128:131], v241 offset:0\n"
      "ds_read_b64_tr_b16 v[178:179], v242 offset:8448\n"
      "v_mfma_f32_16x16x32_bf16 v[16:19], v[160:163], v[132:135], v[16:19]\n"
      "s_add_u32 s80, s70, 4718592\n"
      "s_addc_u32 s81, s71, 0\n"
      "s_add_u32 m0, s82, 147456\n"
      "s_nop 0\n"
      "global_load_lds_dwordx4 v232, s[80:81] offset:0\n"
      "v_mfma_f32_16x16x32_bf16 v[20:23], v[164:167], v[132:135], v[20:23]\n"
      "v_cvt_pk_bf16_f32 v194, v196, v197\n"
      "v_mfma_f32_16x16x32_bf16 v[24:27], v[168:171], v[132:135], v[24:27]\n"
      "ds_read_b64_tr_b16 v[180:181], v243 offset:8192\n"
      "v_cvt_pk_bf16_f32 v195, v198, v199\n"
      "v_mfma_f32_16x16x32_bf16 v[28:31], v[172:175], v[132:135], v[28:31]\n"
      "global_store_dwordx4 v247, v[192:195], s[84:85] offset:0 sc1\n"
      "v_mfma_f32_16x16x32_bf16 v[208:211], v[160:163], v[250:253], v[208:211]\n"
      "ds_read_b128 v[132:135], v241 offset:2048\n"
      "v_accvgpr_write_b32 a144, v192\n"
      "v_mfma_f32_16x16x32_bf16 v[32:35], v[160:163], v[136:139], v[32:35]\n"
      "v_accvgpr_write_b32 a145, v193\n"
      "v_accvgpr_write_b32 a146, v194\n"
      "v_mfma_f32_16x16x32_bf16 v[36:39], v[164:167], v[136:139], v[36:39]\n"
      "ds_read_b64_tr_b16 v[182:183], v243 offset:8448\n"
      "v_accvgpr_write_b32 a147, v195\n"
      "v_mfma_f32_16x16x32_bf16 v[40:43], v[168:171], v[136:139], v[40:43]\n"
      "v_accvgpr_read_b32 v200, a152\n"
      "v_mfma_f32_16x16x32_bf16 v[44:47], v[172:175], v[136:139], v[44:47]\n"
      "ds_read_b128 v[136:139], v241 offset:4096\n"
      "v_accvgpr_read_b32 v201, a153\n"
      "v_mfma_f32_16x16x32_bf16 v[48:51], v[160:163], v[140:143], v[48:51]\n"
      "ds_read_b64_tr_b16 v[184:185], v242 offset:8704\n"
      "global_load_lds_dwordx4 v233, s[80:81] offset:1024\n"
      "v_mfma_f32_16x16x32_bf16 v[52:55], v[164:167], v[140:143], v[52:55]\n"
      "v_accvgpr_read_b32 v202, a154\n"
      "v_accvgpr_read_b32 v203, a155\n"
      "v_mfma_f32_16x16x32_bf16 v[56:59], v[168:171], v[140:143], v[56:59]\n"
      "v_accvgpr_read_b32 v204, a156\n"
      "v_mfma_f32_16x16x32_bf16 v[60:63], v[172:175], v[140:143], v[60:63]\n"
      "ds_read_b64_tr_b16 v[186:187], v242 offset:8960\n"
      "v_accvgpr_read_b32 v205, a157\n"
      "v_mfma_f32_16x16x32_bf16 v[212:215], v[164:167], v[250:253], v[212:215]\n"
      "ds_read_b128 v[140:143], v241 offset:6144\n"
      "v_accvgpr_read_b32 v206, a158\n"
      "v_mfma_f32_16x16x32_bf16 v[64:67], v[160:163], v[144:147], v[64:67]\n"
      "v_accvgpr_read_b32 v207, a159\n"
      "v_mfma_f32_16x16x32_bf16 v[68:71], v[164:167], v[144:147], v[68:71]\n"
      "v_cvt_pk_bf16_f32 v200, v200, v201\n"
      "v_mfma_f32_16x16x32_bf16 v[72:75], v[168:171], v[144:147], v[72:75]\n"
      "ds_read_b64_tr_b16 v[188:189], v243 offset:8704\n"
      "v_cvt_pk_bf16_f32 v201, v202, v203\n"
      "v_mfma_f32_16x16x32_bf16 v[76:79], v[172:175], v[144:147], v[76:79]\n"
      "ds_read_b128 v[144:147], v241 offset:8192\n"
      "v_cvt_pk_bf16_f32 v202, v204, v205\n"
      "v_mfma_f32_16x16x32_bf16 v[80:83], v[160:163], v[148:151], v[80:83]\n"
      "global_load_lds_dwordx4 v234, s[80:81] offset:2048\n"
      "v_mfma_f32_16x16x32_bf16 v[84:87], v[164:167], v[148:151], v[84:87]\n"
      "ds_read_b64_tr_b16 v[190:191], v243 offset:8960\n"
      "v_cvt_pk_bf16_f32 v203, v206, v207\n"
      "v_mfma_f32_16x16x32_bf16 v[88:91], v[168:171], v[148:151], v[88:91]\n"
      "global_store_dwordx4 v247, v[200:203], s[84:85] offset:1024 sc1\n"
      "v_mfma_f32_16x16x32_bf16 v[92:95], v[172:175], v[148:151], v[92:95]\n"
      "v_accvgpr_write_b32 a152, v200\n"
      "v_mfma_f32_16x16x32_bf16 v[216:219], v[168:171], v[250:253], v[216:219]\n"
      "ds_read_b128 v[148:151], v241 offset:10240\n"
      "v_accvgpr_write_b32 a153, v201\n"
      "v_mfma_f32_16x16x32_bf16 v[96:99], v[160:163], v[152:155], v[96:99]\n"
      "v_accvgpr_write_b32 a154, v202\n"
      "v_mfma_f32_16x16x32_bf16 v[100:103], v[164:167], v[152:155], v[100:103]\n"
      "v_accvgpr_write_b32 a155, v203\n"
      "v_mfma_f32_16x16x32_bf16 v[104:107], v[168:171], v[152:155], v[104:107]\n"
      "v_accvgpr_read_b32 v192, a176\n"
      "v_mfma_f32_16x16x32_bf16 v[108:111], v[172:175], v[152:155], v[108:111]\n"
      "ds_read_b128 v[152:155], v241 offset:12288\n"
      "v_accvgpr_read_b32 v193, a177\n"
      "v_mfma_f32_16x16x32_bf16 v[112:115], v[160:163], v[156:159], v[112:115]\n"
      "global_load_lds_dwordx4 v235, s[80:81] offset:3072\n"
      "v_mfma_f32_16x16x32_bf16 v[116:119], v[164:167], v[156:159], v[116:119]\n"
      "v_accvgpr_read_b32 v194, a178\n"
      "v_mfma_f32_16x16x32_bf16 v[120:123], v[168:171], v[156:159], v[120:123]\n"
      "v_accvgpr_read_b32 v195, a179\n"
      "v_accvgpr_read_b32 v196, a180\n"
      "v_mfma_f32_16x16x32_bf16 v[124:127], v[172:175], v[156:159], v[124:127]\n"
      "v_accvgpr_read_b32 v197, a181\n"
      "v_mfma_f32_16x16x32_bf16 v[220:223], v[172:175], v[250:253], v[220:223]\n"
      "ds_read_b128 v[156:159], v241 offset:14336\n"
      "v_accvgpr_read_b32 v198, a182\n"
      "s_waitcnt lgkmcnt(7)\n"
      "v_mfma_f32_16x16x32_bf16 v[0:3], v[176:179], v[128:131], v[0:3]\n"
      "v_mfma_f32_16x16x32_bf16 v[4:7], v[180:183], v[128:131], v[4:7]\n"
      "v_mfma_f32_16x16x32_bf16 v[8:11], v[184:187], v[128:131], v[8:11]\n"
      "s_waitcnt lgkmcnt(0)\n"
      "v_mfma_f32_16x16x32_bf16 v[12:15], v[188:191], v[128:131], v[12:15]\n"
      "s_waitcnt vmcnt(18) lgkmcnt(0)\n"
      "s_barrier\n"
      "v_add_u32 v240, 65536, v236\n"
      "v_add_u32 v241, 65536, v237\n"
      "v_add_u32 v242, 98304, v238\n"
      "v_add_u32 v243, 98304, v239\n"
      "ds_read_b128 v[128:131], v240 offset:0\n"
      "v_mfma_f32_16x16x32_bf16 v[16:19], v[176:179], v[132:135], v[16:19]\n"
      "ds_read_b64_tr_b16 v[160:161], v242 offset:0\n"
      "v_accvgpr_read_b32 v199, a183\n"
      "v_mfma_f32_16x16x32_bf16 v[20:23], v[180:183], v[132:135], v[20:23]\n"
      "v_cvt_pk_bf16_f32 v192, v192, v193\n"
      "v_mfma_f32_16x16x32_bf16 v[24:27], v[184:187], v[132:135], v[24:27]\n"
      "v_cvt_pk_bf16_f32 v193, v194, v195\n"
      "v_mfma_f32_16x16x32_bf16 v[28:31], v[188:191], v[132:135], v[28:31]\n"
      "ds_read_b64_tr_b16 v[162:163], v242 offset:256\n"
      "v_cvt_pk_bf16_f32 v194, v196, v197\n"
      "v_mfma_f32_16x16x32_bf16 v[208:211], v[176:179], v[250:253], v[208:211]\n"
      "ds_read_b128 v[132:135], v240 offset:2048\n"
      "v_cvt_pk_bf16_f32 v195, v198, v199\n"
      "v_mfma_f32_16x16x32_bf16 v[32:35], v[176:179], v[136:139], v[32:35]\n"
      "global_store_dwordx4 v247, v[192:195], s[84:85] offset:2048 sc1\n"
      "v_mfma_f32_16x16x32_bf16 v[36:39], v[180:183], v[136:139], v[36:39]\n"
      "ds_read_b64_tr_b16 v[164:165], v243 offset:0\n"
      "v_accvgpr_write_b32 a176, v192\n"
      "v_mfma_f32_16x16x32_bf16 v[40:43], v[184:187], v[136:139], v[40:43]\n"
      "v_accvgpr_write_b32 a177, v193\n"
      "v_mfma_f32_16x16x32_bf16 v[44:47], v[188:191], v[136:139], v[44:47]\n"
      "ds_read_b128 v[136:139], v240 offset:4096\n"
      "v_accvgpr_write_b32 a178, v194\n"
      "v_mfma_f32_16x16x32_bf16 v[48:51], v[176:179], v[140:143], v[48:51]\n"
      "ds_read_b64_tr_b16 v[166:167], v243 offset:256\n"
      "v_accvgpr_write_b32 a179, v195\n"
      "v_mfma_f32_16x16x32_bf16 v[52:55], v[180:183], v[140:143], v[52:55]\n"
      "v_accvgpr_read_b32 v200, a184\n"
      "v_mfma_f32_16x16x32_bf16 v[56:59], v[184:187], v[140:143], v[56:59]\n"
      "v_accvgpr_read_b32 v201, a185\n"
      "v_mfma_f32_16x16x32_bf16 v[60:63], v[188:191], v[140:143], v[60:63]\n"
      "ds_read_b64_tr_b16 v[168:169], v242 offset:512\n"
      "v_accvgpr_read_b32 v202, a186\n"
      "v_mfma_f32_16x16x32_bf16 v[212:215], v[180:183], v[250:253], v[212:215]\n"
      "ds_read_b128 v[140:143], v240 offset:6144\n"
      "v_accvgpr_read_b32 v203, a187\n"
      "v_mfma_f32_16x16x32_bf16 v[64:67], v[176:179], v[144:147], v[64:67]\n"
      "v_accvgpr_read_b32 v204, a188\n"
      "v_mfma_f32_16x16x32_bf16 v[68:71], v[180:183], v[144:147], v[68:71]\n"
      "ds_read_b64_tr_b16 v[170:171], v242 offset:768\n"
      "v_accvgpr_read_b32 v205, a189\n"
      "v_mfma_f32_16x16x32_bf16 v[72:75], v[184:187], v[144:147], v[72:75]\n"
      "v_accvgpr_read_b32 v206, a190\n"
      "v_mfma_f32_16x16x32_bf16 v[76:79], v[188:191], v[144:147], v[76:79]\n"
      "ds_read_b128 v[144:147], v240 offset:8192\n"
      "v_accvgpr_read_b32 v207, a191\n"
      "v_mfma_f32_16x16x32_bf16 v[80:83], v[176:179], v[148:151], v[80:83]\n"
      "ds_read_b64_tr_b16 v[172:173], v243 offset:512\n"
      "v_cvt_pk_bf16_f32 v200, v200, v201\n"
      "v_mfma_f32_16x16x32_bf16 v[84:87], v[180:183], v[148:151], v[84:87]\n"
      "v_cvt_pk_bf16_f32 v201, v202, v203\n"
      "v_mfma_f32_16x16x32_bf16 v[88:91], v[184:187], v[148:151], v[88:91]\n"
      "v_cvt_pk_bf16_f32 v202, v204, v205\n"
      "v_mfma_f32_16x16x32_bf16 v[92:95], v[188:191], v[148:151], v[92:95]\n"
      "ds_read_b64_tr_b16 v[174:175], v243 offset:768\n"
      "v_cvt_pk_bf16_f32 v203, v206, v207\n"
      "v_mfma_f32_16x16x32_bf16 v[216:219], v[184:187], v[250:253], v[216:219]\n"
      "ds_read_b128 v[148:151], v240 offset:10240\n"
      "v_mfma_f32_16x16x32_bf16 v[96:99], v[176:179], v[152:155], v[96:99]\n"
      "global_store_dwordx4 v247, v[200:203], s[84:85] offset:3072 sc1\n"
      "v_mfma_f32_16x16x32_bf16 v[100:103], v[180:183], v[152:155], v[100:103]\n"
      "v_accvgpr_write_b32 a184, v200\n"
      "v_mfma_f32_16x16x32_bf16 v[104:107], v[184:187], v[152:155], v[104:107]\n"
      "v_accvgpr_write_b32 a185, v201\n"
      "v_accvgpr_write_b32 a186, v202\n"
      "v_mfma_f32_16x16x32_bf16 v[108:111], v[188:191], v[152:155], v[108:111]\n"
      "ds_read_b128 v[152:155], v240 offset:12288\n"
      "v_accvgpr_write_b32 a187, v203\n"
      "v_mfma_f32_16x16x32_bf16 v[112:115], v[176:179], v[156:159], v[112:115]\n"
      "s_add_u32 s84, s74, 20480\n s_addc_u32 s85, s75, 0\n"
      "v_mfma_f32_16x16x32_bf16 v[116:119], v[180:183], v[156:159], v[116:119]\n"
      "v_accvgpr_read_b32 v192, a208\n"
      "v_mfma_f32_16x16x32_bf16 v[120:123], v[184:187], v[156:159], v[120:123]\n"
      "v_accvgpr_read_b32 v193, a209\n"
      "v_mfma_f32_16x16x32_bf16 v[124:127], v[188:191], v[156:159], v[124:127]\n"
      "v_accvgpr_read_b32 v194, a210\n"
      "v_mfma_f32_16x16x32_bf16 v[220:223], v[188:191], v[250:253], v[220:223]\n"
      "ds_read_b128 v[156:159], v240 offset:14336\n"
      "v_accvgpr_read_b32 v195, a211\n"
      "s_waitcnt lgkmcnt(7)\n"
      "v_mfma_f32_16x16x32_bf16 v[0:3], v[160:163], v[128:131], v[0:3]\n"
      "ds_read_b64_tr_b16 v[176:177], v242 offset:8192\n"
      "s_lshr_b32 s76, %12, 1\n"
      "v_mfma_f32_16x16x32_bf16 v[4:7], v[164:167], v[128:131], v[4:7]\n"
      "v_accvgpr_read_b32 v196, a212\n"
      "v_accvgpr_read_b32 v197, a213\n"
      "s_waitcnt lgkmcnt(1)\n"
      "v_mfma_f32_16x16x32_bf16 v[8:11], v[168:171], v[128:131], v[8:11]\n"
      "s_xor_b32 s76, s76, 1\n"
      "v_accvgpr_read_b32 v198, a214\n"
      "v_mfma_f32_16x16x32_bf16 v[12:15], v[172:175], v[128:131], v[12:15]\n"
      "ds_read_b128 v[128:131], v241 offset:0\n"
      "ds_read_b64_tr_b16 v[178:179], v242 offset:8448\n"
      "v_mfma_f32_16x16x32_bf16 v[16:19], v[160:163], v[132:135], v[16:19]\n"
      "s_lshl_b32 s76, s76, 15\n"
      "v_accvgpr_read_b32 v199, a215\n"
      "v_mfma_f32_16x16x32_bf16 v[20:23], v[164:167], v[132:135], v[20:23]\n"
      "v_cvt_pk_bf16_f32 v192, v192, v193\n"
      "v_cvt_pk_bf16_f32 v193, v194, v195\n"
      "v_mfma_f32_16x16x32_bf16 v[24:27], v[168:171], v[132:135], v[24:27]\n"
      "ds_read_b64_tr_b16 v[180:181], v243 offset:8192\n"
      "v_cvt_pk_bf16_f32 v194, v196, v197\n"
      "v_mfma_f32_16x16x32_bf16 v[28:31], v[172:175], v[132:135], v[28:31]\n"
      "s_lshr_b32 s77, s88, 2\n"
      "v_cvt_pk_bf16_f32 v195, v198, v199\n"
      "v_mfma_f32_16x16x32_bf16 v[208:211], v[160:163], v[250:253], v[208:211]\n"
      "ds_read_b128 v[132:135], v241 offset:2048\n"
      "v_mfma_f32_16x16x32_bf16 v[32:35], v[160:163], v[136:139], v[32:35]\n"
      "s_add_u32 s76, s76, s77\n"
      "global_store_dwordx4 v247, v[192:195], s[84:85] offset:0 sc1\n"
      "v_mfma_f32_16x16x32_bf16 v[36:39], v[164:167], v[136:139], v[36:39]\n"
      "ds_read_b64_tr_b16 v[182:183], v243 offset:8448\n"
      "v_accvgpr_write_b32 a208, v192\n"
      "v_mfma_f32_16x16x32_bf16 v[40:43], v[168:171], v[136:139], v[40:43]\n"
      "v_accvgpr_write_b32 a209, v193\n"
      "v_accvgpr_write_b32 a210, v194\n"
      "v_mfma_f32_16x16x32_bf16 v[44:47], v[172:175], v[136:139], v[44:47]\n"
      "ds_read_b128 v[136:139], v241 offset:4096\n"
      "s_add_u32 s76, s76, %11\n"
      "v_mfma_f32_16x16x32_bf16 v[48:51], v[160:163], v[140:143], v[48:51]\n"
      "ds_read_b64_tr_b16 v[184:185], v242 offset:8704\n"
      "v_accvgpr_write_b32 a211, v195\n"
      "v_mfma_f32_16x16x32_bf16 v[52:55], v[164:167], v[140:143], v[52:55]\n"
      "v_add_u32 v224, s76, v244\n"
      "v_accvgpr_read_b32 v200, a216\n"
      "v_mfma_f32_16x16x32_bf16 v[56:59], v[168:171], v[140:143], v[56:59]\n"
      "v_accvgpr_read_b32 v201, a217\n"
      "v_accvgpr_read_b32 v202, a218\n"
      "v_mfma_f32_16x16x32_bf16 v[60:63], v[172:175], v[140:143], v[60:63]\n"
      "ds_read_b64_tr_b16 v[186:187], v242 offset:8960\n"
      "ds_write_b128 v224, a[0:3] offset:0\n"
      "v_mfma_f32_16x16x32_bf16 v[212:215], v[164:167], v[250:253], v[212:215]\n"
      "ds_read_b128 v[140:143], v241 offset:6144\n"
      "v_accvgpr_read_b32 v203, a219\n"
      "v_mfma_f32_16x16x32_bf16 v[64:67], v[160:163], v[144:147], v[64:67]\n"
      "v_accvgpr_read_b32 v204, a220\n"
      "v_accvgpr_read_b32 v205, a221\n"
      "v_mfma_f32_16x16x32_bf16 v[68:71], v[164:167], v[144:147], v[68:71]\n"
      "ds_write_b128 v224, a[8:11] offset:1024\n"
      "v_mfma_f32_16x16x32_bf16 v[72:75], v[168:171], v[144:147], v[72:75]\n"
      "ds_read_b64_tr_b16 v[188:189], v243 offset:8704\n"
      "v_accvgpr_read_b32 v206, a222\n"
      "v_mfma_f32_16x16x32_bf16 v[76:79], v[172:175], v[144:147], v[76:79]\n"
      "ds_read_b128 v[144:147], v241 offset:8192\n"
      "ds_write_b128 v224, a[16:19] offset:4096\n"
      "v_mfma_f32_16x16x32_bf16 v[80:83], v[160:163], v[148:151], v[80:83]\n"
      "v_accvgpr_read_b32 v207, a223\n"
      "v_cvt_pk_bf16_f32 v200, v200, v201\n"
      "v_mfma_f32_16x16x32_bf16 v[84:87], v[164:167], v[148:151], v[84:87]\n"
      "ds_read_b64_tr_b16 v[190:191], v243 offset:8960\n"
      "v_cvt_pk_bf16_f32 v201, v202, v203\n"
      "v_mfma_f32_16x16x32_bf16 v[88:91], v[168:171], v[148:151], v[88:91]\n"
      "ds_write_b128 v224, a[24:27] offset:5120\n"
      "v_mfma_f32_16x16x32_bf16 v[92:95], v[172:175], v[148:151], v[92:95]\n"
      "v_cvt_pk_bf16_f32 v202, v204, v205\n"
      "v_mfma_f32_16x16x32_bf16 v[216:219], v[168:171], v[250:253], v[216:219]\n"
      "ds_read_b128 v[148:151], v241 offset:10240\n"
      "ds_write_b128 v224, a[32:35] offset:8192\n"
      "v_mfma_f32_16x16x32_bf16 v[96:99], v[160:163], v[152:155], v[96:99]\n"
      "v_cvt_pk_bf16_f32 v203, v206, v207\n"
      "v_mfma_f32_16x16x32_bf16 v[100:103], v[164:167], v[152:155], v[100:103]\n"
      "ds_write_b128 v224, a[40:43] offset:9216\n"
      "v_mfma_f32_16x16x32_bf16 v[104:107], v[168:171], v[152:155], v[104:107]\n"
      "global_store_dwordx4 v247, v[200:203], s[84:85] offset:1024 sc1\n"
      "v_mfma_f32_16x16x32_bf16 v[108:111], v[172:175], v[152:155], v[108:111]\n"
      "ds_read_b128 v[152:155], v241 offset:12288\n"
      "v_accvgpr_write_b32 a216, v200\n"
      "v_mfma_f32_16x16x32_bf16 v[112:115], v[160:163], v[156:159], v[112:115]\n"
      "ds_write_b128 v224, a[48:51] offset:12288\n"
      "v_mfma_f32_16x16x32_bf16 v[116:119], v[164:167], v[156:159], v[116:119]\n"
      "v_accvgpr_write_b32 a217, v201\n"
      "v_accvgpr_write_b32 a218, v202\n"
      "v_mfma_f32_16x16x32_bf16 v[120:123], v[168:171], v[156:159], v[120:123]\n"
      "ds_write_b128 v224, a[56:59] offset:13312\n"
      "v_mfma_f32_16x16x32_bf16 v[124:127], v[172:175], v[156:159], v[124:127]\n"
      "v_accvgpr_write_b32 a219, v203\n"
      "v_accvgpr_read_b32 v192, a240\n"
      "v_mfma_f32_16x16x32_bf16 v[220:223], v[172:175], v[250:253], v[220:223]\n"
      "ds_read_b128 v[156:159], v241 offset:14336\n"
      "v_accvgpr_read_b32 v193, a241\n"
      "s_waitcnt lgkmcnt(15)\n"
      "v_mfma_f32_16x16x32_bf16 v[0:3], v[176:179], v[128:131], v[0:3]\n"
      "v_mfma_f32_16x16x32_bf16 v[4:7], v[180:183], v[128:131], v[4:7]\n"
      "v_mfma_f32_16x16x32_bf16 v[8:11], v[184:187], v[128:131], v[8:11]\n"
      "s_waitcnt lgkmcnt(2)\n"
      "v_mfma_f32_16x16x32_bf16 v[12:15], v[188:191], v[128:131], v[12:15]\n"
      "s_waitcnt vmcnt(4) lgkmcnt(0)\n"
      "s_cmp_eq_u32 %12, 0\n s_cbranch_scc1 L_lnw_%=\n L_lnok_%=:\n"
      "s_barrier\n"
      "v_add_u32 v240, 114688, v236\n"
      "v_add_u32 v241, 114688, v237\n"
      "v_add_u32 v242, 147456, v238\n"
      "v_add_u32 v243, 147456, v239\n"
      "ds_read_b128 v[128:131], v240 offset:0\n"
      "v_mfma_f32_16x16x32_bf16 v[16:19], v[176:179], v[132:135], v[16:19]\n"
      "ds_read_b64_tr_b16 v[160:161], v242 offset:0\n"
      "ds_write_b128 v224, a[64:67] offset:16384\n"
      "v_mfma_f32_16x16x32_bf16 v[20:23], v[180:183], v[132:135], v[20:23]\n"
      "v_accvgpr_read_b32 v194, a242\n"
      "v_accvgpr_read_b32 v195, a243\n"
      "v_mfma_f32_16x16x32_bf16 v[24:27], v[184:187], v[132:135], v[24:27]\n"
      "v_accvgpr_read_b32 v196, a244\n"
      "v_accvgpr_read_b32 v197, a245\n"
      "v_mfma_f32_16x16x32_bf16 v[28:31], v[188:191], v[132:135], v[28:31]\n"
      "ds_read_b64_tr_b16 v[162:163], v242 offset:256\n"
      "v_accvgpr_read_b32 v198, a246\n"
      "v_mfma_f32_16x16x32_bf16 v[208:211], v[176:179], v[250:253], v[208:211]\n"
      "ds_read_b128 v[132:135], v240 offset:2048\n"
      "ds_write_b128 v224, a[72:75] offset:17408\n"
      "v_mfma_f32_16x16x32_bf16 v[32:35], v[176:179], v[136:139], v[32:35]\n"
      "v_accvgpr_read_b32 v199, a247\n"
      "v_cvt_pk_bf16_f32 v192, v192, v193\n"
      "v_mfma_f32_16x16x32_bf16 v[36:39], v[180:183], v[136:139], v[36:39]\n"
      "ds_read_b64_tr_b16 v[164:165], v243 offset:0\n"
      "v_cvt_pk_bf16_f32 v193, v194, v195\n"
      "v_mfma_f32_16x16x32_bf16 v[40:43], v[184:187], v[136:139], v[40:43]\n"
      "v_cvt_pk_bf16_f32 v194, v196, v197\n"
      "v_cvt_pk_bf16_f32 v195, v198, v199\n"
      "v_mfma_f32_16x16x32_bf16 v[44:47], v[188:191], v[136:139], v[44:47]\n"
      "ds_read_b128 v[136:139], v240 offset:4096\n"
      "ds_write_b128 v224, a[80:83] offset:20480\n"
      "v_mfma_f32_16x16x32_bf16 v[48:51], v[176:179], v[140:143], v[48:51]\n"
      "ds_read_b64_tr_b16 v[166:167], v243 offset:256\n"
      "v_mfma_f32_16x16x32_bf16 v[52:55], v[180:183], v[140:143], v[52:55]\n"
      "global_store_dwordx4 v247, v[192:195], s[84:85] offset:2048 sc1\n"
      "v_mfma_f32_16x16x32_bf16 v[56:59], v[184:187], v[140:143], v[56:59]\n"
      "v_accvgpr_write_b32 a240, v192\n"
      "v_accvgpr_write_b32 a241, v193\n"
      "v_mfma_f32_16x16x32_bf16 v[60:63], v[188:191], v[140:143], v[60:63]\n"
      "ds_read_b64_tr_b16 v[168:169], v242 offset:512\n"
      "ds_write_b128 v224, a[88:91] offset:21504\n"
      "v_mfma_f32_16x16x32_bf16 v[212:215], v[180:183], v[250:253], v[212:215]\n"
      "ds_read_b128 v[140:143], v240 offset:6144\n"
      "v_accvgpr_write_b32 a242, v194\n"
      "v_mfma_f32_16x16x32_bf16 v[64:67], v[176:179], v[144:147], v[64:67]\n"
      "v_accvgpr_write_b32 a243, v195\n"
      "v_accvgpr_read_b32 v200, a248\n"
      "v_mfma_f32_16x16x32_bf16 v[68:71], v[180:183], v[144:147], v[68:71]\n"
      "ds_read_b64_tr_b16 v[170:171], v242 offset:768\n"
      "v_accvgpr_read_b32 v201, a249\n"
      "v_mfma_f32_16x16x32_bf16 v[72:75], v[184:187], v[144:147], v[72:75]\n"
      "ds_write_b128 v224, a[96:99] offset:24576\n"
      "v_mfma_f32_16x16x32_bf16 v[76:79], v[188:191], v[144:147], v[76:79]\n"
      "ds_read_b128 v[144:147], v240 offset:8192\n"
      "v_accvgpr_read_b32 v202, a250\n"
      "v_mfma_f32_16x16x32_bf16 v[80:83], v[176:179], v[148:151], v[80:83]\n"
      "ds_read_b64_tr_b16 v[172:173], v243 offset:512\n"
      "v_accvgpr_read_b32 v203, a251\n"
      "v_mfma_f32_16x16x32_bf16 v[84:87], v[180:183], v[148:151], v[84:87]\n"
      "v_accvgpr_read_b32 v204, a252\n"
      "v_accvgpr_read_b32 v205, a253\n"
      "v_mfma_f32_16x16x32_bf16 v[88:91], v[184:187], v[148:151], v[88:91]\n"
      "ds_write_b128 v224, a[104:107] offset:25600\n"
      "v_mfma_f32_16x16x32_bf16 v[92:95], v[188:191], v[148:151], v[92:95]\n"
      "ds_read_b64_tr_b16 v[174:175], v243 offset:768\n"
      "v_accvgpr_read_b32 v206, a254\n"
      "v_mfma_f32_16x16x32_bf16 v[216:219], v[184:187], v[250:253], v[216:219]\n"
      "ds_read_b128 v[148:151], v240 offset:10240\n"
      "v_accvgpr_read_b32 v207, a255\n"
      "v_mfma_f32_16x16x32_bf16 v[96:99], v[176:179], v[152:155], v[96:99]\n"
      "v_cvt_pk_bf16_f32 v200, v200, v201\n"
      "v_mfma_f32_16x16x32_bf16 v[100:103], v[180:183], v[152:155], v[100:103]\n"
      "ds_write_b128 v224, a[112:115] offset:28672\n"
      "v_mfma_f32_16x16x32_bf16 v[104:107], v[184:187], v[152:155], v[104:107]\n"
      "v_cvt_pk_bf16_f32 v201, v202, v203\n"
      "v_cvt_pk_bf16_f32 v202, v204, v205\n"
      "v_mfma_f32_16x16x32_bf16 v[108:111], v[188:191], v[152:155], v[108:111]\n"
      "ds_read_b128 v[152:155], v240 offset:12288\n"
      "v_cvt_pk_bf16_f32 v203, v206, v207\n"
      "v_mfma_f32_16x16x32_bf16 v[112:115], v[176:179], v[156:159], v[112:115]\n"
      "global_store_dwordx4 v247, v[200:203], s[84:85] offset:3072 sc1\n"
      "v_mfma_f32_16x16x32_bf16 v[116:119], v[180:183], v[156:159], v[116:119]\n"
      "ds_write_b128 v224, a[120:123] offset:29696\n"
      "v_mfma_f32_16x16x32_bf16 v[120:123], v[184:187], v[156:159], v[120:123]\n"
      "v_accvgpr_write_b32 a248, v200\n"
      "v_accvgpr_write_b32 a249, v201\n"
      "v_mfma_f32_16x16x32_bf16 v[124:127], v[188:191], v[156:159], v[124:127]\n"
      "v_accvgpr_write_b32 a250, v202\n"
      "v_mfma_f32_16x16x32_bf16 v[220:223], v[188:191], v[250:253], v[220:223]\n"
      "ds_read_b128 v[156:159], v240 offset:14336\n"
      "v_accvgpr_write_b32 a251, v203\n"
      "s_waitcnt lgkmcnt(14)\n"
      "v_mfma_f32_16x16x32_bf16 v[0:3], v[160:163], v[128:131], v[0:3]\n"
      "ds_read_b64_tr_b16 v[176:177], v242 offset:8192\n"
      "v_bfe_u32 v246, v244, 4, 4\n"
      "v_mfma_f32_16x16x32_bf16 v[4:7], v[164:167], v[128:131], v[4:7]\n"
      "s_waitcnt lgkmcnt(5)\n"
      "v_mfma_f32_16x16x32_bf16 v[8:11], v[168:171], v[128:131], v[8:11]\n"
      "v_mfma_f32_16x16x32_bf16 v[12:15], v[172:175], v[128:131], v[12:15]\n"
      "ds_read_b128 v[128:131], v241 offset:0\n"
      "ds_read_b64_tr_b16 v[178:179], v242 offset:8448\n"
      "v_lshlrev_b32 v246, 3, v246\n"
      "v_mfma_f32_16x16x32_bf16 v[16:19], v[160:163], v[132:135], v[16:19]\n"
      "v_mfma_f32_16x16x32_bf16 v[20:23], v[164:167], v[132:135], v[20:23]\n"
      "v_mfma_f32_16x16x32_bf16 v[24:27], v[168:171], v[132:135], v[24:27]\n"
      "ds_read_b64_tr_b16 v[180:181], v243 offset:8192\n"
      "global_load_dwordx2 v[224:225], v246, %23 offset:0 sc1\n"
      "v_mfma_f32_16x16x32_bf16 v[28:31], v[172:175], v[132:135], v[28:31]\n"
      "v_mfma_f32_16x16x32_bf16 v[208:211], v[160:163], v[250:253], v[208:211]\n"
      "ds_read_b128 v[132:135], v241 offset:2048\n"
      "v_mfma_f32_16x16x32_bf16 v[32:35], v[160:163], v[136:139], v[32:35]\n"
      "global_load_dwordx2 v[226:227], v246, %23 offset:128 sc1\n"
      "v_mfma_f32_16x16x32_bf16 v[36:39], v[164:167], v[136:139], v[36:39]\n"
      "ds_read_b64_tr_b16 v[182:183], v243 offset:8448\n"
      "v_mfma_f32_16x16x32_bf16 v[40:43], v[168:171], v[136:139], v[40:43]\n"
      "v_mfma_f32_16x16x32_bf16 v[44:47], v[172:175], v[136:139], v[44:47]\n"
      "ds_read_b128 v[136:139], v241 offset:4096\n"
      "v_mfma_f32_16x16x32_bf16 v[48:51], v[160:163], v[140:143], v[48:51]\n"
      "ds_read_b64_tr_b16 v[184:185], v242 offset:8704\n"
      "global_load_dwordx2 v[228:229], v246, %23 offset:256 sc1\n"
      "v_mfma_f32_16x16x32_bf16 v[52:55], v[164:167], v[140:143], v[52:55]\n"
      "v_mfma_f32_16x16x32_bf16 v[56:59], v[168:171], v[140:143], v[56:59]\n"
      "v_mfma_f32_16x16x32_bf16 v[60:63], v[172:175], v[140:143], v[60:63]\n"
      "ds_read_b64_tr_b16 v[186:187], v242 offset:8960\n"
      "global_load_dwordx2 v[230:231], v246, %23 offset:384 sc1\n"
      "v_mfma_f32_16x16x32_bf16 v[212:215], v[164:167], v[250:253], v[212:215]\n"
      "ds_read_b128 v[140:143], v241 offset:6144\n"
      "v_mfma_f32_16x16x32_bf16 v[64:67], v[160:163], v[144:147], v[64:67]\n"
      "v_mfma_f32_16x16x32_bf16 v[68:71], v[164:167], v[144:147], v[68:71]\n"
      "global_load_dwordx2 v[232:233], v246, %23 offset:512 sc1\n"
      "v_mfma_f32_16x16x32_bf16 v[72:75], v[168:171], v[144:147], v[72:75]\n"
      "ds_read_b64_tr_b16 v[188:189], v243 offset:8704\n"
      "v_mfma_f32_16x16x32_bf16 v[76:79], v[172:175], v[144:147], v[76:79]\n"
      "ds_read_b128 v[144:147], v241 offset:8192\n"
      "v_mfma_f32_16x16x32_bf16 v[80:83], v[160:163], v[148:151], v[80:83]\n"
      "global_load_dwordx2 v[234:235], v246, %23 offset:640 sc1\n"
      "v_mfma_f32_16x16x32_bf16 v[84:87], v[164:167], v[148:151], v[84:87]\n"
      "ds_read_b64_tr_b16 v[190:191], v243 offset:8960\n"
      "v_mfma_f32_16x16x32_bf16 v[88:91], v[168:171], v[148:151], v[88:91]\n"
      "v_mfma_f32_16x16x32_bf16 v[92:95], v[172:175], v[148:151], v[92:95]\n"
      "v_mfma_f32_16x16x32_bf16 v[216:219], v[168:171], v[250:253], v[216:219]\n"
      "ds_read_b128 v[148:151], v241 offset:10240\n"
      "global_load_dwordx2 v[236:237], v246, %23 offset:768 sc1\n"
      "s_waitcnt lgkmcnt(10)\n"
      "v_mfma_f32_16x16x32_bf16 v[96:99], v[160:163], v[152:155], v[96:99]\n"
      "v_mfma_f32_16x16x32_bf16 v[100:103], v[164:167], v[152:155], v[100:103]\n"
      "v_mfma_f32_16x16x32_bf16 v[104:107], v[168:171], v[152:155], v[104:107]\n"
      "global_load_dwordx2 v[238:239], v246, %23 offset:896 sc1\n"
      "v_mfma_f32_16x16x32_bf16 v[108:111], v[172:175], v[152:155], v[108:111]\n"
      "ds_read_b128 v[152:155], v241 offset:12288\n"
      "v_mfma_f32_16x16x32_bf16 v[112:115], v[160:163], v[156:159], v[112:115]\n"
      "v_mfma_f32_16x16x32_bf16 v[116:119], v[164:167], v[156:159], v[116:119]\n"
      "global_load_dword v255, v245, %15 sc1\n"
      "v_mfma_f32_16x16x32_bf16 v[120:123], v[168:171], v[156:159], v[120:123]\n"
      "v_mfma_f32_16x16x32_bf16 v[124:127], v[172:175], v[156:159], v[124:127]\n"
      "v_mfma_f32_16x16x32_bf16 v[220:223], v[172:175], v[250:253], v[220:223]\n"
      "ds_read_b128 v[156:159], v241 offset:14336\n"
      "v_mfma_f32_16x16x32_bf16 v[0:3], v[176:179], v[128:131], v[0:3]\n"
      "s_waitcnt lgkmcnt(4)\n"
      "v_mfma_f32_16x16x32_bf16 v[4:7], v[180:183], v[128:131], v[4:7]\n"
      "v_mfma_f32_16x16x32_bf16 v[8:11], v[184:187], v[128:131], v[8:11]\n"
      "s_waitcnt lgkmcnt(0)\n"
      "v_mfma_f32_16x16x32_bf16 v[12:15], v[188:191], v[128:131], v[12:15]\n"
      "s_waitcnt vmcnt(0) lgkmcnt(0)\n"
      "s_barrier\n"
      "s_cmp_eq_u32 %12, 0\n s_cbranch_scc0 L_e2_%=\n s_mov_b32 exec_lo, 0xff\n s_mov_b32 exec_hi, -1\n global_store_dword v249, v255, %15 sc1\n s_mov_b64 exec, -1\n L_e2_%=:\n"
      "s_lshl_b32 s86, %12, 13\n"
      "s_add_u32 s84, %11, s86\n"
      "v_lshlrev_b32 v194, 4, v248\n"
      "v_add_u32 v194, s86, v194\n"
      "s_lshl_b32 s86, %14, 15\n"
      "s_sub_u32 s87, 15, %14\n"
      "s_lshl_b32 s87, s87, 15\n"
      "v_add_u32 v195, s87, v194\n"
      "v_add_u32 v194, s86, v194\n"
      "v_mfma_f32_16x16x32_bf16 v[16:19], v[176:179], v[132:135], v[16:19]\n"
      "v_mfma_f32_16x16x32_bf16 v[20:23], v[180:183], v[132:135], v[20:23]\n"
      "v_mfma_f32_16x16x32_bf16 v[24:27], v[184:187], v[132:135], v[24:27]\n"
      "v_mfma_f32_16x16x32_bf16 v[28:31], v[188:191], v[132:135], v[28:31]\n"
      "v_mfma_f32_16x16x32_bf16 v[208:211], v[176:179], v[250:253], v[208:211]\n"
      "v_mfma_f32_16x16x32_bf16 v[32:35], v[176:179], v[136:139], v[32:35]\n"
      "v_mfma_f32_16x16x32_bf16 v[36:39], v[180:183], v[136:139], v[36:39]\n"
      "v_mfma_f32_16x16x32_bf16 v[40:43], v[184:187], v[136:139], v[40:43]\n"
      "v_mfma_f32_16x16x32_bf16 v[44:47], v[188:191], v[136:139], v[44:47]\n"
      "v_mfma_f32_16x16x32_bf16 v[48:51], v[176:179], v[140:143], v[48:51]\n"
      "v_mfma_f32_16x16x32_bf16 v[52:55], v[180:183], v[140:143], v[52:55]\n"
      "v_mfma_f32_16x16x32_bf16 v[56:59], v[184:187], v[140:143], v[56:59]\n"
      "v_mfma_f32_16x16x32_bf16 v[60:63], v[188:191], v[140:143], v[60:63]\n"
      "v_mfma_f32_16x16x32_bf16 v[212:215], v[180:183], v[250:253], v[212:215]\n"
      "v_mfma_f32_16x16x32_bf16 v[64:67], v[176:179], v[144:147], v[64:67]\n"
      "v_mfma_f32_16x16x32_bf16 v[68:71], v[180:183], v[144:147], v[68:71]\n"
      "v_mfma_f32_16x16x32_bf16 v[72:75], v[184:187], v[144:147], v[72:75]\n"
      "v_mfma_f32_16x16x32_bf16 v[76:79], v[188:191], v[144:147], v[76:79]\n"
      "v_mfma_f32_16x16x32_bf16 v[80:83], v[176:179], v[148:151], v[80:83]\n"
      "v_mfma_f32_16x16x32_bf16 v[84:87], v[180:183], v[148:151], v[84:87]\n"
      "v_mfma_f32_16x16x32_bf16 v[88:91], v[184:187], v[148:151], v[88:91]\n"
      "v_mfma_f32_16x16x32_bf16 v[92:95], v[188:191], v[148:151], v[92:95]\n"
      "v_mfma_f32_16x16x32_bf16 v[216:219], v[184:187], v[250:253], v[216:219]\n"
      "v_mfma_f32_16x16x32_bf16 v[96:99], v[176:179], v[152:155], v[96:99]\n"
      "v_mfma_f32_16x16x32_bf16 v[100:103], v[180:183], v[152:155], v[100:103]\n"
      "v_mfma_f32_16x16x32_bf16 v[104:107], v[184:187], v[152:155], v[104:107]\n"
      "v_mfma_f32_16x16x32_bf16 v[108:111], v[188:191], v[152:155], v[108:111]\n"
      "v_mfma_f32_16x16x32_bf16 v[112:115], v[176:179], v[156:159], v[112:115]\n"
      "v_mfma_f32_16x16x32_bf16 v[116:119], v[180:183], v[156:159], v[116:119]\n"
      "v_mfma_f32_16x16x32_bf16 v[120:123], v[184:187], v[156:159], v[120:123]\n"
      "v_mfma_f32_16x16x32_bf16 v[124:127], v[188:191], v[156:159], v[124:127]\n"
      "v_mfma_f32_16x16x32_bf16 v[220:223], v[188:191], v[250:253], v[220:223]\n"
      "s_cmp_lt_u32 %12, 2\n"
      "s_cbranch_scc1 L_nov_%=\n"
      "s_add_u32 s76, s88, %11\n"
      "s_add_u32 s76, s76, 65536\n"
      "v_add_u32 v192, s76, v244\n"
      "ds_write_b128 v192, a[128:131] offset:0\n"
      "ds_write_b128 v192, a[136:139] offset:1024\n"
      "ds_write_b128 v192, a[160:163] offset:2048\n"
      "ds_write_b128 v192, a[168:171] offset:3072\n"
      "ds_write_b128 v192, a[192:195] offset:4096\n"
      "ds_write_b128 v192, a[200:203] offset:5120\n"
      "ds_write_b128 v192, a[224:227] offset:6144\n"
      "ds_write_b128 v192, a[232:235] offset:7168\n"
      "ds_write_b128 v192, a[144:147] offset:16384\n"
      "ds_write_b128 v192, a[152:155] offset:17408\n"
      "ds_write_b128 v192, a[176:179] offset:18432\n"
      "ds_write_b128 v192, a[184:187] offset:19456\n"
      "ds_write_b128 v192, a[208:211] offset:20480\n"
      "ds_write_b128 v192, a[216:219] offset:21504\n"
      "ds_write_b128 v192, a[240:243] offset:22528\n"
      "ds_write_b128 v192, a[248:251] offset:23552\n"
      "L_nov_%=:\n"
      "s_lshr_b32 s86, %12, 1\n"
      "s_lshl_b32 s86, s86, 15\n"
      "s_add_u32 s86, s86, %11\n"
      "s_add_u32 s86, s86, 98304\n"
      "s_lshr_b32 s87, s88, 2\n"
      "s_add_u32 s86, s86, s87\n"
      "v_add_u32 v200, s86, v244\n"
      "s_nop 3\n"
      "v_pk_mul_f32 v[0:1], v[0:1], v[224:225] op_sel:[0,1] op_sel_hi:[1,1]\n"
      "v_pk_mul_f32 v[2:3], v[2:3], v[224:225] op_sel:[0,1] op_sel_hi:[1,1]\n"
      "v_pk_mul_f32 v[4:5], v[4:5], v[224:225] op_sel:[0,1] op_sel_hi:[1,1]\n"
      "v_pk_mul_f32 v[6:7], v[6:7], v[224:225] op_sel:[0,1] op_sel_hi:[1,1]\n"
      "v_pk_mul_f32 v[8:9], v[8:9], v[224:225] op_sel:[0,1] op_sel_hi:[1,1]\n"
      "v_pk_mul_f32 v[10:11], v[10:11], v[224:225] op_sel:[0,1] op_sel_hi:[1,1]\n"
      "v_pk_mul_f32 v[12:13], v[12:13], v[224:225] op_sel:[0,1] op_sel_hi:[1,1]\n"
      "v_pk_mul_f32 v[14:15], v[14:15], v[224:225] op_sel:[0,1] op_sel_hi:[1,1]\n"
      "v_pk_fma_f32 v[0:1], v[208:209], v[224:225], v[0:1] op_sel:[0,0,0] op_sel_hi:[1,0,1]\n"
      "v_pk_fma_f32 v[2:3], v[210:211], v[224:225], v[2:3] op_sel:[0,0,0] op_sel_hi:[1,0,1]\n"
      "v_pk_fma_f32 v[4:5], v[212:213], v[224:225], v[4:5] op_sel:[0,0,0] op_sel_hi:[1,0,1]\n"
      "v_pk_fma_f32 v[6:7], v[214:215], v[224:225], v[6:7] op_sel:[0,0,0] op_sel_hi:[1,0,1]\n"
      "v_pk_fma_f32 v[8:9], v[216:217], v[224:225], v[8:9] op_sel:[0,0,0] op_sel_hi:[1,0,1]\n"
      "v_pk_fma_f32 v[10:11], v[218:219], v[224:225], v[10:11] op_sel:[0,0,0] op_sel_hi:[1,0,1]\n"
      "v_pk_fma_f32 v[12:13], v[220:221], v[224:225], v[12:13] op_sel:[0,0,0] op_sel_hi:[1,0,1]\n"
      "v_pk_fma_f32 v[14:15], v[222:223], v[224:225], v[14:15] op_sel:[0,0,0] op_sel_hi:[1,0,1]\n"
      "v_cvt_pk_bf16_f32 v0, v0, v1\n"
      "v_cvt_pk_bf16_f32 v1, v2, v3\n"
      "v_cvt_pk_bf16_f32 v2, v4, v5\n"
      "v_cvt_pk_bf16_f32 v3, v6, v7\n"
      "ds_write_b128 v200, v[0:3] offset:0\n"
      "v_cvt_pk_bf16_f32 v8, v8, v9\n"
      "v_cvt_pk_bf16_f32 v9, v10, v11\n"
      "v_cvt_pk_bf16_f32 v10, v12, v13\n"
      "v_cvt_pk_bf16_f32 v11, v14, v15\n"
      "ds_write_b128 v200, v[8:11] offset:1024\n"
      "v_pk_mul_f32 v[16:17], v[16:17], v[226:227] op_sel:[0,1] op_sel_hi:[1,1]\n"
      "v_pk_mul_f32 v[18:19], v[18:19], v[226:227] op_sel:[0,1] op_sel_hi:[1,1]\n"
      "v_pk_mul_f32 v[20:21], v[20:21], v[226:227] op_sel:[0,1] op_sel_hi:[1,1]\n"
      "v_pk_mul_f32 v[22:23], v[22:23], v[226:227] op_sel:[0,1] op_sel_hi:[1,1]\n"
      "v_pk_mul_f32 v[24:25], v[24:25], v[226:227] op_sel:[0,1] op_sel_hi:[1,1]\n"
      "v_pk_mul_f32 v[26:27], v[26:27], v[226:227] op_sel:[0,1] op_sel_hi:[1,1]\n"
      "v_pk_mul_f32 v[28:29], v[28:29], v[226:227] op_sel:[0,1] op_sel_hi:[1,1]\n"
      "v_pk_mul_f32 v[30:31], v[30:31], v[226:227] op_sel:[0,1] op_sel_hi:[1,1]\n"
      "v_pk_fma_f32 v[16:17], v[208:209], v[226:227], v[16:17] op_sel:[0,0,0] op_sel_hi:[1,0,1]\n"
      "v_pk_fma_f32 v[18:19], v[210:211], v[226:227], v[18:19] op_sel:[0,0,0] op_sel_hi:[1,0,1]\n"
      "v_pk_fma_f32 v[20:21], v[212:213], v[226:227], v[20:21] op_sel:[0,0,0] op_sel_hi:[1,0,1]\n"
      "v_pk_fma_f32 v[22:23], v[214:215], v[226:227], v[22:23] op_sel:[0,0,0] op_sel_hi:[1,0,1]\n"
      "v_pk_fma_f32 v[24:25], v[216:217], v[226:227], v[24:25] op_sel:[0,0,0] op_sel_hi:[1,0,1]\n"
      "v_pk_fma_f32 v[26:27], v[218:219], v[226:227], v[26:27] op_sel:[0,0,0] op_sel_hi:[1,0,1]\n"
      "v_pk_fma_f32 v[28:29], v[220:221], v[226:227], v[28:29] op_sel:[0,0,0] op_sel_hi:[1,0,1]\n"
      "v_pk_fma_f32 v[30:31], v[222:223], v[226:227], v[30:31] op_sel:[0,0,0] op_sel_hi:[1,0,1]\n"
      "v_cvt_pk_bf16_f32 v16, v16, v17\n"
      "v_cvt_pk_bf16_f32 v17, v18, v19\n"
      "v_cvt_pk_bf16_f32 v18, v20, v21\n"
      "v_cvt_pk_bf16_f32 v19, v22, v23\n"
      "ds_write_b128 v200, v[16:19] offset:4096\n"
      "v_cvt_pk_bf16_f32 v24, v24, v25\n"
      "v_cvt_pk_bf16_f32 v25, v26, v27\n"
      "v_cvt_pk_bf16_f32 v26, v28, v29\n"
      "v_cvt_pk_bf16_f32 v27, v30, v31\n"
      "ds_write_b128 v200, v[24:27] offset:5120\n"
      "v_pk_mul_f32 v[32:33], v[32:33], v[228:229] op_sel:[0,1] op_sel_hi:[1,1]\n"
      "v_pk_mul_f32 v[34:35], v[34:35], v[228:229] op_sel:[0,1] op_sel_hi:[1,1]\n"
      "v_pk_mul_f32 v[36:37], v[36:37], v[228:229] op_sel:[0,1] op_sel_hi:[1,1]\n"
      "v_pk_mul_f32 v[38:39], v[38:39], v[228:229] op_sel:[0,1] op_sel_hi:[1,1]\n"
      "v_pk_mul_f32 v[40:41], v[40:41], v[228:229] op_sel:[0,1] op_sel_hi:[1,1]\n"
      "v_pk_mul_f32 v[42:43], v[42:43], v[228:229] op_sel:[0,1] op_sel_hi:[1,1]\n"
      "v_pk_mul_f32 v[44:45], v[44:45], v[228:229] op_sel:[0,1] op_sel_hi:[1,1]\n"
      "v_pk_mul_f32 v[46:47], v[46:47], v[228:229] op_sel:[0,1] op_sel_hi:[1,1]\n"
      "v_pk_fma_f32 v[32:33], v[208:209], v[228:229], v[32:33] op_sel:[0,0,0] op_sel_hi:[1,0,1]\n"
      "v_pk_fma_f32 v[34:35], v[210:211], v[228:229], v[34:35] op_sel:[0,0,0] op_sel_hi:[1,0,1]\n"
      "v_pk_fma_f32 v[36:37], v[212:213], v[228:229], v[36:37] op_sel:[0,0,0] op_sel_hi:[1,0,1]\n"
      "v_pk_fma_f32 v[38:39], v[214:215], v[228:229], v[38:39] op_sel:[0,0,0] op_sel_hi:[1,0,1]\n"
      "v_pk_fma_f32 v[40:41], v[216:217], v[228:229], v[40:41] op_sel:[0,0,0] op_sel_hi:[1,0,1]\n"
      "v_pk_fma_f32 v[42:43], v[218:219], v[228:229], v[42:43] op_sel:[0,0,0] op_sel_hi:[1,0,1]\n"
      "v_pk_fma_f32 v[44:45], v[220:221], v[228:229], v[44:45] op_sel:[0,0,0] op_sel_hi:[1,0,1]\n"
      "v_pk_fma_f32 v[46:47], v[222:223], v[228:229], v[46:47] op_sel:[0,0,0] op_sel_hi:[1,0,1]\n"
      "v_cvt_pk_bf16_f32 v32, v32, v33\n"
      "v_cvt_pk_bf16_f32 v33, v34, v35\n"
      "v_cvt_pk_bf16_f32 v34, v36, v37\n"
      "v_cvt_pk_bf16_f32 v35, v38, v39\n"
      "ds_write_b128 v200, v[32:35] offset:8192\n"
      "v_cvt_pk_bf16_f32 v40, v40, v41\n"
      "v_cvt_pk_bf16_f32 v41, v42, v43\n"
      "v_cvt_pk_bf16_f32 v42, v44, v45\n"
      "v_cvt_pk_bf16_f32 v43, v46, v47\n"
      "ds_write_b128 v200, v[40:43] offset:9216\n"
      "v_pk_mul_f32 v[48:49], v[48:49], v[230:231] op_sel:[0,1] op_sel_hi:[1,1]\n"
      "v_pk_mul_f32 v[50:51], v[50:51], v[230:231] op_sel:[0,1] op_sel_hi:[1,1]\n"
      "v_pk_mul_f32 v[52:53], v[52:53], v[230:231] op_sel:[0,1] op_sel_hi:[1,1]\n"
      "v_pk_mul_f32 v[54:55], v[54:55], v[230:231] op_sel:[0,1] op_sel_hi:[1,1]\n"
      "v_pk_mul_f32 v[56:57], v[56:57], v[230:231] op_sel:[0,1] op_sel_hi:[1,1]\n"
      "v_pk_mul_f32 v[58:59], v[58:59], v[230:231] op_sel:[0,1] op_sel_hi:[1,1]\n"
      "v_pk_mul_f32 v[60:61], v[60:61], v[230:231] op_sel:[0,1] op_sel_hi:[1,1]\n"
      "v_pk_mul_f32 v[62:63], v[62:63], v[230:231] op_sel:[0,1] op_sel_hi:[1,1]\n"
      "v_pk_fma_f32 v[48:49], v[208:209], v[230:231], v[48:49] op_sel:[0,0,0] op_sel_hi:[1,0,1]\n"
      "v_pk_fma_f32 v[50:51], v[210:211], v[230:231], v[50:51] op_sel:[0,0,0] op_sel_hi:[1,0,1]\n"
      "v_pk_fma_f32 v[52:53], v[212:213], v[230:231], v[52:53] op_sel:[0,0,0] op_sel_hi:[1,0,1]\n"
      "v_pk_fma_f32 v[54:55], v[214:215], v[230:231], v[54:55] op_sel:[0,0,0] op_sel_hi:[1,0,1]\n"
      "v_pk_fma_f32 v[56:57], v[216:217], v[230:231], v[56:57] op_sel:[0,0,0] op_sel_hi:[1,0,1]\n"
      "v_pk_fma_f32 v[58:59], v[218:219], v[230:231], v[58:59] op_sel:[0,0,0] op_sel_hi:[1,0,1]\n"
      "v_pk_fma_f32 v[60:61], v[220:221], v[230:231], v[60:61] op_sel:[0,0,0] op_sel_hi:[1,0,1]\n"
      "v_pk_fma_f32 v[62:63], v[222:223], v[230:231], v[62:63] op_sel:[0,0,0] op_sel_hi:[1,0,1]\n"
      "v_cvt_pk_bf16_f32 v48, v48, v49\n"
      "v_cvt_pk_bf16_f32 v49, v50, v51\n"
      "v_cvt_pk_bf16_f32 v50, v52, v53\n"
      "v_cvt_pk_bf16_f32 v51, v54, v55\n"
      "ds_write_b128 v200, v[48:51] offset:12288\n"
      "v_cvt_pk_bf16_f32 v56, v56, v57\n"
      "v_cvt_pk_bf16_f32 v57, v58, v59\n"
      "v_cvt_pk_bf16_f32 v58, v60, v61\n"
      "v_cvt_pk_bf16_f32 v59, v62, v63\n"
      "ds_write_b128 v200, v[56:59] offset:13312\n"
      "v_pk_mul_f32 v[64:65], v[64:65], v[232:233] op_sel:[0,1] op_sel_hi:[1,1]\n"
      "v_pk_mul_f32 v[66:67], v[66:67], v[232:233] op_sel:[0,1] op_sel_hi:[1,1]\n"
      "v_pk_mul_f32 v[68:69], v[68:69], v[232:233] op_sel:[0,1] op_sel_hi:[1,1]\n"
      "v_pk_mul_f32 v[70:71], v[70:71], v[232:233] op_sel:[0,1] op_sel_hi:[1,1]\n"
      "v_pk_mul_f32 v[72:73], v[72:73], v[232:233] op_sel:[0,1] op_sel_hi:[1,1]\n"
      "v_pk_mul_f32 v[74:75], v[74:75], v[232:233] op_sel:[0,1] op_sel_hi:[1,1]\n"
      "v_pk_mul_f32 v[76:77], v[76:77], v[232:233] op_sel:[0,1] op_sel_hi:[1,1]\n"
      "v_pk_mul_f32 v[78:79], v[78:79], v[232:233] op_sel:[0,1] op_sel_hi:[1,1]\n"
      "v_pk_fma_f32 v[64:65], v[208:209], v[232:233], v[64:65] op_sel:[0,0,0] op_sel_hi:[1,0,1]\n"
      "v_pk_fma_f32 v[66:67], v[210:211], v[232:233], v[66:67] op_sel:[0,0,0] op_sel_hi:[1,0,1]\n"
      "v_pk_fma_f32 v[68:69], v[212:213], v[232:233], v[68:69] op_sel:[0,0,0] op_sel_hi:[1,0,1]\n"
      "v_pk_fma_f32 v[70:71], v[214:215], v[232:233], v[70:71] op_sel:[0,0,0] op_sel_hi:[1,0,1]\n"
      "v_pk_fma_f32 v[72:73], v[216:217], v[232:233], v[72:73] op_sel:[0,0,0] op_sel_hi:[1,0,1]\n"
      "v_pk_fma_f32 v[74:75], v[218:219], v[232:233], v[74:75] op_sel:[0,0,0] op_sel_hi:[1,0,1]\n"
      "v_pk_fma_f32 v[76:77], v[220:221], v[232:233], v[76:77] op_sel:[0,0,0] op_sel_hi:[1,0,1]\n"
      "v_pk_fma_f32 v[78:79], v[222:223], v[232:233], v[78:79] op_sel:[0,0,0] op_sel_hi:[1,0,1]\n"
      "v_cvt_pk_bf16_f32 v64, v64, v65\n"
      "v_cvt_pk_bf16_f32 v65, v66, v67\n"
      "v_cvt_pk_bf16_f32 v66, v68, v69\n"
      "v_cvt_pk_bf16_f32 v67, v70, v71\n"
      "ds_write_b128 v200, v[64:67] offset:16384\n"
      "v_cvt_pk_bf16_f32 v72, v72, v73\n"
      "v_cvt_pk_bf16_f32 v73, v74, v75\n"
      "v_cvt_pk_bf16_f32 v74, v76, v77\n"
      "v_cvt_pk_bf16_f32 v75, v78, v79\n"
      "ds_write_b128 v200, v[72:75] offset:17408\n"
      "v_pk_mul_f32 v[80:81], v[80:81], v[234:235] op_sel:[0,1] op_sel_hi:[1,1]\n"
      "v_pk_mul_f32 v[82:83], v[82:83], v[234:235] op_sel:[0,1] op_sel_hi:[1,1]\n"
      "v_pk_mul_f32 v[84:85], v[84:85], v[234:235] op_sel:[0,1] op_sel_hi:[1,1]\n"
      "v_pk_mul_f32 v[86:87], v[86:87], v[234:235] op_sel:[0,1] op_sel_hi:[1,1]\n"
      "v_pk_mul_f32 v[88:89], v[88:89], v[234:235] op_sel:[0,1] op_sel_hi:[1,1]\n"
      "v_pk_mul_f32 v[90:91], v[90:91], v[234:235] op_sel:[0,1] op_sel_hi:[1,1]\n"
      "v_pk_mul_f32 v[92:93], v[92:93], v[234:235] op_sel:[0,1] op_sel_hi:[1,1]\n"
      "v_pk_mul_f32 v[94:95], v[94:95], v[234:235] op_sel:[0,1] op_sel_hi:[1,1]\n"
      "v_pk_fma_f32 v[80:81], v[208:209], v[234:235], v[80:81] op_sel:[0,0,0] op_sel_hi:[1,0,1]\n"
      "v_pk_fma_f32 v[82:83], v[210:211], v[234:235], v[82:83] op_sel:[0,0,0] op_sel_hi:[1,0,1]\n"
      "v_pk_fma_f32 v[84:85], v[212:213], v[234:235], v[84:85] op_sel:[0,0,0] op_sel_hi:[1,0,1]\n"
      "v_pk_fma_f32 v[86:87], v[214:215], v[234:235], v[86:87] op_sel:[0,0,0] op_sel_hi:[1,0,1]\n"
      "v_pk_fma_f32 v[88:89], v[216:217], v[234:235], v[88:89] op_sel:[0,0,0] op_sel_hi:[1,0,1]\n"
      "v_pk_fma_f32 v[90:91], v[218:219], v[234:235], v[90:91] op_sel:[0,0,0] op_sel_hi:[1,0,1]\n"
      "v_pk_fma_f32 v[92:93], v[220:221], v[234:235], v[92:93] op_sel:[0,0,0] op_sel_hi:[1,0,1]\n"
      "v_pk_fma_f32 v[94:95], v[222:223], v[234:235], v[94:95] op_sel:[0,0,0] op_sel_hi:[1,0,1]\n"
      "v_cvt_pk_bf16_f32 v80, v80, v81\n"
      "v_cvt_pk_bf16_f32 v81, v82, v83\n"
      "v_cvt_pk_bf16_f32 v82, v84, v85\n"
      "v_cvt_pk_bf16_f32 v83, v86, v87\n"
      "ds_write_b128 v200, v[80:83] offset:20480\n"
      "v_cvt_pk_bf16_f32 v88, v88, v89\n"
      "v_cvt_pk_bf16_f32 v89, v90, v91\n"
      "v_cvt_pk_bf16_f32 v90, v92, v93\n"
      "v_cvt_pk_bf16_f32 v91, v94, v95\n"
      "ds_write_b128 v200, v[88:91] offset:21504\n"
      "v_pk_mul_f32 v[96:97], v[96:97], v[236:237] op_sel:[0,1] op_sel_hi:[1,1]\n"
      "v_pk_mul_f32 v[98:99], v[98:99], v[236:237] op_sel:[0,1] op_sel_hi:[1,1]\n"
      "v_pk_mul_f32 v[100:101], v[100:101], v[236:237] op_sel:[0,1] op_sel_hi:[1,1]\n"
      "v_pk_mul_f32 v[102:103], v[102:103], v[236:237] op_sel:[0,1] op_sel_hi:[1,1]\n"
      "v_pk_mul_f32 v[104:105], v[104:105], v[236:237] op_sel:[0,1] op_sel_hi:[1,1]\n"
      "v_pk_mul_f32 v[106:107], v[106:107], v[236:237] op_sel:[0,1] op_sel_hi:[1,1]\n"
      "v_pk_mul_f32 v[108:109], v[108:109], v[236:237] op_sel:[0,1] op_sel_hi:[1,1]\n"
      "v_pk_mul_f32 v[110:111], v[110:111], v[236:237] op_sel:[0,1] op_sel_hi:[1,1]\n"
      "v_pk_fma_f32 v[96:97], v[208:209], v[236:237], v[96:97] op_sel:[0,0,0] op_sel_hi:[1,0,1]\n"
      "v_pk_fma_f32 v[98:99], v[210:211], v[236:237], v[98:99] op_sel:[0,0,0] op_sel_hi:[1,0,1]\n"
      "v_pk_fma_f32 v[100:101], v[212:213], v[236:237], v[100:101] op_sel:[0,0,0] op_sel_hi:[1,0,1]\n"
      "v_pk_fma_f32 v[102:103], v[214:215], v[236:237], v[102:103] op_sel:[0,0,0] op_sel_hi:[1,0,1]\n"
      "v_pk_fma_f32 v[104:105], v[216:217], v[236:237], v[104:105] op_sel:[0,0,0] op_sel_hi:[1,0,1]\n"
      "v_pk_fma_f32 v[106:107], v[218:219], v[236:237], v[106:107] op_sel:[0,0,0] op_sel_hi:[1,0,1]\n"
      "v_pk_fma_f32 v[108:109], v[220:221], v[236:237], v[108:109] op_sel:[0,0,0] op_sel_hi:[1,0,1]\n"
      "v_pk_fma_f32 v[110:111], v[222:223], v[236:237], v[110:111] op_sel:[0,0,0] op_sel_hi:[1,0,1]\n"
      "v_cvt_pk_bf16_f32 v96, v96, v97\n"
      "v_cvt_pk_bf16_f32 v97, v98, v99\n"
      "v_cvt_pk_bf16_f32 v98, v100, v101\n"
      "v_cvt_pk_bf16_f32 v99, v102, v103\n"
      "ds_write_b128 v200, v[96:99] offset:24576\n"
      "v_cvt_pk_bf16_f32 v104, v104, v105\n"
      "v_cvt_pk_bf16_f32 v105, v106, v107\n"
      "v_cvt_pk_bf16_f32 v106, v108, v109\n"
      "v_cvt_pk_bf16_f32 v107, v110, v111\n"
      "ds_write_b128 v200, v[104:107] offset:25600\n"
      "v_pk_mul_f32 v[112:113], v[112:113], v[238:239] op_sel:[0,1] op_sel_hi:[1,1]\n"
      "v_pk_mul_f32 v[114:115], v[114:115], v[238:239] op_sel:[0,1] op_sel_hi:[1,1]\n"
      "v_pk_mul_f32 v[116:117], v[116:117], v[238:239] op_sel:[0,1] op_sel_hi:[1,1]\n"
      "v_pk_mul_f32 v[118:119], v[118:119], v[238:239] op_sel:[0,1] op_sel_hi:[1,1]\n"
      "v_pk_mul_f32 v[120:121], v[120:121], v[238:239] op_sel:[0,1] op_sel_hi:[1,1]\n"
      "v_pk_mul_f32 v[122:123], v[122:123], v[238:239] op_sel:[0,1] op_sel_hi:[1,1]\n"
      "v_pk_mul_f32 v[124:125], v[124:125], v[238:239] op_sel:[0,1] op_sel_hi:[1,1]\n"
      "v_pk_mul_f32 v[126:127], v[126:127], v[238:239] op_sel:[0,1] op_sel_hi:[1,1]\n"
      "v_pk_fma_f32 v[112:113], v[208:209], v[238:239], v[112:113] op_sel:[0,0,0] op_sel_hi:[1,0,1]\n"
      "v_pk_fma_f32 v[114:115], v[210:211], v[238:239], v[114:115] op_sel:[0,0,0] op_sel_hi:[1,0,1]\n"
      "v_pk_fma_f32 v[116:117], v[212:213], v[238:239], v[116:117] op_sel:[0,0,0] op_sel_hi:[1,0,1]\n"
      "v_pk_fma_f32 v[118:119], v[214:215], v[238:239], v[118:119] op_sel:[0,0,0] op_sel_hi:[1,0,1]\n"
      "v_pk_fma_f32 v[120:121], v[216:217], v[238:239], v[120:121] op_sel:[0,0,0] op_sel_hi:[1,0,1]\n"
      "v_pk_fma_f32 v[122:123], v[218:219], v[238:239], v[122:123] op_sel:[0,0,0] op_sel_hi:[1,0,1]\n"
      "v_pk_fma_f32 v[124:125], v[220:221], v[238:239], v[124:125] op_sel:[0,0,0] op_sel_hi:[1,0,1]\n"
      "v_pk_fma_f32 v[126:127], v[222:223], v[238:239], v[126:127] op_sel:[0,0,0] op_sel_hi:[1,0,1]\n"
      "v_cvt_pk_bf16_f32 v112, v112, v113\n"
      "v_cvt_pk_bf16_f32 v113, v114, v115\n"
      "v_cvt_pk_bf16_f32 v114, v116, v117\n"
      "v_cvt_pk_bf16_f32 v115, v118, v119\n"
      "ds_write_b128 v200, v[112:115] offset:28672\n"
      "v_cvt_pk_bf16_f32 v120, v120, v121\n"
      "v_cvt_pk_bf16_f32 v121, v122, v123\n"
      "v_cvt_pk_bf16_f32 v122, v124, v125\n"
      "v_cvt_pk_bf16_f32 v123, v126, v127\n"
      "ds_write_b128 v200, v[120:123] offset:29696\n"
      "v_add_u32 v196, 0, v248\n"
      "v_mul_u32_u24 v197, 52429, v196\n"
      "v_lshrrev_b32 v197, 20, v197\n"
      "v_mul_u32_u24 v198, %19, v197\n"
      "v_mul_u32_u24 v197, 320, v197\n"
      "v_lshlrev_b32 v196, 4, v196\n"
      "v_sub_u32 v196, v196, v197\n"
      "v_add_u32 v198, v198, v196\n"
      "global_load_dwordx4 v[252:255], v198, %16\n"
      "v_add_u32 v196, 64, v248\n"
      "v_mul_u32_u24 v197, 52429, v196\n"
      "v_lshrrev_b32 v197, 20, v197\n"
      "v_mul_u32_u24 v198, %19, v197\n"
      "v_mul_u32_u24 v197, 320, v197\n"
      "v_lshlrev_b32 v196, 4, v196\n"
      "v_sub_u32 v196, v196, v197\n"
      "v_add_u32 v198, v198, v196\n"
      "global_load_dwordx4 v[252:255], v198, %16\n"
      "v_add_u32 v196, 128, v248\n"
      "v_mul_u32_u24 v197, 52429, v196\n"
      "v_lshrrev_b32 v197, 20, v197\n"
      "v_mul_u32_u24 v198, %19, v197\n"
      "v_mul_u32_u24 v197, 320, v197\n"
      "v_lshlrev_b32 v196, 4, v196\n"
      "v_sub_u32 v196, v196, v197\n"
      "v_add_u32 v198, v198, v196\n"
      "global_load_dwordx4 v[252:255], v198, %16\n"
      "v_add_u32 v196, 192, v248\n"
      "v_mul_u32_u24 v197, 52429, v196\n"
      "v_lshrrev_b32 v197, 20, v197\n"
      "v_mul_u32_u24 v198, %19, v197\n"
      "v_mul_u32_u24 v197, 320, v197\n"
      "v_lshlrev_b32 v196, 4, v196\n"
      "v_sub_u32 v196, v196, v197\n"
      "v_add_u32 v198, v198, v196\n"
      "global_load_dwordx4 v[252:255], v198, %16\n"
      "v_add_u32 v196, 256, v248\n"
      "v_mul_u32_u24 v197, 52429, v196\n"
      "v_lshrrev_b32 v197, 20, v197\n"
      "v_mul_u32_u24 v198, %19, v197\n"
      "v_mul_u32_u24 v197, 320, v197\n"
      "v_lshlrev_b32 v196, 4, v196\n"
      "v_sub_u32 v196, v196, v197\n"
      "v_add_u32 v198, v198, v196\n"
      "global_load_dwordx4 v[252:255], v198, %16\n"
      "v_add_u32 v196, 320, v248\n"
      "v_mul_u32_u24 v197, 52429, v196\n"
      "v_lshrrev_b32 v197, 20, v197\n"
      "v_mul_u32_u24 v198, %19, v197\n"
      "v_mul_u32_u24 v197, 320, v197\n"
      "v_lshlrev_b32 v196, 4, v196\n"
      "v_sub_u32 v196, v196, v197\n"
      "v_add_u32 v198, v198, v196\n"
      "global_load_dwordx4 v[252:255], v198, %16\n"
      "v_add_u32 v196, 384, v248\n"
      "v_mul_u32_u24 v197, 52429, v196\n"
      "v_lshrrev_b32 v197, 20, v197\n"
      "v_mul_u32_u24 v198, %19, v197\n"
      "v_mul_u32_u24 v197, 320, v197\n"
      "v_lshlrev_b32 v196, 4, v196\n"
      "v_sub_u32 v196, v196, v197\n"
      "v_add_u32 v198, v198, v196\n"
      "global_load_dwordx4 v[252:255], v198, %16\n"
      "v_add_u32 v196, 448, v248\n"
      "v_mul_u32_u24 v197, 52429, v196\n"
      "v_lshrrev_b32 v197, 20, v197\n"
      "v_mul_u32_u24 v198, %19, v197\n"
      "v_mul_u32_u24 v197, 320, v197\n"
      "v_lshlrev_b32 v196, 4, v196\n"
      "v_sub_u32 v196, v196, v197\n"
      "v_add_u32 v198, v198, v196\n"
      "global_load_dwordx4 v[252:255], v198, %16\n"
      "v_add_u32 v196, 512, v248\n"
      "v_mul_u32_u24 v197, 52429, v196\n"
      "v_lshrrev_b32 v197, 20, v197\n"
      "v_mul_u32_u24 v198, %19, v197\n"
      "v_mul_u32_u24 v197, 320, v197\n"
      "v_lshlrev_b32 v196, 4, v196\n"
      "v_sub_u32 v196, v196, v197\n"
      "v_add_u32 v198, v198, v196\n"
      "global_load_dwordx4 v[252:255], v198, %16\n"
      "v_add_u32 v196, 576, v248\n"
      "v_mul_u32_u24 v197, 52429, v196\n"
      "v_lshrrev_b32 v197, 20, v197\n"
      "v_mul_u32_u24 v198, %19, v197\n"
      "v_mul_u32_u24 v197, 320, v197\n"
      "v_lshlrev_b32 v196, 4, v196\n"
      "v_sub_u32 v196, v196, v197\n"
      "v_add_u32 v198, v198, v196\n"
      "global_load_dwordx4 v[252:255], v198, %16\n"
      "v_mbcnt_lo_u32_b32 v232, -1, 0\n"
      "v_mbcnt_hi_u32_b32 v232, -1, v232\n"
      "v_lshlrev_b32 v233, 4, v232\n"
      "v_add_u32 v242, %11, v233\n"
      "s_lshr_b32 s55, %12, 1\n"
      "s_lshl_b32 s53, s55, 14\n"
      "v_add_u32 v224, s53, v242\n"
      "v_add_u32 v225, 65536, v224\n"
      "s_lshl_b32 s53, %12, 13\n"
      "s_add_u32 s49, %11, s53\n"
      "v_add_u32 v241, s53, v233\n"
      "v_subrev_u32 v240, 2, v232\n"
      "v_cmp_le_i32 vcc, %14, v240\n"
      "s_nop 1\n"
      "v_addc_co_u32 v240, vcc, 0, v240, vcc\n"
      "v_subrev_u32 v234, %13, v232\n"
      "v_cmp_le_u32 vcc, %13, v232\n"
      "s_nop 1\n"
      "v_cndmask_b32 v240, v240, v234, vcc\n"
      "s_sub_u32 s54, 15, %14\n"
      "v_writelane_b32 v240, %14, 0\n"
      "v_writelane_b32 v240, s54, 1\n"
      "v_and_b32 v240, 15, v240\n"
      "v_lshlrev_b32 v240, 15, v240\n"
      "s_lshl_b32 s54, s54, 15\n"
      "v_add_u32 v227, s54, v241\n"
      "s_lshl_b32 s54, %14, 15\n"
      "v_add_u32 v226, s54, v241\n"
      "s_sub_u32 s52, %13, 1\n"
      "s_lshl_b32 s42, 1, s52\n"
      "s_lshl_b32 s44, 1, %13\n"
      "s_or_b32 s44, s44, 1\n"
      "s_or_b32 s42, s42, s44\n"
      "s_or_b32 s42, s42, 0x14\n"
      "s_mov_b32 s43, 0x10002\n"
      "s_mov_b32 s40, 0\n"
      "s_mov_b32 s45, 0\n"
      "s_mov_b32 s41, 0x7fc00000\n"
      "s_lshl_b32 s53, %12, 1\n"
      "s_lshr_b32 s51, 0x61, s53\n"
      "s_and_b32 s51, s51, 3\n"
      "s_mov_b64 s[58:59], %9\n"
      "s_and_b32 s53, %12, 1\n"
      "s_lshl_b32 s53, s53, 14\n"
      "s_add_u32 s53, s53, 98304\n"
      "v_add_u32 v232, s53, v242\n"
      "v_bfe_u32 v248, v241, 4, 6\n"
      "s_mul_i32 s79, %12, 99\n"
      "s_lshl_b32 s53, %20, 8\n"
      "s_add_u32 s53, s53, 4096\n"
      "v_lshl_add_u32 v249, v248, 2, s53\n"
      "global_load_dword v249, v249, %15 sc1\n"
      "v_mov_b32 v128, 0\n"
      "v_mov_b32 v129, 0\n"
      "v_mov_b32 v130, 0\n"
      "v_mov_b32 v131, 0\n"
      "v_mov_b32 v132, 0\n"
      "v_mov_b32 v133, 0\n"
      "v_mov_b32 v134, 0\n"
      "v_mov_b32 v135, 0\n"
      "v_mov_b32 v136, 0\n"
      "v_mov_b32 v137, 0\n"
      "v_mov_b32 v138, 0\n"
      "v_mov_b32 v139, 0\n"
      "v_mov_b32 v140, 0\n"
      "v_mov_b32 v141, 0\n"
      "v_mov_b32 v142, 0\n"
      "v_mov_b32 v143, 0\n"
      "v_mov_b32 v244, 0x3f803f80\n"
      "v_mov_b32 v245, 0x3f803f80\n"
      "v_mov_b32 v246, 0x3f803f80\n"
      "v_mov_b32 v247, 0x3f803f80\n"
      "s_waitcnt vmcnt(63) lgkmcnt(0)\n"
      "s_barrier\n"
      "ds_read_b128 a[128:131], v232 offset:0\n"
      "ds_read_b128 a[132:135], v232 offset:1024\n"
      "ds_read_b128 a[136:139], v232 offset:2048\n"
      "ds_read_b128 a[140:143], v232 offset:3072\n"
      "ds_read_b128 a[144:147], v232 offset:4096\n"
      "ds_read_b128 a[148:151], v232 offset:5120\n"
      "ds_read_b128 a[152:155], v232 offset:6144\n"
      "ds_read_b128 a[156:159], v232 offset:7168\n"
      "ds_read_b128 a[160:163], v232 offset:8192\n"
      "ds_read_b128 a[164:167], v232 offset:9216\n"
      "ds_read_b128 a[168:171], v232 offset:10240\n"
      "ds_read_b128 a[172:175], v232 offset:11264\n"
      "ds_read_b128 a[176:179], v232 offset:12288\n"
      "ds_read_b128 a[180:183], v232 offset:13312\n"
      "ds_read_b128 a[184:187], v232 offset:14336\n"
      "ds_read_b128 a[188:191], v232 offset:15360\n"
      "ds_read_b128 a[192:195], v224 offset:0\n"
      "ds_read_b128 a[208:211], v224 offset:4096\n"
      "ds_read_b128 a[224:227], v224 offset:8192\n"
      "ds_read_b128 a[240:243], v224 offset:12288\n"
      "ds_read_b128 a[196:199], v224 offset:1024\n"
      "ds_read_b128 a[212:215], v224 offset:5120\n"
      "ds_read_b128 a[228:231], v224 offset:9216\n"
      "ds_read_b128 a[244:247], v224 offset:13312\n"
      "ds_read_b128 a[200:203], v224 offset:2048\n"
      "ds_read_b128 a[216:219], v224 offset:6144\n"
      "ds_read_b128 a[232:235], v224 offset:10240\n"
      "ds_read_b128 a[248:251], v224 offset:14336\n"
      "ds_read_b128 a[204:207], v224 offset:3072\n"
      "ds_read_b128 a[220:223], v224 offset:7168\n"
      "ds_read_b128 a[236:239], v224 offset:11264\n"
      "ds_read_b128 a[252:255], v224 offset:15360\n"
      "s_waitcnt lgkmcnt(0)\n"
      "L_loop_%=:\n"
      "v_mfma_f32_16x16x32_bf16 v[0:3], a[192:195], a[128:131], v[128:131]\n"
      "ds_read_b128 v[160:163], v225 offset:32768\n"
      "s_add_u32 m0, s49, 69632\n"
      "v_mfma_f32_16x16x32_bf16 v[8:11], a[192:195], a[144:147], v[132:135]\n"
      "global_load_lds_dwordx4 v226, s[92:93] offset:0 sc1\n"
      "v_mfma_f32_16x16x32_bf16 v[16:19], a[192:195], a[160:163], v[136:139]\n"
      "v_cvt_pk_bf16_f32 v81, v82, v83\n"
      "v_mfma_f32_16x16x32_bf16 v[24:27], a[192:195], a[176:179], v[140:143]\n"
      "ds_read_b128 v[164:167], v225 offset:34816\n"
      "v_exp_f32 v85, v85\n"
      "v_mfma_f32_16x16x32_bf16 v[4:7], a[208:211], a[128:131], v[128:131]\n"
      "v_exp_f32 v86, v86\n"
      "v_mfma_f32_16x16x32_bf16 v[12:15], a[208:211], a[144:147], v[132:135]\n"
      "v_cvt_pk_bf16_f32 v82, v84, v85\n"
      "v_mfma_f32_16x16x32_bf16 v[20:23], a[208:211], a[160:163], v[136:139]\n"
      "ds_read_b128 v[168:171], v225 offset:36864\n"
      "v_exp_f32 v87, v87\n"
      "v_mfma_f32_16x16x32_bf16 v[28:31], a[208:211], a[176:179], v[140:143]\n"
      "global_load_lds_dwordx4 v226, s[92:93] offset:1024 sc1\n"
      "v_mfma_f32_16x16x32_bf16 v[32:35], a[224:227], a[128:131], v[128:131]\n"
      "v_exp_f32 v88, v88\n"
      "v_mfma_f32_16x16x32_bf16 v[40:43], a[224:227], a[144:147], v[132:135]\n"
      "ds_read_b128 v[172:175], v225 offset:38912\n"
      "v_cvt_pk_bf16_f32 v83, v86, v87\n"
      "v_mfma_f32_16x16x32_bf16 v[48:51], a[224:227], a[160:163], v[136:139]\n"
      "v_exp_f32 v89, v89\n"
      "v_mfma_f32_16x16x32_bf16 v[56:59], a[224:227], a[176:179], v[140:143]\n"
      "v_exp_f32 v90, v90\n"
      "v_mfma_f32_16x16x32_bf16 v[36:39], a[240:243], a[128:131], v[128:131]\n"
      "ds_read_b128 v[176:179], v225 offset:40960\n"
      "v_cvt_pk_bf16_f32 v88, v88, v89\n"
      "v_mfma_f32_16x16x32_bf16 v[44:47], a[240:243], a[144:147], v[132:135]\n"
      "global_load_lds_dwordx4 v226, s[92:93] offset:2048 sc1\n"
      "v_mfma_f32_16x16x32_bf16 v[52:55], a[240:243], a[160:163], v[136:139]\n"
      "v_exp_f32 v91, v91\n"
      "v_mfma_f32_16x16x32_bf16 v[60:63], a[240:243], a[176:179], v[140:143]\n"
      "ds_read_b128 v[180:183], v225 offset:43008\n"
      "v_exp_f32 v92, v92\n"
      "v_mfma_f32_16x16x32_bf16 v[0:3], a[196:199], a[132:135], v[0:3]\n"
      "v_cvt_pk_bf16_f32 v89, v90, v91\n"
      "v_exp_f32 v93, v93\n"
      "v_mfma_f32_16x16x32_bf16 v[8:11], a[196:199], a[148:151], v[8:11]\n"
      "v_exp_f32 v94, v94\n"
      "v_mfma_f32_16x16x32_bf16 v[16:19], a[196:199], a[164:167], v[16:19]\n"
      "ds_read_b128 v[184:187], v225 offset:45056\n"
      "v_cvt_pk_bf16_f32 v90, v92, v93\n"
      "v_mfma_f32_16x16x32_bf16 v[24:27], a[196:199], a[180:183], v[24:27]\n"
      "global_load_lds_dwordx4 v226, s[92:93] offset:3072 sc1\n"
      "v_mfma_f32_16x16x32_bf16 v[4:7], a[212:215], a[132:135], v[4:7]\n"
      "v_exp_f32 v95, v95\n"
      "v_mfma_f32_16x16x32_bf16 v[12:15], a[212:215], a[148:151], v[12:15]\n"
      "ds_read_b128 v[188:191], v225 offset:47104\n"
      "v_exp_f32 v96, v96\n"
      "v_mfma_f32_16x16x32_bf16 v[20:23], a[212:215], a[164:167], v[20:23]\n"
      "v_cvt_pk_bf16_f32 v91, v94, v95\n"
      "v_mfma_f32_16x16x32_bf16 v[28:31], a[212:215], a[180:183], v[28:31]\n"
      "v_exp_f32 v97, v97\n"
      "v_mfma_f32_16x16x32_bf16 v[32:35], a[228:231], a[132:135], v[32:35]\n"
      "ds_read_b128 v[192:195], v225 offset:33792\n"
      "v_exp_f32 v98, v98\n"
      "v_mfma_f32_16x16x32_bf16 v[40:43], a[228:231], a[148:151], v[40:43]\n"
      "v_cvt_pk_bf16_f32 v96, v96, v97\n"
      "v_exp_f32 v99, v99\n"
      "v_mfma_f32_16x16x32_bf16 v[48:51], a[228:231], a[164:167], v[48:51]\n"
      "v_exp_f32 v100, v100\n"
      "v_mfma_f32_16x16x32_bf16 v[56:59], a[228:231], a[180:183], v[56:59]\n"
      "ds_read_b128 v[196:199], v225 offset:35840\n"
      "v_cvt_pk_bf16_f32 v97, v98, v99\n"
      "v_mfma_f32_16x16x32_bf16 v[36:39], a[244:247], a[132:135], v[36:39]\n"
      "v_exp_f32 v101, v101\n"
      "v_mfma_f32_16x16x32_bf16 v[44:47], a[244:247], a[148:151], v[44:47]\n"
      "v_exp_f32 v102, v102\n"
      "v_mfma_f32_16x16x32_bf16 v[52:55], a[244:247], a[164:167], v[52:55]\n"
      "ds_read_b128 v[200:203], v225 offset:37888\n"
      "v_cvt_pk_bf16_f32 v98, v100, v101\n"
      "v_mfma_f32_16x16x32_bf16 v[60:63], a[244:247], a[180:183], v[60:63]\n"
      "v_exp_f32 v103, v103\n"
      "v_mfma_f32_16x16x32_bf16 v[0:3], a[200:203], a[136:139], v[0:3]\n"
      "v_exp_f32 v104, v104\n"
      "v_mfma_f32_16x16x32_bf16 v[8:11], a[200:203], a[152:155], v[8:11]\n"
      "ds_read_b128 v[204:207], v225 offset:39936\n"
      "v_cvt_pk_bf16_f32 v99, v102, v103\n"
      "v_mfma_f32_16x16x32_bf16 v[16:19], a[200:203], a[168:171], v[16:19]\n"
      "v_exp_f32 v105, v105\n"
      "v_mfma_f32_16x16x32_bf16 v[24:27], a[200:203], a[184:187], v[24:27]\n"
      "v_exp_f32 v106, v106\n"
      "v_mfma_f32_16x16x32_bf16 v[4:7], a[216:219], a[136:139], v[4:7]\n"
      "ds_read_b128 v[208:211], v225 offset:41984\n"
      "v_cvt_pk_bf16_f32 v104, v104, v105\n"
      "v_mfma_f32_16x16x32_bf16 v[12:15], a[216:219], a[152:155], v[12:15]\n"
      "v_exp_f32 v107, v107\n"
      "v_mfma_f32_16x16x32_bf16 v[20:23], a[216:219], a[168:171], v[20:23]\n"
      "v_exp_f32 v108, v108\n"
      "v_mfma_f32_16x16x32_bf16 v[28:31], a[216:219], a[184:187], v[28:31]\n"
      "ds_read_b128 v[212:215], v225 offset:44032\n"
      "v_cvt_pk_bf16_f32 v105, v106, v107\n"
      "v_mfma_f32_16x16x32_bf16 v[32:35], a[232:235], a[136:139], v[32:35]\n"
      "v_exp_f32 v109, v109\n"
      "v_mfma_f32_16x16x32_bf16 v[40:43], a[232:235], a[152:155], v[40:43]\n"
      "v_exp_f32 v110, v110\n"
      "v_mfma_f32_16x16x32_bf16 v[48:51], a[232:235], a[168:171], v[48:51]\n"
      "ds_read_b128 v[216:219], v225 offset:46080\n"
      "v_cvt_pk_bf16_f32 v106, v108, v109\n"
      "v_mfma_f32_16x16x32_bf16 v[56:59], a[232:235], a[184:187], v[56:59]\n"
      "v_exp_f32 v111, v111\n"
      "v_mfma_f32_16x16x32_bf16 v[36:39], a[248:251], a[136:139], v[36:39]\n"
      "v_exp_f32 v112, v112\n"
      "v_mfma_f32_16x16x32_bf16 v[44:47], a[248:251], a[152:155], v[44:47]\n"
      "ds_read_b128 v[220:223], v225 offset:48128\n"
      "v_cvt_pk_bf16_f32 v107, v110, v111\n"
      "v_mfma_f32_16x16x32_bf16 v[52:55], a[248:251], a[168:171], v[52:55]\n"
      "v_exp_f32 v113, v113\n"
      "v_mfma_f32_16x16x32_bf16 v[60:63], a[248:251], a[184:187], v[60:63]\n"
      "v_exp_f32 v114, v114\n"
      "v_mfma_f32_16x16x32_bf16 v[0:3], a[204:207], a[140:143], v[0:3]\n"
      "v_cvt_pk_bf16_f32 v112, v112, v113\n"
      "v_exp_f32 v115, v115\n"
      "v_mfma_f32_16x16x32_bf16 v[8:11], a[204:207], a[156:159], v[8:11]\n"
      "v_exp_f32 v116, v116\n"
      "v_mfma_f32_16x16x32_bf16 v[16:19], a[204:207], a[172:175], v[16:19]\n"
      "v_cvt_pk_bf16_f32 v113, v114, v115\n"
      "v_exp_f32 v117, v117\n"
      "v_mfma_f32_16x16x32_bf16 v[24:27], a[204:207], a[188:191], v[24:27]\n"
      "v_exp_f32 v118, v118\n"
      "v_mfma_f32_16x16x32_bf16 v[4:7], a[220:223], a[140:143], v[4:7]\n"
      "v_cvt_pk_bf16_f32 v114, v116, v117\n"
      "v_exp_f32 v119, v119\n"
      "v_mfma_f32_16x16x32_bf16 v[12:15], a[220:223], a[156:159], v[12:15]\n"
      "v_exp_f32 v120, v120\n"
      "v_mfma_f32_16x16x32_bf16 v[20:23], a[220:223], a[172:175], v[20:23]\n"
      "v_cvt_pk_bf16_f32 v115, v118, v119\n"
      "v_exp_f32 v121, v121\n"
      "v_mfma_f32_16x16x32_bf16 v[28:31], a[220:223], a[188:191], v[28:31]\n"
      "v_exp_f32 v122, v122\n"
      "v_mfma_f32_16x16x32_bf16 v[32:35], a[236:239], a[140:143], v[32:35]\n"
      "v_cvt_pk_bf16_f32 v120, v120, v121\n"
      "v_exp_f32 v123, v123\n"
      "v_mfma_f32_16x16x32_bf16 v[40:43], a[236:239], a[156:159], v[40:43]\n"
      "s_add_u32 s48, s40, 2\n"
      "v_exp_f32 v124, v124\n"
      "v_mfma_f32_16x16x32_bf16 v[48:51], a[236:239], a[172:175], v[48:51]\n"
      "v_cvt_pk_bf16_f32 v121, v122, v123\n"
      "v_mfma_f32_16x16x32_bf16 v[56:59], a[236:239], a[188:191], v[56:59]\n"
      "v_readlane_b32 s46, v240, s48\n"
      "v_exp_f32 v125, v125\n"
      "v_mfma_f32_16x16x32_bf16 v[36:39], a[252:255], a[140:143], v[36:39]\n"
      "v_exp_f32 v126, v126\n"
      "v_mfma_f32_16x16x32_bf16 v[44:47], a[252:255], a[156:159], v[44:47]\n"
      "v_cvt_pk_bf16_f32 v122, v124, v125\n"
      "v_mfma_f32_16x16x32_bf16 v[52:55], a[252:255], a[172:175], v[52:55]\n"
      "v_add_u32 v226, s46, v241\n"
      "v_exp_f32 v127, v127\n"
      "v_mfma_f32_16x16x32_bf16 v[60:63], a[252:255], a[188:191], v[60:63]\n"
      "v_cvt_pk_bf16_f32 v123, v126, v127\n"
      "s_waitcnt vmcnt(0) lgkmcnt(0)\n"
      "s_cmp_eq_u32 s40, s79\n"
      "s_cbranch_scc1 L_poll_%=\n"
      "L_pollok_%=:\n"
      "s_barrier\n"
      "v_mfma_f32_16x16x32_bf16 a[0:3], v[160:163], v[64:67], a[0:3]\n"
      "ds_read_b128 a[192:195], v224 offset:32768\n"
      "v_mfma_f32_16x16x32_bf16 a[8:11], v[160:163], v[72:75], a[8:11]\n"
      "s_add_u32 m0, s49, 0\n"
      "v_mfma_f32_16x16x32_bf16 a[16:19], v[160:163], v[80:83], a[16:19]\n"
      "ds_read_b128 a[208:211], v224 offset:36864\n"
      "global_load_lds_dwordx4 v226, s[72:73] offset:0 sc1\n"
      "v_mfma_f32_16x16x32_bf16 a[24:27], v[160:163], v[88:91], a[24:27]\n"
      "v_mfma_f32_16x16x32_bf16 a[4:7], v[164:167], v[64:67], a[4:7]\n"
      "v_mfma_f32_16x16x32_bf16 a[12:15], v[164:167], v[72:75], a[12:15]\n"
      "ds_read_b128 a[224:227], v224 offset:40960\n"
      "s_bitcmp1_b32 s43, s40\n"
      "s_cbranch_scc1 L_mask0_%=\n"
      "L_maskback0_%=:\n"
      "v_mfma_f32_16x16x32_bf16 a[20:23], v[164:167], v[80:83], a[20:23]\n"
      "v_max3_f32 v228, v0, v1, v2\n"
      "v_max3_f32 v229, v8, v9, v10\n"
      "v_mfma_f32_16x16x32_bf16 a[28:31], v[164:167], v[88:91], a[28:31]\n"
      "ds_read_b128 a[240:243], v224 offset:45056\n"
      "global_load_lds_dwordx4 v226, s[72:73] offset:1024 sc1\n"
      "v_mfma_f32_16x16x32_bf16 a[32:35], v[168:171], v[64:67], a[32:35]\n"
      "v_max3_f32 v230, v16, v17, v18\n"
      "v_max3_f32 v231, v24, v25, v26\n"
      "v_mfma_f32_16x16x32_bf16 a[40:43], v[168:171], v[72:75], a[40:43]\n"
      "v_max3_f32 v228, v228, v3, v4\n"
      "v_max3_f32 v229, v229, v11, v12\n"
      "v_mfma_f32_16x16x32_bf16 a[48:51], v[168:171], v[80:83], a[48:51]\n"
      "ds_read_b128 a[196:199], v224 offset:33792\n"
      "v_max3_f32 v230, v230, v19, v20\n"
      "v_mfma_f32_16x16x32_bf16 a[56:59], v[168:171], v[88:91], a[56:59]\n"
      "v_max3_f32 v231, v231, v27, v28\n"
      "v_max3_f32 v228, v228, v5, v6\n"
      "v_mfma_f32_16x16x32_bf16 a[36:39], v[172:175], v[64:67], a[36:39]\n"
      "ds_read_b128 a[212:215], v224 offset:37888\n"
      "v_max3_f32 v229, v229, v13, v14\n"
      "v_mfma_f32_16x16x32_bf16 a[44:47], v[172:175], v[72:75], a[44:47]\n"
      "global_load_lds_dwordx4 v226, s[72:73] offset:2048 sc1\n"
      "v_mfma_f32_16x16x32_bf16 a[52:55], v[172:175], v[80:83], a[52:55]\n"
      "v_max3_f32 v230, v230, v21, v22\n"
      "v_max3_f32 v231, v231, v29, v30\n"
      "v_mfma_f32_16x16x32_bf16 a[60:63], v[172:175], v[88:91], a[60:63]\n"
      "ds_read_b128 a[228:231], v224 offset:41984\n"
      "v_max3_f32 v228, v228, v7, v32\n"
      "v_mfma_f32_16x16x32_bf16 a[64:67], v[176:179], v[64:67], a[64:67]\n"
      "v_max3_f32 v229, v229, v15, v40\n"
      "v_max3_f32 v230, v230, v23, v48\n"
      "v_mfma_f32_16x16x32_bf16 a[72:75], v[176:179], v[72:75], a[72:75]\n"
      "ds_read_b128 a[244:247], v224 offset:46080\n"
      "v_max3_f32 v231, v231, v31, v56\n"
      "v_mfma_f32_16x16x32_bf16 a[80:83], v[176:179], v[80:83], a[80:83]\n"
      "v_max3_f32 v228, v228, v33, v34\n"
      "v_max3_f32 v229, v229, v41, v42\n"
      "v_mfma_f32_16x16x32_bf16 a[88:91], v[176:179], v[88:91], a[88:91]\n"
      "global_load_lds_dwordx4 v226, s[72:73] offset:3072 sc1\n"
      "v_mfma_f32_16x16x32_bf16 a[68:71], v[180:183], v[64:67], a[68:71]\n"
      "ds_read_b128 a[200:203], v224 offset:34816\n"
      "v_max3_f32 v230, v230, v49, v50\n"
      "v_mfma_f32_16x16x32_bf16 a[76:79], v[180:183], v[72:75], a[76:79]\n"
      "v_max3_f32 v231, v231, v57, v58\n"
      "v_max3_f32 v228, v228, v35, v36\n"
      "v_mfma_f32_16x16x32_bf16 a[84:87], v[180:183], v[80:83], a[84:87]\n"
      "ds_read_b128 a[216:219], v224 offset:38912\n"
      "v_max3_f32 v229, v229, v43, v44\n"
      "v_mfma_f32_16x16x32_bf16 a[92:95], v[180:183], v[88:91], a[92:95]\n"
      "s_add_u32 m0, s49, 4096\n"
      "v_max3_f32 v230, v230, v51, v52\n"
      "v_mfma_f32_16x16x32_bf16 a[96:99], v[184:187], v[64:67], a[96:99]\n"
      "global_load_lds_dwordx4 v226, s[90:91] offset:0 sc1\n"
      "v_mfma_f32_16x16x32_bf16 a[104:107], v[184:187], v[72:75], a[104:107]\n"
      "ds_read_b128 a[232:235], v224 offset:43008\n"
      "v_max3_f32 v231, v231, v59, v60\n"
      "v_mfma_f32_16x16x32_bf16 a[112:115], v[184:187], v[80:83], a[112:115]\n"
      "v_max3_f32 v228, v228, v37, v38\n"
      "v_max3_f32 v229, v229, v45, v46\n"
      "v_mfma_f32_16x16x32_bf16 a[120:123], v[184:187], v[88:91], a[120:123]\n"
      "ds_read_b128 a[248:251], v224 offset:47104\n"
      "v_max3_f32 v230, v230, v53, v54\n"
      "v_mfma_f32_16x16x32_bf16 a[100:103], v[188:191], v[64:67], a[100:103]\n"
      "v_max3_f32 v231, v231, v61, v62\n"
      "v_max_f32 v228, v228, v39\n"
      "v_mfma_f32_16x16x32_bf16 a[108:111], v[188:191], v[72:75], a[108:111]\n"
      "v_max_f32 v229, v229, v47\n"
      "v_max_f32 v230, v230, v55\n"
      "v_mfma_f32_16x16x32_bf16 a[116:119], v[188:191], v[80:83], a[116:119]\n"
      "ds_read_b128 a[204:207], v224 offset:35840\n"
      "global_load_lds_dwordx4 v226, s[90:91] offset:1024 sc1\n"
      "v_mfma_f32_16x16x32_bf16 a[124:127], v[188:191], v[88:91], a[124:127]\n"
      "v_max_f32 v231, v231, v63\n"
      "v_max3_f32 v232, v228, v229, v230\n"
      "v_mfma_f32_16x16x32_bf16 v[144:147], v[244:247], v[64:67], v[144:147]\n"
      "ds_read_b128 a[220:223], v224 offset:39936\n"
      "v_max_f32 v232, v232, v231\n"
      "v_mfma_f32_16x16x32_bf16 v[148:151], v[244:247], v[72:75], v[148:151]\n"
      "v_cmp_nle_f32_e64 vcc, v232, s41\n"
      "s_cbranch_vccnz L_rare0_%=\n"
      "L_rareback0_%=:\n"
      "v_mfma_f32_16x16x32_bf16 v[152:155], v[244:247], v[80:83], v[152:155]\n"
      "v_exp_f32 v0, v0\n"
      "v_mfma_f32_16x16x32_bf16 v[156:159], v[244:247], v[88:91], v[156:159]\n"
      "ds_read_b128 a[236:239], v224 offset:44032\n"
      "v_mfma_f32_16x16x32_bf16 a[0:3], v[192:195], v[96:99], a[0:3]\n"
      "global_load_lds_dwordx4 v226, s[90:91] offset:2048 sc1\n"
      "v_mfma_f32_16x16x32_bf16 a[8:11], v[192:195], v[104:107], a[8:11]\n"
      "ds_read_b128 a[252:255], v224 offset:48128\n"
      "v_mfma_f32_16x16x32_bf16 a[16:19], v[192:195], v[112:115], a[16:19]\n"
      "v_exp_f32 v1, v1\n"
      "v_mfma_f32_16x16x32_bf16 a[24:27], v[192:195], v[120:123], a[24:27]\n"
      "v_exp_f32 v2, v2\n"
      "v_mfma_f32_16x16x32_bf16 a[4:7], v[196:199], v[96:99], a[4:7]\n"
      "v_cvt_pk_bf16_f32 v0, v0, v1\n"
      "v_mfma_f32_16x16x32_bf16 a[12:15], v[196:199], v[104:107], a[12:15]\n"
      "global_load_lds_dwordx4 v226, s[90:91] offset:3072 sc1\n"
      "v_mfma_f32_16x16x32_bf16 a[20:23], v[196:199], v[112:115], a[20:23]\n"
      "v_exp_f32 v3, v3\n"
      "v_mfma_f32_16x16x32_bf16 a[28:31], v[196:199], v[120:123], a[28:31]\n"
      "v_exp_f32 v4, v4\n"
      "v_mfma_f32_16x16x32_bf16 a[32:35], v[200:203], v[96:99], a[32:35]\n"
      "v_cvt_pk_bf16_f32 v1, v2, v3\n"
      "v_mfma_f32_16x16x32_bf16 a[40:43], v[200:203], v[104:107], a[40:43]\n"
      "v_exp_f32 v5, v5\n"
      "v_mfma_f32_16x16x32_bf16 a[48:51], v[200:203], v[112:115], a[48:51]\n"
      "s_add_u32 m0, s49, 98304\n"
      "v_mfma_f32_16x16x32_bf16 a[56:59], v[200:203], v[120:123], a[56:59]\n"
      "global_load_lds_dwordx4 v227, s[74:75] offset:0 sc1\n"
      "v_mfma_f32_16x16x32_bf16 a[36:39], v[204:207], v[96:99], a[36:39]\n"
      "v_exp_f32 v6, v6\n"
      "v_mfma_f32_16x16x32_bf16 a[44:47], v[204:207], v[104:107], a[44:47]\n"
      "v_cvt_pk_bf16_f32 v2, v4, v5\n"
      "v_mfma_f32_16x16x32_bf16 a[52:55], v[204:207], v[112:115], a[52:55]\n"
      "v_exp_f32 v7, v7\n"
      "v_mfma_f32_16x16x32_bf16 a[60:63], v[204:207], v[120:123], a[60:63]\n"
      "v_exp_f32 v8, v8\n"
      "v_mfma_f32_16x16x32_bf16 a[64:67], v[208:211], v[96:99], a[64:67]\n"
      "v_cvt_pk_bf16_f32 v3, v6, v7\n"
      "v_mfma_f32_16x16x32_bf16 a[72:75], v[208:211], v[104:107], a[72:75]\n"
      "global_load_lds_dwordx4 v227, s[74:75] offset:1024 sc1\n"
      "v_mfma_f32_16x16x32_bf16 a[80:83], v[208:211], v[112:115], a[80:83]\n"
      "v_exp_f32 v9, v9\n"
      "v_mfma_f32_16x16x32_bf16 a[88:91], v[208:211], v[120:123], a[88:91]\n"
      "v_exp_f32 v10, v10\n"
      "v_mfma_f32_16x16x32_bf16 a[68:71], v[212:215], v[96:99], a[68:71]\n"
      "v_cvt_pk_bf16_f32 v8, v8, v9\n"
      "v_mfma_f32_16x16x32_bf16 a[76:79], v[212:215], v[104:107], a[76:79]\n"
      "v_exp_f32 v11, v11\n"
      "v_mfma_f32_16x16x32_bf16 a[84:87], v[212:215], v[112:115], a[84:87]\n"
      "global_load_lds_dwordx4 v227, s[74:75] offset:2048 sc1\n"
      "v_mfma_f32_16x16x32_bf16 a[92:95], v[212:215], v[120:123], a[92:95]\n"
      "v_exp_f32 v12, v12\n"
      "v_mfma_f32_16x16x32_bf16 a[96:99], v[216:219], v[96:99], a[96:99]\n"
      "v_cvt_pk_bf16_f32 v9, v10, v11\n"
      "v_mfma_f32_16x16x32_bf16 a[104:107], v[216:219], v[104:107], a[104:107]\n"
      "v_exp_f32 v13, v13\n"
      "v_mfma_f32_16x16x32_bf16 a[112:115], v[216:219], v[112:115], a[112:115]\n"
      "v_exp_f32 v14, v14\n"
      "v_mfma_f32_16x16x32_bf16 a[120:123], v[216:219], v[120:123], a[120:123]\n"
      "v_cvt_pk_bf16_f32 v10, v12, v13\n"
      "v_mfma_f32_16x16x32_bf16 a[100:103], v[220:223], v[96:99], a[100:103]\n"
      "global_load_lds_dwordx4 v227, s[74:75] offset:3072 sc1\n"
      "v_mfma_f32_16x16x32_bf16 a[108:111], v[220:223], v[104:107], a[108:111]\n"
      "v_exp_f32 v15, v15\n"
      "v_mfma_f32_16x16x32_bf16 a[116:119], v[220:223], v[112:115], a[116:119]\n"
      "v_exp_f32 v16, v16\n"
      "v_mfma_f32_16x16x32_bf16 a[124:127], v[220:223], v[120:123], a[124:127]\n"
      "v_cvt_pk_bf16_f32 v11, v14, v15\n"
      "v_mfma_f32_16x16x32_bf16 v[144:147], v[244:247], v[96:99], v[144:147]\n"
      "v_exp_f32 v17, v17\n"
      "v_mfma_f32_16x16x32_bf16 v[148:151], v[244:247], v[104:107], v[148:151]\n"
      "v_exp_f32 v18, v18\n"
      "v_mfma_f32_16x16x32_bf16 v[152:155], v[244:247], v[112:115], v[152:155]\n"
      "v_cvt_pk_bf16_f32 v16, v16, v17\n"
      "v_exp_f32 v19, v19\n"
      "v_mfma_f32_16x16x32_bf16 v[156:159], v[244:247], v[120:123], v[156:159]\n"
      "v_exp_f32 v20, v20\n"
      "s_bitcmp1_b32 s42, s40\n"
      "s_cbranch_scc0 L_nohook0_%=\n"
      "s_call_b64 s[56:57], L_hook_%=\n"
      "L_nohook0_%=:\n"
      "s_waitcnt lgkmcnt(0)\n"
      "s_cmp_eq_u32 s40, 16\n"
      "s_cbranch_scc1 L_drain_%=\n"
      "s_add_u32 s40, s40, 1\n"
      "v_mfma_f32_16x16x32_bf16 v[64:67], a[192:195], a[128:131], v[128:131]\n"
      "ds_read_b128 v[160:163], v225 offset:0\n"
      "s_add_u32 m0, s49, 102400\n"
      "v_mfma_f32_16x16x32_bf16 v[72:75], a[192:195], a[144:147], v[132:135]\n"
      "global_load_lds_dwordx4 v227, s[92:93] offset:0 sc1\n"
      "v_mfma_f32_16x16x32_bf16 v[80:83], a[192:195], a[160:163], v[136:139]\n"
      "v_cvt_pk_bf16_f32 v17, v18, v19\n"
      "v_mfma_f32_16x16x32_bf16 v[88:91], a[192:195], a[176:179], v[140:143]\n"
      "ds_read_b128 v[164:167], v225 offset:2048\n"
      "v_exp_f32 v21, v21\n"
      "v_mfma_f32_16x16x32_bf16 v[68:71], a[208:211], a[128:131], v[128:131]\n"
      "v_exp_f32 v22, v22\n"
      "v_mfma_f32_16x16x32_bf16 v[76:79], a[208:211], a[144:147], v[132:135]\n"
      "v_cvt_pk_bf16_f32 v18, v20, v21\n"
      "v_mfma_f32_16x16x32_bf16 v[84:87], a[208:211], a[160:163], v[136:139]\n"
      "ds_read_b128 v[168:171], v225 offset:4096\n"
      "v_exp_f32 v23, v23\n"
      "v_mfma_f32_16x16x32_bf16 v[92:95], a[208:211], a[176:179], v[140:143]\n"
      "global_load_lds_dwordx4 v227, s[92:93] offset:1024 sc1\n"
      "v_mfma_f32_16x16x32_bf16 v[96:99], a[224:227], a[128:131], v[128:131]\n"
      "v_exp_f32 v24, v24\n"
      "v_mfma_f32_16x16x32_bf16 v[104:107], a[224:227], a[144:147], v[132:135]\n"
      "ds_read_b128 v[172:175], v225 offset:6144\n"
      "v_cvt_pk_bf16_f32 v19, v22, v23\n"
      "v_mfma_f32_16x16x32_bf16 v[112:115], a[224:227], a[160:163], v[136:139]\n"
      "v_exp_f32 v25, v25\n"
      "v_mfma_f32_16x16x32_bf16 v[120:123], a[224:227], a[176:179], v[140:143]\n"
      "v_exp_f32 v26, v26\n"
      "v_mfma_f32_16x16x32_bf16 v[100:103], a[240:243], a[128:131], v[128:131]\n"
      "ds_read_b128 v[176:179], v225 offset:8192\n"
      "v_cvt_pk_bf16_f32 v24, v24, v25\n"
      "v_mfma_f32_16x16x32_bf16 v[108:111], a[240:243], a[144:147], v[132:135]\n"
      "global_load_lds_dwordx4 v227, s[92:93] offset:2048 sc1\n"
      "v_mfma_f32_16x16x32_bf16 v[116:119], a[240:243], a[160:163], v[136:139]\n"
      "v_exp_f32 v27, v27\n"
      "v_mfma_f32_16x16x32_bf16 v[124:127], a[240:243], a[176:179], v[140:143]\n"
      "ds_read_b128 v[180:183], v225 offset:10240\n"
      "v_exp_f32 v28, v28\n"
      "v_mfma_f32_16x16x32_bf16 v[64:67], a[196:199], a[132:135], v[64:67]\n"
      "v_cvt_pk_bf16_f32 v25, v26, v27\n"
      "v_exp_f32 v29, v29\n"
      "v_mfma_f32_16x16x32_bf16 v[72:75], a[196:199], a[148:151], v[72:75]\n"
      "v_exp_f32 v30, v30\n"
      "v_mfma_f32_16x16x32_bf16 v[80:83], a[196:199], a[164:167], v[80:83]\n"
      "ds_read_b128 v[184:187], v225 offset:12288\n"
      "v_cvt_pk_bf16_f32 v26, v28, v29\n"
      "v_mfma_f32_16x16x32_bf16 v[88:91], a[196:199], a[180:183], v[88:91]\n"
      "global_load_lds_dwordx4 v227, s[92:93] offset:3072 sc1\n"
      "v_mfma_f32_16x16x32_bf16 v[68:71], a[212:215], a[132:135], v[68:71]\n"
      "v_exp_f32 v31, v31\n"
      "v_mfma_f32_16x16x32_bf16 v[76:79], a[212:215], a[148:151], v[76:79]\n"
      "ds_read_b128 v[188:191], v225 offset:14336\n"
      "v_exp_f32 v32, v32\n"
      "v_mfma_f32_16x16x32_bf16 v[84:87], a[212:215], a[164:167], v[84:87]\n"
      "v_cvt_pk_bf16_f32 v27, v30, v31\n"
      "v_mfma_f32_16x16x32_bf16 v[92:95], a[212:215], a[180:183], v[92:95]\n"
      "v_exp_f32 v33, v33\n"
      "v_mfma_f32_16x16x32_bf16 v[96:99], a[228:231], a[132:135], v[96:99]\n"
      "ds_read_b128 v[192:195], v225 offset:1024\n"
      "v_exp_f32 v34, v34\n"
      "v_mfma_f32_16x16x32_bf16 v[104:107], a[228:231], a[148:151], v[104:107]\n"
      "v_cvt_pk_bf16_f32 v32, v32, v33\n"
      "v_exp_f32 v35, v35\n"
      "v_mfma_f32_16x16x32_bf16 v[112:115], a[228:231], a[164:167], v[112:115]\n"
      "v_exp_f32 v36, v36\n"
      "v_mfma_f32_16x16x32_bf16 v[120:123], a[228:231], a[180:183], v[120:123]\n"
      "ds_read_b128 v[196:199], v225 offset:3072\n"
      "v_cvt_pk_bf16_f32 v33, v34, v35\n"
      "v_mfma_f32_16x16x32_bf16 v[100:103], a[244:247], a[132:135], v[100:103]\n"
      "v_exp_f32 v37, v37\n"
      "v_mfma_f32_16x16x32_bf16 v[108:111], a[244:247], a[148:151], v[108:111]\n"
      "v_exp_f32 v38, v38\n"
      "v_mfma_f32_16x16x32_bf16 v[116:119], a[244:247], a[164:167], v[116:119]\n"
      "ds_read_b128 v[200:203], v225 offset:5120\n"
      "v_cvt_pk_bf16_f32 v34, v36, v37\n"
      "v_mfma_f32_16x16x32_bf16 v[124:127], a[244:247], a[180:183], v[124:127]\n"
      "v_exp_f32 v39, v39\n"
      "v_mfma_f32_16x16x32_bf16 v[64:67], a[200:203], a[136:139], v[64:67]\n"
      "v_exp_f32 v40, v40\n"
      "v_mfma_f32_16x16x32_bf16 v[72:75], a[200:203], a[152:155], v[72:75]\n"
      "ds_read_b128 v[204:207], v225 offset:7168\n"
      "v_cvt_pk_bf16_f32 v35, v38, v39\n"
      "v_mfma_f32_16x16x32_bf16 v[80:83], a[200:203], a[168:171], v[80:83]\n"
      "v_exp_f32 v41, v41\n"
      "v_mfma_f32_16x16x32_bf16 v[88:91], a[200:203], a[184:187], v[88:91]\n"
      "v_exp_f32 v42, v42\n"
      "v_mfma_f32_16x16x32_bf16 v[68:71], a[216:219], a[136:139], v[68:71]\n"
      "ds_read_b128 v[208:211], v225 offset:9216\n"
      "v_cvt_pk_bf16_f32 v40, v40, v41\n"
      "v_mfma_f32_16x16x32_bf16 v[76:79], a[216:219], a[152:155], v[76:79]\n"
      "v_exp_f32 v43, v43\n"
      "v_mfma_f32_16x16x32_bf16 v[84:87], a[216:219], a[168:171], v[84:87]\n"
      "v_exp_f32 v44, v44\n"
      "v_mfma_f32_16x16x32_bf16 v[92:95], a[216:219], a[184:187], v[92:95]\n"
      "ds_read_b128 v[212:215], v225 offset:11264\n"
      "v_cvt_pk_bf16_f32 v41, v42, v43\n"
      "v_mfma_f32_16x16x32_bf16 v[96:99], a[232:235], a[136:139], v[96:99]\n"
      "v_exp_f32 v45, v45\n"
      "v_mfma_f32_16x16x32_bf16 v[104:107], a[232:235], a[152:155], v[104:107]\n"
      "v_exp_f32 v46, v46\n"
      "v_mfma_f32_16x16x32_bf16 v[112:115], a[232:235], a[168:171], v[112:115]\n"
      "ds_read_b128 v[216:219], v225 offset:13312\n"
      "v_cvt_pk_bf16_f32 v42, v44, v45\n"
      "v_mfma_f32_16x16x32_bf16 v[120:123], a[232:235], a[184:187], v[120:123]\n"
      "v_exp_f32 v47, v47\n"
      "v_mfma_f32_16x16x32_bf16 v[100:103], a[248:251], a[136:139], v[100:103]\n"
      "v_exp_f32 v48, v48\n"
      "v_mfma_f32_16x16x32_bf16 v[108:111], a[248:251], a[152:155], v[108:111]\n"
      "ds_read_b128 v[220:223], v225 offset:15360\n"
      "v_cvt_pk_bf16_f32 v43, v46, v47\n"
      "v_mfma_f32_16x16x32_bf16 v[116:119], a[248:251], a[168:171], v[116:119]\n"
      "v_exp_f32 v49, v49\n"
      "v_mfma_f32_16x16x32_bf16 v[124:127], a[248:251], a[184:187], v[124:127]\n"
      "v_exp_f32 v50, v50\n"
      "v_mfma_f32_16x16x32_bf16 v[64:67], a[204:207], a[140:143], v[64:67]\n"
      "v_cvt_pk_bf16_f32 v48, v48, v49\n"
      "v_exp_f32 v51, v51\n"
      "v_mfma_f32_16x16x32_bf16 v[72:75], a[204:207], a[156:159], v[72:75]\n"
      "v_exp_f32 v52, v52\n"
      "v_mfma_f32_16x16x32_bf16 v[80:83], a[204:207], a[172:175], v[80:83]\n"
      "v_cvt_pk_bf16_f32 v49, v50, v51\n"
      "v_exp_f32 v53, v53\n"
      "v_mfma_f32_16x16x32_bf16 v[88:91], a[204:207], a[188:191], v[88:91]\n"
      "v_exp_f32 v54, v54\n"
      "v_mfma_f32_16x16x32_bf16 v[68:71], a[220:223], a[140:143], v[68:71]\n"
      "v_cvt_pk_bf16_f32 v50, v52, v53\n"
      "v_exp_f32 v55, v55\n"
      "v_mfma_f32_16x16x32_bf16 v[76:79], a[220:223], a[156:159], v[76:79]\n"
      "v_exp_f32 v56, v56\n"
      "v_mfma_f32_16x16x32_bf16 v[84:87], a[220:223], a[172:175], v[84:87]\n"
      "v_cvt_pk_bf16_f32 v51, v54, v55\n"
      "v_exp_f32 v57, v57\n"
      "v_mfma_f32_16x16x32_bf16 v[92:95], a[220:223], a[188:191], v[92:95]\n"
      "v_exp_f32 v58, v58\n"
      "v_mfma_f32_16x16x32_bf16 v[96:99], a[236:239], a[140:143], v[96:99]\n"
      "v_cvt_pk_bf16_f32 v56, v56, v57\n"
      "v_exp_f32 v59, v59\n"
      "v_mfma_f32_16x16x32_bf16 v[104:107], a[236:239], a[156:159], v[104:107]\n"
      "s_add_u32 s48, s40, 2\n"
      "v_exp_f32 v60, v60\n"
      "v_mfma_f32_16x16x32_bf16 v[112:115], a[236:239], a[172:175], v[112:115]\n"
      "v_cvt_pk_bf16_f32 v57, v58, v59\n"
      "v_mfma_f32_16x16x32_bf16 v[120:123], a[236:239], a[188:191], v[120:123]\n"
      "v_readlane_b32 s46, v240, s48\n"
      "v_exp_f32 v61, v61\n"
      "v_mfma_f32_16x16x32_bf16 v[100:103], a[252:255], a[140:143], v[100:103]\n"
      "v_exp_f32 v62, v62\n"
      "v_mfma_f32_16x16x32_bf16 v[108:111], a[252:255], a[156:159], v[108:111]\n"
      "v_cvt_pk_bf16_f32 v58, v60, v61\n"
      "v_mfma_f32_16x16x32_bf16 v[116:119], a[252:255], a[172:175], v[116:119]\n"
      "v_add_u32 v227, s46, v241\n"
      "v_exp_f32 v63, v63\n"
      "v_mfma_f32_16x16x32_bf16 v[124:127], a[252:255], a[188:191], v[124:127]\n"
      "v_cvt_pk_bf16_f32 v59, v62, v63\n"
      "s_waitcnt vmcnt(0) lgkmcnt(0)\n"
      "s_barrier\n"
      "s_bitcmp1_b32 0x28, s40\n"
      "s_cbranch_scc0 L_nort_%=\n"
      "s_call_b64 s[56:57], L_rt2_%=\n"
      "L_nort_%=:\n"
      "v_mfma_f32_16x16x32_bf16 a[0:3], v[160:163], v[0:3], a[0:3]\n"
      "ds_read_b128 a[192:195], v224 offset:0\n"
      "v_mfma_f32_16x16x32_bf16 a[8:11], v[160:163], v[8:11], a[8:11]\n"
      "s_add_u32 m0, s49, 32768\n"
      "v_mfma_f32_16x16x32_bf16 a[16:19], v[160:163], v[16:19], a[16:19]\n"
      "ds_read_b128 a[208:211], v224 offset:4096\n"
      "global_load_lds_dwordx4 v227, s[72:73] offset:0 sc1\n"
      "v_mfma_f32_16x16x32_bf16 a[24:27], v[160:163], v[24:27], a[24:27]\n"
      "v_mfma_f32_16x16x32_bf16 a[4:7], v[164:167], v[0:3], a[4:7]\n"
      "v_mfma_f32_16x16x32_bf16 a[12:15], v[164:167], v[8:11], a[12:15]\n"
      "ds_read_b128 a[224:227], v224 offset:8192\n"
      "s_bitcmp1_b32 s43, s40\n"
      "s_cbranch_scc1 L_mask1_%=\n"
      "L_maskback1_%=:\n"
      "v_mfma_f32_16x16x32_bf16 a[20:23], v[164:167], v[16:19], a[20:23]\n"
      "v_max3_f32 v228, v64, v65, v66\n"
      "v_max3_f32 v229, v72, v73, v74\n"
      "v_mfma_f32_16x16x32_bf16 a[28:31], v[164:167], v[24:27], a[28:31]\n"
      "ds_read_b128 a[240:243], v224 offset:12288\n"
      "global_load_lds_dwordx4 v227, s[72:73] offset:1024 sc1\n"
      "v_mfma_f32_16x16x32_bf16 a[32:35], v[168:171], v[0:3], a[32:35]\n"
      "v_max3_f32 v230, v80, v81, v82\n"
      "v_max3_f32 v231, v88, v89, v90\n"
      "v_mfma_f32_16x16x32_bf16 a[40:43], v[168:171], v[8:11], a[40:43]\n"
      "v_max3_f32 v228, v228, v67, v68\n"
      "v_max3_f32 v229, v229, v75, v76\n"
      "v_mfma_f32_16x16x32_bf16 a[48:51], v[168:171], v[16:19], a[48:51]\n"
      "ds_read_b128 a[196:199], v224 offset:1024\n"
      "v_max3_f32 v230, v230, v83, v84\n"
      "v_mfma_f32_16x16x32_bf16 a[56:59], v[168:171], v[24:27], a[56:59]\n"
      "v_max3_f32 v231, v231, v91, v92\n"
      "v_max3_f32 v228, v228, v69, v70\n"
      "v_mfma_f32_16x16x32_bf16 a[36:39], v[172:175], v[0:3], a[36:39]\n"
      "ds_read_b128 a[212:215], v224 offset:5120\n"
      "v_max3_f32 v229, v229, v77, v78\n"
      "v_mfma_f32_16x16x32_bf16 a[44:47], v[172:175], v[8:11], a[44:47]\n"
      "global_load_lds_dwordx4 v227, s[72:73] offset:2048 sc1\n"
      "v_mfma_f32_16x16x32_bf16 a[52:55], v[172:175], v[16:19], a[52:55]\n"
      "v_max3_f32 v230, v230, v85, v86\n"
      "v_max3_f32 v231, v231, v93, v94\n"
      "v_mfma_f32_16x16x32_bf16 a[60:63], v[172:175], v[24:27], a[60:63]\n"
      "ds_read_b128 a[228:231], v224 offset:9216\n"
      "v_max3_f32 v228, v228, v71, v96\n"
      "v_mfma_f32_16x16x32_bf16 a[64:67], v[176:179], v[0:3], a[64:67]\n"
      "v_max3_f32 v229, v229, v79, v104\n"
      "v_max3_f32 v230, v230, v87, v112\n"
      "v_mfma_f32_16x16x32_bf16 a[72:75], v[176:179], v[8:11], a[72:75]\n"
      "ds_read_b128 a[244:247], v224 offset:13312\n"
      "v_max3_f32 v231, v231, v95, v120\n"
      "v_mfma_f32_16x16x32_bf16 a[80:83], v[176:179], v[16:19], a[80:83]\n"
      "v_max3_f32 v228, v228, v97, v98\n"
      "v_max3_f32 v229, v229, v105, v106\n"
      "v_mfma_f32_16x16x32_bf16 a[88:91], v[176:179], v[24:27], a[88:91]\n"
      "global_load_lds_dwordx4 v227, s[72:73] offset:3072 sc1\n"
      "v_mfma_f32_16x16x32_bf16 a[68:71], v[180:183], v[0:3], a[68:71]\n"
      "ds_read_b128 a[200:203], v224 offset:2048\n"
      "v_max3_f32 v230, v230, v113, v114\n"
      "v_mfma_f32_16x16x32_bf16 a[76:79], v[180:183], v[8:11], a[76:79]\n"
      "v_max3_f32 v231, v231, v121, v122\n"
      "v_max3_f32 v228, v228, v99, v100\n"
      "v_mfma_f32_16x16x32_bf16 a[84:87], v[180:183], v[16:19], a[84:87]\n"
      "ds_read_b128 a[216:219], v224 offset:6144\n"
      "v_max3_f32 v229, v229, v107, v108\n"
      "v_mfma_f32_16x16x32_bf16 a[92:95], v[180:183], v[24:27], a[92:95]\n"
      "s_add_u32 m0, s49, 36864\n"
      "v_max3_f32 v230, v230, v115, v116\n"
      "v_mfma_f32_16x16x32_bf16 a[96:99], v[184:187], v[0:3], a[96:99]\n"
      "global_load_lds_dwordx4 v227, s[90:91] offset:0 sc1\n"
      "v_mfma_f32_16x16x32_bf16 a[104:107], v[184:187], v[8:11], a[104:107]\n"
      "ds_read_b128 a[232:235], v224 offset:10240\n"
      "v_max3_f32 v231, v231, v123, v124\n"
      "v_mfma_f32_16x16x32_bf16 a[112:115], v[184:187], v[16:19], a[112:115]\n"
      "v_max3_f32 v228, v228, v101, v102\n"
      "v_max3_f32 v229, v229, v109, v110\n"
      "v_mfma_f32_16x16x32_bf16 a[120:123], v[184:187], v[24:27], a[120:123]\n"
      "ds_read_b128 a[248:251], v224 offset:14336\n"
      "v_max3_f32 v230, v230, v117, v118\n"
      "v_mfma_f32_16x16x32_bf16 a[100:103], v[188:191], v[0:3], a[100:103]\n"
      "v_max3_f32 v231, v231, v125, v126\n"
      "v_max_f32 v228, v228, v103\n"
      "v_mfma_f32_16x16x32_bf16 a[108:111], v[188:191], v[8:11], a[108:111]\n"
      "v_max_f32 v229, v229, v111\n"
      "v_max_f32 v230, v230, v119\n"
      "v_mfma_f32_16x16x32_bf16 a[116:119], v[188:191], v[16:19], a[116:119]\n"
      "ds_read_b128 a[204:207], v224 offset:3072\n"
      "global_load_lds_dwordx4 v227, s[90:91] offset:1024 sc1\n"
      "v_mfma_f32_16x16x32_bf16 a[124:127], v[188:191], v[24:27], a[124:127]\n"
      "v_max_f32 v231, v231, v127\n"
      "v_max3_f32 v232, v228, v229, v230\n"
      "v_mfma_f32_16x16x32_bf16 v[144:147], v[244:247], v[0:3], v[144:147]\n"
      "ds_read_b128 a[220:223], v224 offset:7168\n"
      "v_max_f32 v232, v232, v231\n"
      "v_mfma_f32_16x16x32_bf16 v[148:151], v[244:247], v[8:11], v[148:151]\n"
      "v_cmp_nle_f32_e64 vcc, v232, s41\n"
      "s_cbranch_vccnz L_rare1_%=\n"
      "L_rareback1_%=:\n"
      "v_mfma_f32_16x16x32_bf16 v[152:155], v[244:247], v[16:19], v[152:155]\n"
      "v_exp_f32 v64, v64\n"
      "v_mfma_f32_16x16x32_bf16 v[156:159], v[244:247], v[24:27], v[156:159]\n"
      "ds_read_b128 a[236:239], v224 offset:11264\n"
      "v_mfma_f32_16x16x32_bf16 a[0:3], v[192:195], v[32:35], a[0:3]\n"
      "global_load_lds_dwordx4 v227, s[90:91] offset:2048 sc1\n"
      "v_mfma_f32_16x16x32_bf16 a[8:11], v[192:195], v[40:43], a[8:11]\n"
      "ds_read_b128 a[252:255], v224 offset:15360\n"
      "v_mfma_f32_16x16x32_bf16 a[16:19], v[192:195], v[48:51], a[16:19]\n"
      "v_exp_f32 v65, v65\n"
      "v_mfma_f32_16x16x32_bf16 a[24:27], v[192:195], v[56:59], a[24:27]\n"
      "v_exp_f32 v66, v66\n"
      "v_mfma_f32_16x16x32_bf16 a[4:7], v[196:199], v[32:35], a[4:7]\n"
      "v_cvt_pk_bf16_f32 v64, v64, v65\n"
      "v_mfma_f32_16x16x32_bf16 a[12:15], v[196:199], v[40:43], a[12:15]\n"
      "global_load_lds_dwordx4 v227, s[90:91] offset:3072 sc1\n"
      "v_mfma_f32_16x16x32_bf16 a[20:23], v[196:199], v[48:51], a[20:23]\n"
      "v_exp_f32 v67, v67\n"
      "v_mfma_f32_16x16x32_bf16 a[28:31], v[196:199], v[56:59], a[28:31]\n"
      "v_exp_f32 v68, v68\n"
      "v_mfma_f32_16x16x32_bf16 a[32:35], v[200:203], v[32:35], a[32:35]\n"
      "v_cvt_pk_bf16_f32 v65, v66, v67\n"
      "v_mfma_f32_16x16x32_bf16 a[40:43], v[200:203], v[40:43], a[40:43]\n"
      "v_exp_f32 v69, v69\n"
      "v_mfma_f32_16x16x32_bf16 a[48:51], v[200:203], v[48:51], a[48:51]\n"
      "s_add_u32 m0, s49, 65536\n"
      "v_mfma_f32_16x16x32_bf16 a[56:59], v[200:203], v[56:59], a[56:59]\n"
      "global_load_lds_dwordx4 v226, s[74:75] offset:0 sc1\n"
      "v_mfma_f32_16x16x32_bf16 a[36:39], v[204:207], v[32:35], a[36:39]\n"
      "v_exp_f32 v70, v70\n"
      "v_mfma_f32_16x16x32_bf16 a[44:47], v[204:207], v[40:43], a[44:47]\n"
      "v_cvt_pk_bf16_f32 v66, v68, v69\n"
      "v_mfma_f32_16x16x32_bf16 a[52:55], v[204:207], v[48:51], a[52:55]\n"
      "v_exp_f32 v71, v71\n"
      "v_mfma_f32_16x16x32_bf16 a[60:63], v[204:207], v[56:59], a[60:63]\n"
      "v_exp_f32 v72, v72\n"
      "v_mfma_f32_16x16x32_bf16 a[64:67], v[208:211], v[32:35], a[64:67]\n"
      "v_cvt_pk_bf16_f32 v67, v70, v71\n"
      "v_mfma_f32_16x16x32_bf16 a[72:75], v[208:211], v[40:43], a[72:75]\n"
      "global_load_lds_dwordx4 v226, s[74:75] offset:1024 sc1\n"
      "v_mfma_f32_16x16x32_bf16 a[80:83], v[208:211], v[48:51], a[80:83]\n"
      "v_exp_f32 v73, v73\n"
      "v_mfma_f32_16x16x32_bf16 a[88:91], v[208:211], v[56:59], a[88:91]\n"
      "v_exp_f32 v74, v74\n"
      "v_mfma_f32_16x16x32_bf16 a[68:71], v[212:215], v[32:35], a[68:71]\n"
      "v_cvt_pk_bf16_f32 v72, v72, v73\n"
      "v_mfma_f32_16x16x32_bf16 a[76:79], v[212:215], v[40:43], a[76:79]\n"
      "v_exp_f32 v75, v75\n"
      "v_mfma_f32_16x16x32_bf16 a[84:87], v[212:215], v[48:51], a[84:87]\n"
      "global_load_lds_dwordx4 v226, s[74:75] offset:2048 sc1\n"
      "v_mfma_f32_16x16x32_bf16 a[92:95], v[212:215], v[56:59], a[92:95]\n"
      "v_exp_f32 v76, v76\n"
      "v_mfma_f32_16x16x32_bf16 a[96:99], v[216:219], v[32:35], a[96:99]\n"
      "v_cvt_pk_bf16_f32 v73, v74, v75\n"
      "v_mfma_f32_16x16x32_bf16 a[104:107], v[216:219], v[40:43], a[104:107]\n"
      "v_exp_f32 v77, v77\n"
      "v_mfma_f32_16x16x32_bf16 a[112:115], v[216:219], v[48:51], a[112:115]\n"
      "v_exp_f32 v78, v78\n"
      "v_mfma_f32_16x16x32_bf16 a[120:123], v[216:219], v[56:59], a[120:123]\n"
      "v_cvt_pk_bf16_f32 v74, v76, v77\n"
      "v_mfma_f32_16x16x32_bf16 a[100:103], v[220:223], v[32:35], a[100:103]\n"
      "global_load_lds_dwordx4 v226, s[74:75] offset:3072 sc1\n"
      "v_mfma_f32_16x16x32_bf16 a[108:111], v[220:223], v[40:43], a[108:111]\n"
      "v_exp_f32 v79, v79\n"
      "v_mfma_f32_16x16x32_bf16 a[116:119], v[220:223], v[48:51], a[116:119]\n"
      "v_exp_f32 v80, v80\n"
      "v_mfma_f32_16x16x32_bf16 a[124:127], v[220:223], v[56:59], a[124:127]\n"
      "v_cvt_pk_bf16_f32 v75, v78, v79\n"
      "v_mfma_f32_16x16x32_bf16 v[144:147], v[244:247], v[32:35], v[144:147]\n"
      "v_exp_f32 v81, v81\n"
      "v_mfma_f32_16x16x32_bf16 v[148:151], v[244:247], v[40:43], v[148:151]\n"
      "v_exp_f32 v82, v82\n"
      "v_mfma_f32_16x16x32_bf16 v[152:155], v[244:247], v[48:51], v[152:155]\n"
      "v_cvt_pk_bf16_f32 v80, v80, v81\n"
      "v_exp_f32 v83, v83\n"
      "v_mfma_f32_16x16x32_bf16 v[156:159], v[244:247], v[56:59], v[156:159]\n"
      "v_exp_f32 v84, v84\n"
      "s_bitcmp1_b32 s42, s40\n"
      "s_cbranch_scc0 L_nohook1_%=\n"
      "s_call_b64 s[56:57], L_hook_%=\n"
      "L_nohook1_%=:\n"
      "s_waitcnt lgkmcnt(0)\n"
      "s_add_u32 s40, s40, 1\n"
      "s_branch L_loop_%=\n"
      "L_drain_%=:\n"
      "s_barrier\n"
      "ds_read_b128 v[160:163], v225 offset:0\n"
      "ds_read_b128 v[164:167], v225 offset:2048\n"
      "ds_read_b128 v[168:171], v225 offset:4096\n"
      "ds_read_b128 v[172:175], v225 offset:6144\n"
      "ds_read_b128 v[176:179], v225 offset:8192\n"
      "ds_read_b128 v[180:183], v225 offset:10240\n"
      "ds_read_b128 v[184:187], v225 offset:12288\n"
      "ds_read_b128 v[188:191], v225 offset:14336\n"
      "ds_read_b128 v[192:195], v225 offset:1024\n"
      "ds_read_b128 v[196:199], v225 offset:3072\n"
      "ds_read_b128 v[200:203], v225 offset:5120\n"
      "ds_read_b128 v[204:207], v225 offset:7168\n"
      "ds_read_b128 v[208:211], v225 offset:9216\n"
      "ds_read_b128 v[212:215], v225 offset:11264\n"
      "ds_read_b128 v[216:219], v225 offset:13312\n"
      "ds_read_b128 v[220:223], v225 offset:15360\n"
      "v_cvt_pk_bf16_f32 v17, v18, v19\n"
      "v_exp_f32 v21, v21\n"
      "v_exp_f32 v22, v22\n"
      "v_cvt_pk_bf16_f32 v18, v20, v21\n"
      "v_exp_f32 v23, v23\n"
      "v_exp_f32 v24, v24\n"
      "v_cvt_pk_bf16_f32 v19, v22, v23\n"
      "v_exp_f32 v25, v25\n"
      "v_exp_f32 v26, v26\n"
      "v_cvt_pk_bf16_f32 v24, v24, v25\n"
      "v_exp_f32 v27, v27\n"
      "v_exp_f32 v28, v28\n"
      "v_cvt_pk_bf16_f32 v25, v26, v27\n"
      "v_exp_f32 v29, v29\n"
      "v_exp_f32 v30, v30\n"
      "v_cvt_pk_bf16_f32 v26, v28, v29\n"
      "v_exp_f32 v31, v31\n"
      "v_exp_f32 v32, v32\n"
      "v_cvt_pk_bf16_f32 v27, v30, v31\n"
      "v_exp_f32 v33, v33\n"
      "s_waitcnt lgkmcnt(8)\n"
      "s_nop 1\n"
      "v_mfma_f32_16x16x32_bf16 a[0:3], v[160:163], v[0:3], a[0:3]\n"
      "v_exp_f32 v34, v34\n"
      "v_mfma_f32_16x16x32_bf16 a[8:11], v[160:163], v[8:11], a[8:11]\n"
      "v_cvt_pk_bf16_f32 v32, v32, v33\n"
      "v_mfma_f32_16x16x32_bf16 a[16:19], v[160:163], v[16:19], a[16:19]\n"
      "v_exp_f32 v35, v35\n"
      "v_mfma_f32_16x16x32_bf16 a[24:27], v[160:163], v[24:27], a[24:27]\n"
      "v_exp_f32 v36, v36\n"
      "v_mfma_f32_16x16x32_bf16 a[4:7], v[164:167], v[0:3], a[4:7]\n"
      "v_cvt_pk_bf16_f32 v33, v34, v35\n"
      "v_exp_f32 v37, v37\n"
      "v_mfma_f32_16x16x32_bf16 a[12:15], v[164:167], v[8:11], a[12:15]\n"
      "v_exp_f32 v38, v38\n"
      "v_mfma_f32_16x16x32_bf16 a[20:23], v[164:167], v[16:19], a[20:23]\n"
      "v_cvt_pk_bf16_f32 v34, v36, v37\n"
      "v_exp_f32 v39, v39\n"
      "v_mfma_f32_16x16x32_bf16 a[28:31], v[164:167], v[24:27], a[28:31]\n"
      "v_exp_f32 v40, v40\n"
      "v_mfma_f32_16x16x32_bf16 a[32:35], v[168:171], v[0:3], a[32:35]\n"
      "v_cvt_pk_bf16_f32 v35, v38, v39\n"
      "v_mfma_f32_16x16x32_bf16 a[40:43], v[168:171], v[8:11], a[40:43]\n"
      "v_exp_f32 v41, v41\n"
      "v_mfma_f32_16x16x32_bf16 a[48:51], v[168:171], v[16:19], a[48:51]\n"
      "v_exp_f32 v42, v42\n"
      "v_mfma_f32_16x16x32_bf16 a[56:59], v[168:171], v[24:27], a[56:59]\n"
      "v_cvt_pk_bf16_f32 v40, v40, v41\n"
      "v_exp_f32 v43, v43\n"
      "v_mfma_f32_16x16x32_bf16 a[36:39], v[172:175], v[0:3], a[36:39]\n"
      "v_exp_f32 v44, v44\n"
      "v_mfma_f32_16x16x32_bf16 a[44:47], v[172:175], v[8:11], a[44:47]\n"
      "v_cvt_pk_bf16_f32 v41, v42, v43\n"
      "v_exp_f32 v45, v45\n"
      "v_mfma_f32_16x16x32_bf16 a[52:55], v[172:175], v[16:19], a[52:55]\n"
      "v_exp_f32 v46, v46\n"
      "v_mfma_f32_16x16x32_bf16 a[60:63], v[172:175], v[24:27], a[60:63]\n"
      "v_cvt_pk_bf16_f32 v42, v44, v45\n"
      "v_mfma_f32_16x16x32_bf16 a[64:67], v[176:179], v[0:3], a[64:67]\n"
      "v_exp_f32 v47, v47\n"
      "v_mfma_f32_16x16x32_bf16 a[72:75], v[176:179], v[8:11], a[72:75]\n"
      "v_exp_f32 v48, v48\n"
      "v_mfma_f32_16x16x32_bf16 a[80:83], v[176:179], v[16:19], a[80:83]\n"
      "v_cvt_pk_bf16_f32 v43, v46, v47\n"
      "v_exp_f32 v49, v49\n"
      "v_mfma_f32_16x16x32_bf16 a[88:91], v[176:179], v[24:27], a[88:91]\n"
      "v_exp_f32 v50, v50\n"
      "v_mfma_f32_16x16x32_bf16 a[68:71], v[180:183], v[0:3], a[68:71]\n"
      "v_cvt_pk_bf16_f32 v48, v48, v49\n"
      "v_exp_f32 v51, v51\n"
      "v_mfma_f32_16x16x32_bf16 a[76:79], v[180:183], v[8:11], a[76:79]\n"
      "v_exp_f32 v52, v52\n"
      "v_mfma_f32_16x16x32_bf16 a[84:87], v[180:183], v[16:19], a[84:87]\n"
      "v_cvt_pk_bf16_f32 v49, v50, v51\n"
      "v_mfma_f32_16x16x32_bf16 a[92:95], v[180:183], v[24:27], a[92:95]\n"
      "v_exp_f32 v53, v53\n"
      "v_mfma_f32_16x16x32_bf16 a[96:99], v[184:187], v[0:3], a[96:99]\n"
      "v_exp_f32 v54, v54\n"
      "v_mfma_f32_16x16x32_bf16 a[104:107], v[184:187], v[8:11], a[104:107]\n"
      "v_cvt_pk_bf16_f32 v50, v52, v53\n"
      "v_exp_f32 v55, v55\n"
      "v_mfma_f32_16x16x32_bf16 a[112:115], v[184:187], v[16:19], a[112:115]\n"
      "v_exp_f32 v56, v56\n"
      "v_mfma_f32_16x16x32_bf16 a[120:123], v[184:187], v[24:27], a[120:123]\n"
      "v_cvt_pk_bf16_f32 v51, v54, v55\n"
      "v_exp_f32 v57, v57\n"
      "v_mfma_f32_16x16x32_bf16 a[100:103], v[188:191], v[0:3], a[100:103]\n"
      "v_exp_f32 v58, v58\n"
      "v_mfma_f32_16x16x32_bf16 a[108:111], v[188:191], v[8:11], a[108:111]\n"
      "v_cvt_pk_bf16_f32 v56, v56, v57\n"
      "v_mfma_f32_16x16x32_bf16 a[116:119], v[188:191], v[16:19], a[116:119]\n"
      "v_exp_f32 v59, v59\n"
      "v_mfma_f32_16x16x32_bf16 a[124:127], v[188:191], v[24:27], a[124:127]\n"
      "v_exp_f32 v60, v60\n"
      "v_mfma_f32_16x16x32_bf16 v[144:147], v[244:247], v[0:3], v[144:147]\n"
      "v_cvt_pk_bf16_f32 v57, v58, v59\n"
      "v_exp_f32 v61, v61\n"
      "v_mfma_f32_16x16x32_bf16 v[148:151], v[244:247], v[8:11], v[148:151]\n"
      "v_exp_f32 v62, v62\n"
      "v_mfma_f32_16x16x32_bf16 v[152:155], v[244:247], v[16:19], v[152:155]\n"
      "v_cvt_pk_bf16_f32 v58, v60, v61\n"
      "v_exp_f32 v63, v63\n"
      "v_mfma_f32_16x16x32_bf16 v[156:159], v[244:247], v[24:27], v[156:159]\n"
      "v_cvt_pk_bf16_f32 v59, v62, v63\n"
      "s_waitcnt lgkmcnt(0)\n"
      "s_nop 1\n"
      "v_mfma_f32_16x16x32_bf16 a[0:3], v[192:195], v[32:35], a[0:3]\n"
      "v_mfma_f32_16x16x32_bf16 a[8:11], v[192:195], v[40:43], a[8:11]\n"
      "v_mfma_f32_16x16x32_bf16 a[16:19], v[192:195], v[48:51], a[16:19]\n"
      "v_mfma_f32_16x16x32_bf16 a[24:27], v[192:195], v[56:59], a[24:27]\n"
      "v_mfma_f32_16x16x32_bf16 a[4:7], v[196:199], v[32:35], a[4:7]\n"
      "v_mfma_f32_16x16x32_bf16 a[12:15], v[196:199], v[40:43], a[12:15]\n"
      "v_mfma_f32_16x16x32_bf16 a[20:23], v[196:199], v[48:51], a[20:23]\n"
      "v_mfma_f32_16x16x32_bf16 a[28:31], v[196:199], v[56:59], a[28:31]\n"
      "v_mfma_f32_16x16x32_bf16 a[32:35], v[200:203], v[32:35], a[32:35]\n"
      "v_mfma_f32_16x16x32_bf16 a[40:43], v[200:203], v[40:43], a[40:43]\n"
      "v_mfma_f32_16x16x32_bf16 a[48:51], v[200:203], v[48:51], a[48:51]\n"
      "v_mfma_f32_16x16x32_bf16 a[56:59], v[200:203], v[56:59], a[56:59]\n"
      "v_mfma_f32_16x16x32_bf16 a[36:39], v[204:207], v[32:35], a[36:39]\n"
      "v_mfma_f32_16x16x32_bf16 a[44:47], v[204:207], v[40:43], a[44:47]\n"
      "v_mfma_f32_16x16x32_bf16 a[52:55], v[204:207], v[48:51], a[52:55]\n"
      "v_mfma_f32_16x16x32_bf16 a[60:63], v[204:207], v[56:59], a[60:63]\n"
      "v_mfma_f32_16x16x32_bf16 a[64:67], v[208:211], v[32:35], a[64:67]\n"
      "v_mfma_f32_16x16x32_bf16 a[72:75], v[208:211], v[40:43], a[72:75]\n"
      "v_mfma_f32_16x16x32_bf16 a[80:83], v[208:211], v[48:51], a[80:83]\n"
      "v_mfma_f32_16x16x32_bf16 a[88:91], v[208:211], v[56:59], a[88:91]\n"
      "v_mfma_f32_16x16x32_bf16 a[68:71], v[212:215], v[32:35], a[68:71]\n"
      "v_mfma_f32_16x16x32_bf16 a[76:79], v[212:215], v[40:43], a[76:79]\n"
      "v_mfma_f32_16x16x32_bf16 a[84:87], v[212:215], v[48:51], a[84:87]\n"
      "v_mfma_f32_16x16x32_bf16 a[92:95], v[212:215], v[56:59], a[92:95]\n"
      "v_mfma_f32_16x16x32_bf16 a[96:99], v[216:219], v[32:35], a[96:99]\n"
      "v_mfma_f32_16x16x32_bf16 a[104:107], v[216:219], v[40:43], a[104:107]\n"
      "v_mfma_f32_16x16x32_bf16 a[112:115], v[216:219], v[48:51], a[112:115]\n"
      "v_mfma_f32_16x16x32_bf16 a[120:123], v[216:219], v[56:59], a[120:123]\n"
      "v_mfma_f32_16x16x32_bf16 a[100:103], v[220:223], v[32:35], a[100:103]\n"
      "v_mfma_f32_16x16x32_bf16 a[108:111], v[220:223], v[40:43], a[108:111]\n"
      "v_mfma_f32_16x16x32_bf16 a[116:119], v[220:223], v[48:51], a[116:119]\n"
      "v_mfma_f32_16x16x32_bf16 a[124:127], v[220:223], v[56:59], a[124:127]\n"
      "v_mfma_f32_16x16x32_bf16 v[144:147], v[244:247], v[32:35], v[144:147]\n"
      "v_mfma_f32_16x16x32_bf16 v[148:151], v[244:247], v[40:43], v[148:151]\n"
      "v_mfma_f32_16x16x32_bf16 v[152:155], v[244:247], v[48:51], v[152:155]\n"
      "v_mfma_f32_16x16x32_bf16 v[156:159], v[244:247], v[56:59], v[156:159]\n"
      "v_sub_f32 v236, 0, v128\n"
      "v_sub_f32 v237, 0, v132\n"
      "v_sub_f32 v238, 0, v136\n"
      "v_sub_f32 v239, 0, v140\n"
      "s_mov_b64 s[58:59], %10\n"
      "s_nop 7\n"
      "s_call_b64 s[56:57], L_epi_%=\n"
      "s_branch L_end_%=\n"
      "L_tok_%=:\n"
      "v_readfirstlane_b32 s78, v254\n"
      "L_tok2_%=:\n"
      "s_add_u32 s78, s78, 1\n"
      "v_cmp_eq_u32 vcc, s78, v254\n"
      "s_cbranch_vccnz L_tok2_%=\n"
      "s_lshl_b32 s53, %20, 2\n"
      "v_mov_b32 v254, s53\n"
      "v_mov_b32 v255, s78\n"
      "s_mov_b64 exec, 1\n"
      "global_store_dword v254, v255, %15 sc1\n"
      "s_mov_b64 exec, -1\n"
      "s_branch L_tokback_%=\n"
      "L_lnw_%=:\n"
      "s_mov_b32 exec_lo, 0\n"
      "s_mov_b32 exec_hi, -1\n"
      "v_cmp_ne_u32 vcc, s78, v254\n"
      "s_mov_b64 exec, -1\n"
      "s_cbranch_vccz L_lnok_%=\n"
      "s_mov_b32 s47, 0\n"
      "L_lnw2_%=:\n"
      "global_load_dword v255, v245, %15 sc1\n"
      "s_lshl_b32 s53, %20, 8\n"
      "s_add_u32 s53, s53, 4096\n"
      "v_lshl_add_u32 v254, v248, 2, s53\n"
      "global_load_dword v254, v254, %15 sc1\n"
      "s_waitcnt vmcnt(0)\n"
      "s_mov_b32 exec_lo, 0\n"
      "s_mov_b32 exec_hi, -1\n"
      "global_store_dword v249, v255, %15 sc1\n"
      "v_cmp_ne_u32 vcc, s78, v254\n"
      "s_mov_b64 exec, -1\n"
      "s_cbranch_vccz L_lnok_%=\n"
      "s_add_u32 s47, s47, 1\n"
      "s_cmp_gt_u32 s47, 0x20000\n"
      "s_cbranch_scc1 L_lnok_%=\n"
      "s_sleep 1\n"
      "s_branch L_lnw2_%=\n"
      "L_poll_%=:\n"
      "s_mov_b32 exec_lo, 0xff\n"
      "s_mov_b32 exec_hi, 0\n"
      "v_cmp_ne_u32 vcc, s78, v249\n"
      "s_mov_b64 exec, -1\n"
      "s_cbranch_vccz L_pollok_%=\n"
      "v_bfe_u32 v248, v241, 4, 6\n"
      "s_and_b32 s53, %20, 31\n"
      "s_lshl_b32 s53, s53, 2\n"
      "v_bfe_u32 v250, v241, 4, 3\n"
      "v_lshl_add_u32 v250, v250, 7, s53\n"
      "s_lshl_b32 s53, %14, 2\n"
      "s_add_u32 s53, s53, 4096\n"
      "v_lshl_add_u32 v251, v250, 6, s53\n"
      "s_mov_b32 s47, 0\n"
      "L_poll2_%=:\n"
      "global_load_dword v252, v250, %15 sc1\n"
      "s_lshl_b32 s53, %20, 8\n"
      "s_add_u32 s53, s53, 4096\n"
      "v_lshl_add_u32 v249, v248, 2, s53\n"
      "global_load_dword v249, v249, %15 sc1\n"
      "s_waitcnt vmcnt(0)\n"
      "s_mov_b32 exec_lo, 0xff\n"
      "s_mov_b32 exec_hi, 0\n"
      "global_store_dword v251, v252, %15 sc1\n"
      "v_cmp_ne_u32 vcc, s78, v249\n"
      "s_mov_b64 exec, -1\n"
      "s_cbranch_vccz L_pollok_%=\n"
      "s_add_u32 s47, s47, 1\n"
      "s_cmp_gt_u32 s47, 0x20000\n"
      "s_cbranch_scc1 L_pollok_%=\n"
      "s_sleep 1\n"
      "s_branch L_poll2_%=\n"
      "L_mask0_%=:\n"
      "s_nop 7\n"
      "s_cmp_eq_u32 s51, 0\n"
      "s_cbranch_scc1 L_maskback0_%=\n"
      "v_mov_b32 v248, 0xff800000\n"
      "s_cmp_eq_u32 s51, 2\n"
      "s_cbranch_scc1 L_maskall0_%=\n"
      "v_bfe_u32 v249, v241, 4, 4\n"
      "v_bfe_u32 v250, v241, 8, 2\n"
      "v_lshlrev_b32 v250, 2, v250\n"
      "v_sub_u32 v249, v249, v250\n"
      "v_cmp_gt_i32_e64 s[60:61], 0, v249\n"
      "v_cmp_gt_i32_e64 s[62:63], 1, v249\n"
      "v_cmp_gt_i32_e64 s[64:65], 2, v249\n"
      "v_cmp_gt_i32_e64 s[66:67], 3, v249\n"
      "v_mov_b32 v4, v248\n"
      "v_mov_b32 v5, v248\n"
      "v_mov_b32 v6, v248\n"
      "v_mov_b32 v7, v248\n"
      "v_mov_b32 v32, v248\n"
      "v_mov_b32 v33, v248\n"
      "v_mov_b32 v34, v248\n"
      "v_mov_b32 v35, v248\n"
      "v_mov_b32 v40, v248\n"
      "v_mov_b32 v41, v248\n"
      "v_mov_b32 v42, v248\n"
      "v_mov_b32 v43, v248\n"
      "v_mov_b32 v36, v248\n"
      "v_mov_b32 v37, v248\n"
      "v_mov_b32 v38, v248\n"
      "v_mov_b32 v39, v248\n"
      "v_mov_b32 v44, v248\n"
      "v_mov_b32 v45, v248\n"
      "v_mov_b32 v46, v248\n"
      "v_mov_b32 v47, v248\n"
      "v_mov_b32 v52, v248\n"
      "v_mov_b32 v53, v248\n"
      "v_mov_b32 v54, v248\n"
      "v_mov_b32 v55, v248\n"
      "v_cndmask_b32_e64 v0, v0, v248, s[60:61]\n"
      "v_cndmask_b32_e64 v1, v1, v248, s[62:63]\n"
      "v_cndmask_b32_e64 v2, v2, v248, s[64:65]\n"
      "v_cndmask_b32_e64 v3, v3, v248, s[66:67]\n"
      "v_cndmask_b32_e64 v12, v12, v248, s[60:61]\n"
      "v_cndmask_b32_e64 v13, v13, v248, s[62:63]\n"
      "v_cndmask_b32_e64 v14, v14, v248, s[64:65]\n"
      "v_cndmask_b32_e64 v15, v15, v248, s[66:67]\n"
      "v_cndmask_b32_e64 v48, v48, v248, s[60:61]\n"
      "v_cndmask_b32_e64 v49, v49, v248, s[62:63]\n"
      "v_cndmask_b32_e64 v50, v50, v248, s[64:65]\n"
      "v_cndmask_b32_e64 v51, v51, v248, s[66:67]\n"
      "v_cndmask_b32_e64 v60, v60, v248, s[60:61]\n"
      "v_cndmask_b32_e64 v61, v61, v248, s[62:63]\n"
      "v_cndmask_b32_e64 v62, v62, v248, s[64:65]\n"
      "v_cndmask_b32_e64 v63, v63, v248, s[66:67]\n"
      "s_branch L_maskback0_%=\n"
      "L_maskall0_%=:\n"
      "v_mov_b32 v0, v248\n"
      "v_mov_b32 v1, v248\n"
      "v_mov_b32 v2, v248\n"
      "v_mov_b32 v3, v248\n"
      "v_mov_b32 v4, v248\n"
      "v_mov_b32 v5, v248\n"
      "v_mov_b32 v6, v248\n"
      "v_mov_b32 v7, v248\n"
      "v_mov_b32 v8, v248\n"
      "v_mov_b32 v9, v248\n"
      "v_mov_b32 v10, v248\n"
      "v_mov_b32 v11, v248\n"
      "v_mov_b32 v12, v248\n"
      "v_mov_b32 v13, v248\n"
      "v_mov_b32 v14, v248\n"
      "v_mov_b32 v15, v248\n"
      "v_mov_b32 v16, v248\n"
      "v_mov_b32 v17, v248\n"
      "v_mov_b32 v18, v248\n"
      "v_mov_b32 v19, v248\n"
      "v_mov_b32 v20, v248\n"
      "v_mov_b32 v21, v248\n"
      "v_mov_b32 v22, v248\n"
      "v_mov_b32 v23, v248\n"
      "v_mov_b32 v24, v248\n"
      "v_mov_b32 v25, v248\n"
      "v_mov_b32 v26, v248\n"
      "v_mov_b32 v27, v248\n"
      "v_mov_b32 v28, v248\n"
      "v_mov_b32 v29, v248\n"
      "v_mov_b32 v30, v248\n"
      "v_mov_b32 v31, v248\n"
      "v_mov_b32 v32, v248\n"
      "v_mov_b32 v33, v248\n"
      "v_mov_b32 v34, v248\n"
      "v_mov_b32 v35, v248\n"
      "v_mov_b32 v36, v248\n"
      "v_mov_b32 v37, v248\n"
      "v_mov_b32 v38, v248\n"
      "v_mov_b32 v39, v248\n"
      "v_mov_b32 v40, v248\n"
      "v_mov_b32 v41, v248\n"
      "v_mov_b32 v42, v248\n"
      "v_mov_b32 v43, v248\n"
      "v_mov_b32 v44, v248\n"
      "v_mov_b32 v45, v248\n"
      "v_mov_b32 v46, v248\n"
      "v_mov_b32 v47, v248\n"
      "v_mov_b32 v48, v248\n"
      "v_mov_b32 v49, v248\n"
      "v_mov_b32 v50, v248\n"
      "v_mov_b32 v51, v248\n"
      "v_mov_b32 v52, v248\n"
      "v_mov_b32 v53, v248\n"
      "v_mov_b32 v54, v248\n"
      "v_mov_b32 v55, v248\n"
      "v_mov_b32 v56, v248\n"
      "v_mov_b32 v57, v248\n"
      "v_mov_b32 v58, v248\n"
      "v_mov_b32 v59, v248\n"
      "v_mov_b32 v60, v248\n"
      "v_mov_b32 v61, v248\n"
      "v_mov_b32 v62, v248\n"
      "v_mov_b32 v63, v248\n"
      "s_branch L_maskback0_%=\n"
      "L_mask1_%=:\n"
      "s_nop 7\n"
      "s_cmp_eq_u32 s51, 0\n"
      "s_cbranch_scc1 L_maskback1_%=\n"
      "v_mov_b32 v248, 0xff800000\n"
      "s_cmp_eq_u32 s51, 2\n"
      "s_cbranch_scc1 L_maskall1_%=\n"
      "v_bfe_u32 v249, v241, 4, 4\n"
      "v_bfe_u32 v250, v241, 8, 2\n"
      "v_lshlrev_b32 v250, 2, v250\n"
      "v_sub_u32 v249, v249, v250\n"
      "v_cmp_gt_i32_e64 s[60:61], 0, v249\n"
      "v_cmp_gt_i32_e64 s[62:63], 1, v249\n"
      "v_cmp_gt_i32_e64 s[64:65], 2, v249\n"
      "v_cmp_gt_i32_e64 s[66:67], 3, v249\n"
      "v_mov_b32 v68, v248\n"
      "v_mov_b32 v69, v248\n"
      "v_mov_b32 v70, v248\n"
      "v_mov_b32 v71, v248\n"
      "v_mov_b32 v96, v248\n"
      "v_mov_b32 v97, v248\n"
      "v_mov_b32 v98, v248\n"
      "v_mov_b32 v99, v248\n"
      "v_mov_b32 v104, v248\n"
      "v_mov_b32 v105, v248\n"
      "v_mov_b32 v106, v248\n"
      "v_mov_b32 v107, v248\n"
      "v_mov_b32 v100, v248\n"
      "v_mov_b32 v101, v248\n"
      "v_mov_b32 v102, v248\n"
      "v_mov_b32 v103, v248\n"
      "v_mov_b32 v108, v248\n"
      "v_mov_b32 v109, v248\n"
      "v_mov_b32 v110, v248\n"
      "v_mov_b32 v111, v248\n"
      "v_mov_b32 v116, v248\n"
      "v_mov_b32 v117, v248\n"
      "v_mov_b32 v118, v248\n"
      "v_mov_b32 v119, v248\n"
      "v_cndmask_b32_e64 v64, v64, v248, s[60:61]\n"
      "v_cndmask_b32_e64 v65, v65, v248, s[62:63]\n"
      "v_cndmask_b32_e64 v66, v66, v248, s[64:65]\n"
      "v_cndmask_b32_e64 v67, v67, v248, s[66:67]\n"
      "v_cndmask_b32_e64 v76, v76, v248, s[60:61]\n"
      "v_cndmask_b32_e64 v77, v77, v248, s[62:63]\n"
      "v_cndmask_b32_e64 v78, v78, v248, s[64:65]\n"
      "v_cndmask_b32_e64 v79, v79, v248, s[66:67]\n"
      "v_cndmask_b32_e64 v112, v112, v248, s[60:61]\n"
      "v_cndmask_b32_e64 v113, v113, v248, s[62:63]\n"
      "v_cndmask_b32_e64 v114, v114, v248, s[64:65]\n"
      "v_cndmask_b32_e64 v115, v115, v248, s[66:67]\n"
      "v_cndmask_b32_e64 v124, v124, v248, s[60:61]\n"
      "v_cndmask_b32_e64 v125, v125, v248, s[62:63]\n"
      "v_cndmask_b32_e64 v126, v126, v248, s[64:65]\n"
      "v_cndmask_b32_e64 v127, v127, v248, s[66:67]\n"
      "s_branch L_maskback1_%=\n"
      "L_maskall1_%=:\n"
      "v_mov_b32 v64, v248\n"
      "v_mov_b32 v65, v248\n"
      "v_mov_b32 v66, v248\n"
      "v_mov_b32 v67, v248\n"
      "v_mov_b32 v68, v248\n"
      "v_mov_b32 v69, v248\n"
      "v_mov_b32 v70, v248\n"
      "v_mov_b32 v71, v248\n"
      "v_mov_b32 v72, v248\n"
      "v_mov_b32 v73, v248\n"
      "v_mov_b32 v74, v248\n"
      "v_mov_b32 v75, v248\n"
      "v_mov_b32 v76, v248\n"
      "v_mov_b32 v77, v248\n"
      "v_mov_b32 v78, v248\n"
      "v_mov_b32 v79, v248\n"
      "v_mov_b32 v80, v248\n"
      "v_mov_b32 v81, v248\n"
      "v_mov_b32 v82, v248\n"
      "v_mov_b32 v83, v248\n"
      "v_mov_b32 v84, v248\n"
      "v_mov_b32 v85, v248\n"
      "v_mov_b32 v86, v248\n"
      "v_mov_b32 v87, v248\n"
      "v_mov_b32 v88, v248\n"
      "v_mov_b32 v89, v248\n"
      "v_mov_b32 v90, v248\n"
      "v_mov_b32 v91, v248\n"
      "v_mov_b32 v92, v248\n"
      "v_mov_b32 v93, v248\n"
      "v_mov_b32 v94, v248\n"
      "v_mov_b32 v95, v248\n"
      "v_mov_b32 v96, v248\n"
      "v_mov_b32 v97, v248\n"
      "v_mov_b32 v98, v248\n"
      "v_mov_b32 v99, v248\n"
      "v_mov_b32 v100, v248\n"
      "v_mov_b32 v101, v248\n"
      "v_mov_b32 v102, v248\n"
      "v_mov_b32 v103, v248\n"
      "v_mov_b32 v104, v248\n"
      "v_mov_b32 v105, v248\n"
      "v_mov_b32 v106, v248\n"
      "v_mov_b32 v107, v248\n"
      "v_mov_b32 v108, v248\n"
      "v_mov_b32 v109, v248\n"
      "v_mov_b32 v110, v248\n"
      "v_mov_b32 v111, v248\n"
      "v_mov_b32 v112, v248\n"
      "v_mov_b32 v113, v248\n"
      "v_mov_b32 v114, v248\n"
      "v_mov_b32 v115, v248\n"
      "v_mov_b32 v116, v248\n"
      "v_mov_b32 v117, v248\n"
      "v_mov_b32 v118, v248\n"
      "v_mov_b32 v119, v248\n"
      "v_mov_b32 v120, v248\n"
      "v_mov_b32 v121, v248\n"
      "v_mov_b32 v122, v248\n"
      "v_mov_b32 v123, v248\n"
      "v_mov_b32 v124, v248\n"
      "v_mov_b32 v125, v248\n"
      "v_mov_b32 v126, v248\n"
      "v_mov_b32 v127, v248\n"
      "s_branch L_maskback1_%=\n"
      "L_rare0_%=:\n"
      "v_mov_b32 v232, v228\n"
      "v_mov_b32 v233, v229\n"
      "v_mov_b32 v234, v230\n"
      "v_mov_b32 v235, v231\n"
      "s_nop 1\n"
      "v_permlane32_swap_b32 v228, v232\n"
      "v_permlane32_swap_b32 v229, v233\n"
      "v_permlane32_swap_b32 v230, v234\n"
      "v_permlane32_swap_b32 v231, v235\n"
      "s_nop 1\n"
      "v_max_f32 v228, v228, v232\n"
      "v_max_f32 v229, v229, v233\n"
      "v_max_f32 v230, v230, v234\n"
      "v_max_f32 v231, v231, v235\n"
      "v_mov_b32 v232, v228\n"
      "v_mov_b32 v233, v229\n"
      "v_mov_b32 v234, v230\n"
      "v_mov_b32 v235, v231\n"
      "s_nop 1\n"
      "v_permlane16_swap_b32 v228, v232\n"
      "v_permlane16_swap_b32 v229, v233\n"
      "v_permlane16_swap_b32 v230, v234\n"
      "v_permlane16_swap_b32 v231, v235\n"
      "s_nop 1\n"
      "v_max_f32 v228, v228, v232\n"
      "v_max_f32 v229, v229, v233\n"
      "v_max_f32 v230, v230, v234\n"
      "v_max_f32 v231, v231, v235\n"
      "s_bitcmp1_b32 s44, s40\n"
      "s_cbranch_scc1 L_first0_%=\n"
      "v_max_f32 v228, 0, v228\n"
      "v_max_f32 v229, 0, v229\n"
      "v_max_f32 v230, 0, v230\n"
      "v_max_f32 v231, 0, v231\n"
      "v_exp_f32 v236, -v228\n"
      "v_exp_f32 v237, -v229\n"
      "v_exp_f32 v238, -v230\n"
      "v_exp_f32 v239, -v231\n"
      "s_bitset1_b32 s42, s40\n"
      "s_mov_b32 s45, 1\n"
      "s_branch L_common0_%=\n"
      "L_first0_%=:\n"
      "s_mov_b32 s41, 0x41000000\n"
      "v_max_f32_e64 v232, |v228|, |v229|\n"
      "v_max3_f32 v232, v232, |v230|, |v231|\n"
      "v_cmp_nle_f32_e64 vcc, v232, s41\n"
      "s_cbranch_vccz L_rareback0_%=\n"
      "v_max_f32 v228, 0xf149f2ca, v228\n"
      "v_max_f32 v229, 0xf149f2ca, v229\n"
      "v_max_f32 v230, 0xf149f2ca, v230\n"
      "v_max_f32 v231, 0xf149f2ca, v231\n"
      "L_common0_%=:\n"
      "v_sub_f32 v0, v0, v228\n"
      "v_sub_f32 v1, v1, v228\n"
      "v_sub_f32 v2, v2, v228\n"
      "v_sub_f32 v3, v3, v228\n"
      "v_sub_f32 v8, v8, v229\n"
      "v_sub_f32 v9, v9, v229\n"
      "v_sub_f32 v10, v10, v229\n"
      "v_sub_f32 v11, v11, v229\n"
      "v_sub_f32 v16, v16, v230\n"
      "v_sub_f32 v17, v17, v230\n"
      "v_sub_f32 v18, v18, v230\n"
      "v_sub_f32 v19, v19, v230\n"
      "v_sub_f32 v24, v24, v231\n"
      "v_sub_f32 v25, v25, v231\n"
      "v_sub_f32 v26, v26, v231\n"
      "v_sub_f32 v27, v27, v231\n"
      "v_sub_f32 v4, v4, v228\n"
      "v_sub_f32 v5, v5, v228\n"
      "v_sub_f32 v6, v6, v228\n"
      "v_sub_f32 v7, v7, v228\n"
      "v_sub_f32 v12, v12, v229\n"
      "v_sub_f32 v13, v13, v229\n"
      "v_sub_f32 v14, v14, v229\n"
      "v_sub_f32 v15, v15, v229\n"
      "v_sub_f32 v20, v20, v230\n"
      "v_sub_f32 v21, v21, v230\n"
      "v_sub_f32 v22, v22, v230\n"
      "v_sub_f32 v23, v23, v230\n"
      "v_sub_f32 v28, v28, v231\n"
      "v_sub_f32 v29, v29, v231\n"
      "v_sub_f32 v30, v30, v231\n"
      "v_sub_f32 v31, v31, v231\n"
      "v_sub_f32 v32, v32, v228\n"
      "v_sub_f32 v33, v33, v228\n"
      "v_sub_f32 v34, v34, v228\n"
      "v_sub_f32 v35, v35, v228\n"
      "v_sub_f32 v40, v40, v229\n"
      "v_sub_f32 v41, v41, v229\n"
      "v_sub_f32 v42, v42, v229\n"
      "v_sub_f32 v43, v43, v229\n"
      "v_sub_f32 v48, v48, v230\n"
      "v_sub_f32 v49, v49, v230\n"
      "v_sub_f32 v50, v50, v230\n"
      "v_sub_f32 v51, v51, v230\n"
      "v_sub_f32 v56, v56, v231\n"
      "v_sub_f32 v57, v57, v231\n"
      "v_sub_f32 v58, v58, v231\n"
      "v_sub_f32 v59, v59, v231\n"
      "v_sub_f32 v36, v36, v228\n"
      "v_sub_f32 v37, v37, v228\n"
      "v_sub_f32 v38, v38, v228\n"
      "v_sub_f32 v39, v39, v228\n"
      "v_sub_f32 v44, v44, v229\n"
      "v_sub_f32 v45, v45, v229\n"
      "v_sub_f32 v46, v46, v229\n"
      "v_sub_f32 v47, v47, v229\n"
      "v_sub_f32 v52, v52, v230\n"
      "v_sub_f32 v53, v53, v230\n"
      "v_sub_f32 v54, v54, v230\n"
      "v_sub_f32 v55, v55, v230\n"
      "v_sub_f32 v60, v60, v231\n"
      "v_sub_f32 v61, v61, v231\n"
      "v_sub_f32 v62, v62, v231\n"
      "v_sub_f32 v63, v63, v231\n"
      "v_sub_f32 v128, v128, v228\n"
      "v_sub_f32 v129, v129, v228\n"
      "v_sub_f32 v130, v130, v228\n"
      "v_sub_f32 v131, v131, v228\n"
      "v_sub_f32 v132, v132, v229\n"
      "v_sub_f32 v133, v133, v229\n"
      "v_sub_f32 v134, v134, v229\n"
      "v_sub_f32 v135, v135, v229\n"
      "v_sub_f32 v136, v136, v230\n"
      "v_sub_f32 v137, v137, v230\n"
      "v_sub_f32 v138, v138, v230\n"
      "v_sub_f32 v139, v139, v230\n"
      "v_sub_f32 v140, v140, v231\n"
      "v_sub_f32 v141, v141, v231\n"
      "v_sub_f32 v142, v142, v231\n"
      "v_sub_f32 v143, v143, v231\n"
      "s_branch L_rareback0_%=\n"
      "L_rare1_%=:\n"
      "v_mov_b32 v232, v228\n"
      "v_mov_b32 v233, v229\n"
      "v_mov_b32 v234, v230\n"
      "v_mov_b32 v235, v231\n"
      "s_nop 1\n"
      "v_permlane32_swap_b32 v228, v232\n"
      "v_permlane32_swap_b32 v229, v233\n"
      "v_permlane32_swap_b32 v230, v234\n"
      "v_permlane32_swap_b32 v231, v235\n"
      "s_nop 1\n"
      "v_max_f32 v228, v228, v232\n"
      "v_max_f32 v229, v229, v233\n"
      "v_max_f32 v230, v230, v234\n"
      "v_max_f32 v231, v231, v235\n"
      "v_mov_b32 v232, v228\n"
      "v_mov_b32 v233, v229\n"
      "v_mov_b32 v234, v230\n"
      "v_mov_b32 v235, v231\n"
      "s_nop 1\n"
      "v_permlane16_swap_b32 v228, v232\n"
      "v_permlane16_swap_b32 v229, v233\n"
      "v_permlane16_swap_b32 v230, v234\n"
      "v_permlane16_swap_b32 v231, v235\n"
      "s_nop 1\n"
      "v_max_f32 v228, v228, v232\n"
      "v_max_f32 v229, v229, v233\n"
      "v_max_f32 v230, v230, v234\n"
      "v_max_f32 v231, v231, v235\n"
      "s_bitcmp1_b32 s44, s40\n"
      "s_cbranch_scc1 L_first1_%=\n"
      "v_max_f32 v228, 0, v228\n"
      "v_max_f32 v229, 0, v229\n"
      "v_max_f32 v230, 0, v230\n"
      "v_max_f32 v231, 0, v231\n"
      "v_exp_f32 v236, -v228\n"
      "v_exp_f32 v237, -v229\n"
      "v_exp_f32 v238, -v230\n"
      "v_exp_f32 v239, -v231\n"
      "s_bitset1_b32 s42, s40\n"
      "s_mov_b32 s45, 1\n"
      "s_branch L_common1_%=\n"
      "L_first1_%=:\n"
      "s_mov_b32 s41, 0x41000000\n"
      "v_max_f32_e64 v232, |v228|, |v229|\n"
      "v_max3_f32 v232, v232, |v230|, |v231|\n"
      "v_cmp_nle_f32_e64 vcc, v232, s41\n"
      "s_cbranch_vccz L_rareback1_%=\n"
      "v_max_f32 v228, 0xf149f2ca, v228\n"
      "v_max_f32 v229, 0xf149f2ca, v229\n"
      "v_max_f32 v230, 0xf149f2ca, v230\n"
      "v_max_f32 v231, 0xf149f2ca, v231\n"
      "L_common1_%=:\n"
      "v_sub_f32 v64, v64, v228\n"
      "v_sub_f32 v65, v65, v228\n"
      "v_sub_f32 v66, v66, v228\n"
      "v_sub_f32 v67, v67, v228\n"
      "v_sub_f32 v72, v72, v229\n"
      "v_sub_f32 v73, v73, v229\n"
      "v_sub_f32 v74, v74, v229\n"
      "v_sub_f32 v75, v75, v229\n"
      "v_sub_f32 v80, v80, v230\n"
      "v_sub_f32 v81, v81, v230\n"
      "v_sub_f32 v82, v82, v230\n"
      "v_sub_f32 v83, v83, v230\n"
      "v_sub_f32 v88, v88, v231\n"
      "v_sub_f32 v89, v89, v231\n"
      "v_sub_f32 v90, v90, v231\n"
      "v_sub_f32 v91, v91, v231\n"
      "v_sub_f32 v68, v68, v228\n"
      "v_sub_f32 v69, v69, v228\n"
      "v_sub_f32 v70, v70, v228\n"
      "v_sub_f32 v71, v71, v228\n"
      "v_sub_f32 v76, v76, v229\n"
      "v_sub_f32 v77, v77, v229\n"
      "v_sub_f32 v78, v78, v229\n"
      "v_sub_f32 v79, v79, v229\n"
      "v_sub_f32 v84, v84, v230\n"
      "v_sub_f32 v85, v85, v230\n"
      "v_sub_f32 v86, v86, v230\n"
      "v_sub_f32 v87, v87, v230\n"
      "v_sub_f32 v92, v92, v231\n"
      "v_sub_f32 v93, v93, v231\n"
      "v_sub_f32 v94, v94, v231\n"
      "v_sub_f32 v95, v95, v231\n"
      "v_sub_f32 v96, v96, v228\n"
      "v_sub_f32 v97, v97, v228\n"
      "v_sub_f32 v98, v98, v228\n"
      "v_sub_f32 v99, v99, v228\n"
      "v_sub_f32 v104, v104, v229\n"
      "v_sub_f32 v105, v105, v229\n"
      "v_sub_f32 v106, v106, v229\n"
      "v_sub_f32 v107, v107, v229\n"
      "v_sub_f32 v112, v112, v230\n"
      "v_sub_f32 v113, v113, v230\n"
      "v_sub_f32 v114, v114, v230\n"
      "v_sub_f32 v115, v115, v230\n"
      "v_sub_f32 v120, v120, v231\n"
      "v_sub_f32 v121, v121, v231\n"
      "v_sub_f32 v122, v122, v231\n"
      "v_sub_f32 v123, v123, v231\n"
      "v_sub_f32 v100, v100, v228\n"
      "v_sub_f32 v101, v101, v228\n"
      "v_sub_f32 v102, v102, v228\n"
      "v_sub_f32 v103, v103, v228\n"
      "v_sub_f32 v108, v108, v229\n"
      "v_sub_f32 v109, v109, v229\n"
      "v_sub_f32 v110, v110, v229\n"
      "v_sub_f32 v111, v111, v229\n"
      "v_sub_f32 v116, v116, v230\n"
      "v_sub_f32 v117, v117, v230\n"
      "v_sub_f32 v118, v118, v230\n"
      "v_sub_f32 v119, v119, v230\n"
      "v_sub_f32 v124, v124, v231\n"
      "v_sub_f32 v125, v125, v231\n"
      "v_sub_f32 v126, v126, v231\n"
      "v_sub_f32 v127, v127, v231\n"
      "v_sub_f32 v128, v128, v228\n"
      "v_sub_f32 v129, v129, v228\n"
      "v_sub_f32 v130, v130, v228\n"
      "v_sub_f32 v131, v131, v228\n"
      "v_sub_f32 v132, v132, v229\n"
      "v_sub_f32 v133, v133, v229\n"
      "v_sub_f32 v134, v134, v229\n"
      "v_sub_f32 v135, v135, v229\n"
      "v_sub_f32 v136, v136, v230\n"
      "v_sub_f32 v137, v137, v230\n"
      "v_sub_f32 v138, v138, v230\n"
      "v_sub_f32 v139, v139, v230\n"
      "v_sub_f32 v140, v140, v231\n"
      "v_sub_f32 v141, v141, v231\n"
      "v_sub_f32 v142, v142, v231\n"
      "v_sub_f32 v143, v143, v231\n"
      "s_branch L_rareback1_%=\n"
      "L_hook_%=:\n"
      "s_nop 7\n"
      "s_cmp_eq_u32 s45, 0\n"
      "s_cbranch_scc1 L_h1_%=\n"
      "v_accvgpr_read_b32 v248, a0\n"
      "v_accvgpr_read_b32 v249, a1\n"
      "v_accvgpr_read_b32 v250, a2\n"
      "v_accvgpr_read_b32 v251, a3\n"
      "v_accvgpr_read_b32 v252, a4\n"
      "v_accvgpr_read_b32 v253, a5\n"
      "v_accvgpr_read_b32 v254, a6\n"
      "v_accvgpr_read_b32 v255, a7\n"
      "v_mul_f32 v248, v248, v236\n"
      "v_mul_f32 v249, v249, v236\n"
      "v_mul_f32 v250, v250, v236\n"
      "v_mul_f32 v251, v251, v236\n"
      "v_mul_f32 v252, v252, v236\n"
      "v_mul_f32 v253, v253, v236\n"
      "v_mul_f32 v254, v254, v236\n"
      "v_mul_f32 v255, v255, v236\n"
      "v_accvgpr_write_b32 a0, v248\n"
      "v_accvgpr_write_b32 a1, v249\n"
      "v_accvgpr_write_b32 a2, v250\n"
      "v_accvgpr_write_b32 a3, v251\n"
      "v_accvgpr_write_b32 a4, v252\n"
      "v_accvgpr_write_b32 a5, v253\n"
      "v_accvgpr_write_b32 a6, v254\n"
      "v_accvgpr_write_b32 a7, v255\n"
      "v_accvgpr_read_b32 v248, a8\n"
      "v_accvgpr_read_b32 v249, a9\n"
      "v_accvgpr_read_b32 v250, a10\n"
      "v_accvgpr_read_b32 v251, a11\n"
      "v_accvgpr_read_b32 v252, a12\n"
      "v_accvgpr_read_b32 v253, a13\n"
      "v_accvgpr_read_b32 v254, a14\n"
      "v_accvgpr_read_b32 v255, a15\n"
      "v_mul_f32 v248, v248, v237\n"
      "v_mul_f32 v249, v249, v237\n"
      "v_mul_f32 v250, v250, v237\n"
      "v_mul_f32 v251, v251, v237\n"
      "v_mul_f32 v252, v252, v237\n"
      "v_mul_f32 v253, v253, v237\n"
      "v_mul_f32 v254, v254, v237\n"
      "v_mul_f32 v255, v255, v237\n"
      "v_accvgpr_write_b32 a8, v248\n"
      "v_accvgpr_write_b32 a9, v249\n"
      "v_accvgpr_write_b32 a10, v250\n"
      "v_accvgpr_write_b32 a11, v251\n"
      "v_accvgpr_write_b32 a12, v252\n"
      "v_accvgpr_write_b32 a13, v253\n"
      "v_accvgpr_write_b32 a14, v254\n"
      "v_accvgpr_write_b32 a15, v255\n"
      "v_accvgpr_read_b32 v248, a16\n"
      "v_accvgpr_read_b32 v249, a17\n"
      "v_accvgpr_read_b32 v250, a18\n"
      "v_accvgpr_read_b32 v251, a19\n"
      "v_accvgpr_read_b32 v252, a20\n"
      "v_accvgpr_read_b32 v253, a21\n"
      "v_accvgpr_read_b32 v254, a22\n"
      "v_accvgpr_read_b32 v255, a23\n"
      "v_mul_f32 v248, v248, v238\n"
      "v_mul_f32 v249, v249, v238\n"
      "v_mul_f32 v250, v250, v238\n"
      "v_mul_f32 v251, v251, v238\n"
      "v_mul_f32 v252, v252, v238\n"
      "v_mul_f32 v253, v253, v238\n"
      "v_mul_f32 v254, v254, v238\n"
      "v_mul_f32 v255, v255, v238\n"
      "v_accvgpr_write_b32 a16, v248\n"
      "v_accvgpr_write_b32 a17, v249\n"
      "v_accvgpr_write_b32 a18, v250\n"
      "v_accvgpr_write_b32 a19, v251\n"
      "v_accvgpr_write_b32 a20, v252\n"
      "v_accvgpr_write_b32 a21, v253\n"
      "v_accvgpr_write_b32 a22, v254\n"
      "v_accvgpr_write_b32 a23, v255\n"
      "v_accvgpr_read_b32 v248, a24\n"
      "v_accvgpr_read_b32 v249, a25\n"
      "v_accvgpr_read_b32 v250, a26\n"
      "v_accvgpr_read_b32 v251, a27\n"
      "v_accvgpr_read_b32 v252, a28\n"
      "v_accvgpr_read_b32 v253, a29\n"
      "v_accvgpr_read_b32 v254, a30\n"
      "v_accvgpr_read_b32 v255, a31\n"
      "v_mul_f32 v248, v248, v239\n"
      "v_mul_f32 v249, v249, v239\n"
      "v_mul_f32 v250, v250, v239\n"
      "v_mul_f32 v251, v251, v239\n"
      "v_mul_f32 v252, v252, v239\n"
      "v_mul_f32 v253, v253, v239\n"
      "v_mul_f32 v254, v254, v239\n"
      "v_mul_f32 v255, v255, v239\n"
      "v_accvgpr_write_b32 a24, v248\n"
      "v_accvgpr_write_b32 a25, v249\n"
      "v_accvgpr_write_b32 a26, v250\n"
      "v_accvgpr_write_b32 a27, v251\n"
      "v_accvgpr_write_b32 a28, v252\n"
      "v_accvgpr_write_b32 a29, v253\n"
      "v_accvgpr_write_b32 a30, v254\n"
      "v_accvgpr_write_b32 a31, v255\n"
      "v_accvgpr_read_b32 v248, a32\n"
      "v_accvgpr_read_b32 v249, a33\n"
      "v_accvgpr_read_b32 v250, a34\n"
      "v_accvgpr_read_b32 v251, a35\n"
      "v_accvgpr_read_b32 v252, a36\n"
      "v_accvgpr_read_b32 v253, a37\n"
      "v_accvgpr_read_b32 v254, a38\n"
      "v_accvgpr_read_b32 v255, a39\n"
      "v_mul_f32 v248, v248, v236\n"
      "v_mul_f32 v249, v249, v236\n"
      "v_mul_f32 v250, v250, v236\n"
      "v_mul_f32 v251, v251, v236\n"
      "v_mul_f32 v252, v252, v236\n"
      "v_mul_f32 v253, v253, v236\n"
      "v_mul_f32 v254, v254, v236\n"
      "v_mul_f32 v255, v255, v236\n"
      "v_accvgpr_write_b32 a32, v248\n"
      "v_accvgpr_write_b32 a33, v249\n"
      "v_accvgpr_write_b32 a34, v250\n"
      "v_accvgpr_write_b32 a35, v251\n"
      "v_accvgpr_write_b32 a36, v252\n"
      "v_accvgpr_write_b32 a37, v253\n"
      "v_accvgpr_write_b32 a38, v254\n"
      "v_accvgpr_write_b32 a39, v255\n"
      "v_accvgpr_read_b32 v248, a40\n"
      "v_accvgpr_read_b32 v249, a41\n"
      "v_accvgpr_read_b32 v250, a42\n"
      "v_accvgpr_read_b32 v251, a43\n"
      "v_accvgpr_read_b32 v252, a44\n"
      "v_accvgpr_read_b32 v253, a45\n"
      "v_accvgpr_read_b32 v254, a46\n"
      "v_accvgpr_read_b32 v255, a47\n"
      "v_mul_f32 v248, v248, v237\n"
      "v_mul_f32 v249, v249, v237\n"
      "v_mul_f32 v250, v250, v237\n"
      "v_mul_f32 v251, v251, v237\n"
      "v_mul_f32 v252, v252, v237\n"
      "v_mul_f32 v253, v253, v237\n"
      "v_mul_f32 v254, v254, v237\n"
      "v_mul_f32 v255, v255, v237\n"
      "v_accvgpr_write_b32 a40, v248\n"
      "v_accvgpr_write_b32 a41, v249\n"
      "v_accvgpr_write_b32 a42, v250\n"
      "v_accvgpr_write_b32 a43, v251\n"
      "v_accvgpr_write_b32 a44, v252\n"
      "v_accvgpr_write_b32 a45, v253\n"
      "v_accvgpr_write_b32 a46, v254\n"
      "v_accvgpr_write_b32 a47, v255\n"
      "v_accvgpr_read_b32 v248, a48\n"
      "v_accvgpr_read_b32 v249, a49\n"
      "v_accvgpr_read_b32 v250, a50\n"
      "v_accvgpr_read_b32 v251, a51\n"
      "v_accvgpr_read_b32 v252, a52\n"
      "v_accvgpr_read_b32 v253, a53\n"
      "v_accvgpr_read_b32 v254, a54\n"
      "v_accvgpr_read_b32 v255, a55\n"
      "v_mul_f32 v248, v248, v238\n"
      "v_mul_f32 v249, v249, v238\n"
      "v_mul_f32 v250, v250, v238\n"
      "v_mul_f32 v251, v251, v238\n"
      "v_mul_f32 v252, v252, v238\n"
      "v_mul_f32 v253, v253, v238\n"
      "v_mul_f32 v254, v254, v238\n"
      "v_mul_f32 v255, v255, v238\n"
      "v_accvgpr_write_b32 a48, v248\n"
      "v_accvgpr_write_b32 a49, v249\n"
      "v_accvgpr_write_b32 a50, v250\n"
      "v_accvgpr_write_b32 a51, v251\n"
      "v_accvgpr_write_b32 a52, v252\n"
      "v_accvgpr_write_b32 a53, v253\n"
      "v_accvgpr_write_b32 a54, v254\n"
      "v_accvgpr_write_b32 a55, v255\n"
      "v_accvgpr_read_b32 v248, a56\n"
      "v_accvgpr_read_b32 v249, a57\n"
      "v_accvgpr_read_b32 v250, a58\n"
      "v_accvgpr_read_b32 v251, a59\n"
      "v_accvgpr_read_b32 v252, a60\n"
      "v_accvgpr_read_b32 v253, a61\n"
      "v_accvgpr_read_b32 v254, a62\n"
      "v_accvgpr_read_b32 v255, a63\n"
      "v_mul_f32 v248, v248, v239\n"
      "v_mul_f32 v249, v249, v239\n"
      "v_mul_f32 v250, v250, v239\n"
      "v_mul_f32 v251, v251, v239\n"
      "v_mul_f32 v252, v252, v239\n"
      "v_mul_f32 v253, v253, v239\n"
      "v_mul_f32 v254, v254, v239\n"
      "v_mul_f32 v255, v255, v239\n"
      "v_accvgpr_write_b32 a56, v248\n"
      "v_accvgpr_write_b32 a57, v249\n"
      "v_accvgpr_write_b32 a58, v250\n"
      "v_accvgpr_write_b32 a59, v251\n"
      "v_accvgpr_write_b32 a60, v252\n"
      "v_accvgpr_write_b32 a61, v253\n"
      "v_accvgpr_write_b32 a62, v254\n"
      "v_accvgpr_write_b32 a63, v255\n"
      "v_accvgpr_read_b32 v248, a64\n"
      "v_accvgpr_read_b32 v249, a65\n"
      "v_accvgpr_read_b32 v250, a66\n"
      "v_accvgpr_read_b32 v251, a67\n"
      "v_accvgpr_read_b32 v252, a68\n"
      "v_accvgpr_read_b32 v253, a69\n"
      "v_accvgpr_read_b32 v254, a70\n"
      "v_accvgpr_read_b32 v255, a71\n"
      "v_mul_f32 v248, v248, v236\n"
      "v_mul_f32 v249, v249, v236\n"
      "v_mul_f32 v250, v250, v236\n"
      "v_mul_f32 v251, v251, v236\n"
      "v_mul_f32 v252, v252, v236\n"
      "v_mul_f32 v253, v253, v236\n"
      "v_mul_f32 v254, v254, v236\n"
      "v_mul_f32 v255, v255, v236\n"
      "v_accvgpr_write_b32 a64, v248\n"
      "v_accvgpr_write_b32 a65, v249\n"
      "v_accvgpr_write_b32 a66, v250\n"
      "v_accvgpr_write_b32 a67, v251\n"
      "v_accvgpr_write_b32 a68, v252\n"
      "v_accvgpr_write_b32 a69, v253\n"
      "v_accvgpr_write_b32 a70, v254\n"
      "v_accvgpr_write_b32 a71, v255\n"
      "v_accvgpr_read_b32 v248, a72\n"
      "v_accvgpr_read_b32 v249, a73\n"
      "v_accvgpr_read_b32 v250, a74\n"
      "v_accvgpr_read_b32 v251, a75\n"
      "v_accvgpr_read_b32 v252, a76\n"
      "v_accvgpr_read_b32 v253, a77\n"
      "v_accvgpr_read_b32 v254, a78\n"
      "v_accvgpr_read_b32 v255, a79\n"
      "v_mul_f32 v248, v248, v237\n"
      "v_mul_f32 v249, v249, v237\n"
      "v_mul_f32 v250, v250, v237\n"
      "v_mul_f32 v251, v251, v237\n"
      "v_mul_f32 v252, v252, v237\n"
      "v_mul_f32 v253, v253, v237\n"
      "v_mul_f32 v254, v254, v237\n"
      "v_mul_f32 v255, v255, v237\n"
      "v_accvgpr_write_b32 a72, v248\n"
      "v_accvgpr_write_b32 a73, v249\n"
      "v_accvgpr_write_b32 a74, v250\n"
      "v_accvgpr_write_b32 a75, v251\n"
      "v_accvgpr_write_b32 a76, v252\n"
      "v_accvgpr_write_b32 a77, v253\n"
      "v_accvgpr_write_b32 a78, v254\n"
      "v_accvgpr_write_b32 a79, v255\n"
      "v_accvgpr_read_b32 v248, a80\n"
      "v_accvgpr_read_b32 v249, a81\n"
      "v_accvgpr_read_b32 v250, a82\n"
      "v_accvgpr_read_b32 v251, a83\n"
      "v_accvgpr_read_b32 v252, a84\n"
      "v_accvgpr_read_b32 v253, a85\n"
      "v_accvgpr_read_b32 v254, a86\n"
      "v_accvgpr_read_b32 v255, a87\n"
      "v_mul_f32 v248, v248, v238\n"
      "v_mul_f32 v249, v249, v238\n"
      "v_mul_f32 v250, v250, v238\n"
      "v_mul_f32 v251, v251, v238\n"
      "v_mul_f32 v252, v252, v238\n"
      "v_mul_f32 v253, v253, v238\n"
      "v_mul_f32 v254, v254, v238\n"
      "v_mul_f32 v255, v255, v238\n"
      "v_accvgpr_write_b32 a80, v248\n"
      "v_accvgpr_write_b32 a81, v249\n"
      "v_accvgpr_write_b32 a82, v250\n"
      "v_accvgpr_write_b32 a83, v251\n"
      "v_accvgpr_write_b32 a84, v252\n"
      "v_accvgpr_write_b32 a85, v253\n"
      "v_accvgpr_write_b32 a86, v254\n"
      "v_accvgpr_write_b32 a87, v255\n"
      "v_accvgpr_read_b32 v248, a88\n"
      "v_accvgpr_read_b32 v249, a89\n"
      "v_accvgpr_read_b32 v250, a90\n"
      "v_accvgpr_read_b32 v251, a91\n"
      "v_accvgpr_read_b32 v252, a92\n"
      "v_accvgpr_read_b32 v253, a93\n"
      "v_accvgpr_read_b32 v254, a94\n"
      "v_accvgpr_read_b32 v255, a95\n"
      "v_mul_f32 v248, v248, v239\n"
      "v_mul_f32 v249, v249, v239\n"
      "v_mul_f32 v250, v250, v239\n"
      "v_mul_f32 v251, v251, v239\n"
      "v_mul_f32 v252, v252, v239\n"
      "v_mul_f32 v253, v253, v239\n"
      "v_mul_f32 v254, v254, v239\n"
      "v_mul_f32 v255, v255, v239\n"
      "v_accvgpr_write_b32 a88, v248\n"
      "v_accvgpr_write_b32 a89, v249\n"
      "v_accvgpr_write_b32 a90, v250\n"
      "v_accvgpr_write_b32 a91, v251\n"
      "v_accvgpr_write_b32 a92, v252\n"
      "v_accvgpr_write_b32 a93, v253\n"
      "v_accvgpr_write_b32 a94, v254\n"
      "v_accvgpr_write_b32 a95, v255\n"
      "v_accvgpr_read_b32 v248, a96\n"
      "v_accvgpr_read_b32 v249, a97\n"
      "v_accvgpr_read_b32 v250, a98\n"
      "v_accvgpr_read_b32 v251, a99\n"
      "v_accvgpr_read_b32 v252, a100\n"
      "v_accvgpr_read_b32 v253, a101\n"
      "v_accvgpr_read_b32 v254, a102\n"
      "v_accvgpr_read_b32 v255, a103\n"
      "v_mul_f32 v248, v248, v236\n"
      "v_mul_f32 v249, v249, v236\n"
      "v_mul_f32 v250, v250, v236\n"
      "v_mul_f32 v251, v251, v236\n"
      "v_mul_f32 v252, v252, v236\n"
      "v_mul_f32 v253, v253, v236\n"
      "v_mul_f32 v254, v254, v236\n"
      "v_mul_f32 v255, v255, v236\n"
      "v_accvgpr_write_b32 a96, v248\n"
      "v_accvgpr_write_b32 a97, v249\n"
      "v_accvgpr_write_b32 a98, v250\n"
      "v_accvgpr_write_b32 a99, v251\n"
      "v_accvgpr_write_b32 a100, v252\n"
      "v_accvgpr_write_b32 a101, v253\n"
      "v_accvgpr_write_b32 a102, v254\n"
      "v_accvgpr_write_b32 a103, v255\n"
      "v_accvgpr_read_b32 v248, a104\n"
      "v_accvgpr_read_b32 v249, a105\n"
      "v_accvgpr_read_b32 v250, a106\n"
      "v_accvgpr_read_b32 v251, a107\n"
      "v_accvgpr_read_b32 v252, a108\n"
      "v_accvgpr_read_b32 v253, a109\n"
      "v_accvgpr_read_b32 v254, a110\n"
      "v_accvgpr_read_b32 v255, a111\n"
      "v_mul_f32 v248, v248, v237\n"
      "v_mul_f32 v249, v249, v237\n"
      "v_mul_f32 v250, v250, v237\n"
      "v_mul_f32 v251, v251, v237\n"
      "v_mul_f32 v252, v252, v237\n"
      "v_mul_f32 v253, v253, v237\n"
      "v_mul_f32 v254, v254, v237\n"
      "v_mul_f32 v255, v255, v237\n"
      "v_accvgpr_write_b32 a104, v248\n"
      "v_accvgpr_write_b32 a105, v249\n"
      "v_accvgpr_write_b32 a106, v250\n"
      "v_accvgpr_write_b32 a107, v251\n"
      "v_accvgpr_write_b32 a108, v252\n"
      "v_accvgpr_write_b32 a109, v253\n"
      "v_accvgpr_write_b32 a110, v254\n"
      "v_accvgpr_write_b32 a111, v255\n"
      "v_accvgpr_read_b32 v248, a112\n"
      "v_accvgpr_read_b32 v249, a113\n"
      "v_accvgpr_read_b32 v250, a114\n"
      "v_accvgpr_read_b32 v251, a115\n"
      "v_accvgpr_read_b32 v252, a116\n"
      "v_accvgpr_read_b32 v253, a117\n"
      "v_accvgpr_read_b32 v254, a118\n"
      "v_accvgpr_read_b32 v255, a119\n"
      "v_mul_f32 v248, v248, v238\n"
      "v_mul_f32 v249, v249, v238\n"
      "v_mul_f32 v250, v250, v238\n"
      "v_mul_f32 v251, v251, v238\n"
      "v_mul_f32 v252, v252, v238\n"
      "v_mul_f32 v253, v253, v238\n"
      "v_mul_f32 v254, v254, v238\n"
      "v_mul_f32 v255, v255, v238\n"
      "v_accvgpr_write_b32 a112, v248\n"
      "v_accvgpr_write_b32 a113, v249\n"
      "v_accvgpr_write_b32 a114, v250\n"
      "v_accvgpr_write_b32 a115, v251\n"
      "v_accvgpr_write_b32 a116, v252\n"
      "v_accvgpr_write_b32 a117, v253\n"
      "v_accvgpr_write_b32 a118, v254\n"
      "v_accvgpr_write_b32 a119, v255\n"
      "v_accvgpr_read_b32 v248, a120\n"
      "v_accvgpr_read_b32 v249, a121\n"
      "v_accvgpr_read_b32 v250, a122\n"
      "v_accvgpr_read_b32 v251, a123\n"
      "v_accvgpr_read_b32 v252, a124\n"
      "v_accvgpr_read_b32 v253, a125\n"
      "v_accvgpr_read_b32 v254, a126\n"
      "v_accvgpr_read_b32 v255, a127\n"
      "v_mul_f32 v248, v248, v239\n"
      "v_mul_f32 v249, v249, v239\n"
      "v_mul_f32 v250, v250, v239\n"
      "v_mul_f32 v251, v251, v239\n"
      "v_mul_f32 v252, v252, v239\n"
      "v_mul_f32 v253, v253, v239\n"
      "v_mul_f32 v254, v254, v239\n"
      "v_mul_f32 v255, v255, v239\n"
      "v_accvgpr_write_b32 a120, v248\n"
      "v_accvgpr_write_b32 a121, v249\n"
      "v_accvgpr_write_b32 a122, v250\n"
      "v_accvgpr_write_b32 a123, v251\n"
      "v_accvgpr_write_b32 a124, v252\n"
      "v_accvgpr_write_b32 a125, v253\n"
      "v_accvgpr_write_b32 a126, v254\n"
      "v_accvgpr_write_b32 a127, v255\n"
      "v_mul_f32 v144, v144, v236\n"
      "v_mul_f32 v145, v145, v236\n"
      "v_mul_f32 v146, v146, v236\n"
      "v_mul_f32 v147, v147, v236\n"
      "v_mul_f32 v148, v148, v237\n"
      "v_mul_f32 v149, v149, v237\n"
      "v_mul_f32 v150, v150, v237\n"
      "v_mul_f32 v151, v151, v237\n"
      "v_mul_f32 v152, v152, v238\n"
      "v_mul_f32 v153, v153, v238\n"
      "v_mul_f32 v154, v154, v238\n"
      "v_mul_f32 v155, v155, v238\n"
      "v_mul_f32 v156, v156, v239\n"
      "v_mul_f32 v157, v157, v239\n"
      "v_mul_f32 v158, v158, v239\n"
      "v_mul_f32 v159, v159, v239\n"
      "s_mov_b32 s45, 0\n"
      "L_h1_%=:\n"
      "s_cmp_eq_u32 s40, s52\n"
      "s_cbranch_scc0 L_h2_%=\n"
      "s_and_b32 s53, %12, 1\n"
      "s_lshl_b32 s53, s53, 14\n"
      "s_add_u32 s53, s53, 131072\n"
      "v_add_u32 v232, s53, v242\n"
      "ds_read_b128 a[128:131], v232 offset:0\n"
      "ds_read_b128 a[132:135], v232 offset:1024\n"
      "ds_read_b128 a[136:139], v232 offset:2048\n"
      "ds_read_b128 a[140:143], v232 offset:3072\n"
      "ds_read_b128 a[144:147], v232 offset:4096\n"
      "ds_read_b128 a[148:151], v232 offset:5120\n"
      "ds_read_b128 a[152:155], v232 offset:6144\n"
      "ds_read_b128 a[156:159], v232 offset:7168\n"
      "ds_read_b128 a[160:163], v232 offset:8192\n"
      "ds_read_b128 a[164:167], v232 offset:9216\n"
      "ds_read_b128 a[168:171], v232 offset:10240\n"
      "ds_read_b128 a[172:175], v232 offset:11264\n"
      "ds_read_b128 a[176:179], v232 offset:12288\n"
      "ds_read_b128 a[180:183], v232 offset:13312\n"
      "ds_read_b128 a[184:187], v232 offset:14336\n"
      "ds_read_b128 a[188:191], v232 offset:15360\n"
      "v_sub_f32 v236, 0, v128\n"
      "v_sub_f32 v237, 0, v132\n"
      "v_sub_f32 v238, 0, v136\n"
      "v_sub_f32 v239, 0, v140\n"
      "v_mov_b32 v128, 0\n"
      "v_mov_b32 v129, 0\n"
      "v_mov_b32 v130, 0\n"
      "v_mov_b32 v131, 0\n"
      "v_mov_b32 v132, 0\n"
      "v_mov_b32 v133, 0\n"
      "v_mov_b32 v134, 0\n"
      "v_mov_b32 v135, 0\n"
      "v_mov_b32 v136, 0\n"
      "v_mov_b32 v137, 0\n"
      "v_mov_b32 v138, 0\n"
      "v_mov_b32 v139, 0\n"
      "v_mov_b32 v140, 0\n"
      "v_mov_b32 v141, 0\n"
      "v_mov_b32 v142, 0\n"
      "v_mov_b32 v143, 0\n"
      "s_mov_b32 s41, 0x7fc00000\n"
      "L_h2_%=:\n"
      "s_bitcmp1_b32 s44, s40\n"
      "s_cbranch_scc0 L_h4_%=\n"
      "s_cmp_eq_u32 s40, 0\n"
      "s_cbranch_scc1 L_h3_%=\n"
      "L_epi_%=:\n"
      "s_lshl_b32 s53, %12, 11\n"
      "s_add_u32 s53, s53, 131072\n"
      "v_add_u32 v232, s53, v242\n"
      "v_mov_b32 v248, v144\n"
      "v_mov_b32 v249, v148\n"
      "v_mov_b32 v250, v152\n"
      "v_mov_b32 v251, v156\n"
      "ds_write_b128 v232, v[236:239]\n"
      "ds_write_b128 v232, v[248:251] offset:1024\n"
      "s_xor_b32 s53, %12, 2\n"
      "s_lshl_b32 s53, s53, 11\n"
      "s_add_u32 s53, s53, 131072\n"
      "v_add_u32 v233, s53, v242\n"
      "s_add_u32 s53, s40, 1\n"
      "s_and_b32 s53, s53, 1\n"
      "s_lshl_b32 s53, s53, 15\n"
      "s_and_b32 s54, %12, 1\n"
      "s_lshl_b32 s54, s54, 14\n"
      "s_add_u32 s53, s53, s54\n"
      "s_add_u32 s54, s54, 131072\n"
      "s_waitcnt lgkmcnt(0)\n"
      "s_barrier\n"
      "ds_read_b128 v[228:231], v233\n"
      "ds_read_b128 v[252:255], v233 offset:1024\n"
      "v_add_u32 v232, s53, v242\n"
      "v_add_u32 v233, s54, v242\n"
      "v_and_b32 v235, 0x3f0, v241\n"
      "s_waitcnt lgkmcnt(0)\n"
      "s_barrier\n"
      "v_max_f32 v234, v236, v228\n"
      "v_sub_f32 v236, v236, v234\n"
      "v_sub_f32 v228, v228, v234\n"
      "v_exp_f32 v236, v236\n"
      "v_exp_f32 v228, v228\n"
      "s_nop 0\n"
      "v_mul_f32 v234, v236, v248\n"
      "v_fmac_f32 v234, v228, v252\n"
      "v_rcp_f32 v234, v234\n"
      "s_nop 0\n"
      "v_mul_f32 v236, v236, v234\n"
      "v_mul_f32 v228, v228, v234\n"
      "v_max_f32 v234, v237, v229\n"
      "v_sub_f32 v237, v237, v234\n"
      "v_sub_f32 v229, v229, v234\n"
      "v_exp_f32 v237, v237\n"
      "v_exp_f32 v229, v229\n"
      "s_nop 0\n"
      "v_mul_f32 v234, v237, v249\n"
      "v_fmac_f32 v234, v229, v253\n"
      "v_rcp_f32 v234, v234\n"
      "s_nop 0\n"
      "v_mul_f32 v237, v237, v234\n"
      "v_mul_f32 v229, v229, v234\n"
      "v_max_f32 v234, v238, v230\n"
      "v_sub_f32 v238, v238, v234\n"
      "v_sub_f32 v230, v230, v234\n"
      "v_exp_f32 v238, v238\n"
      "v_exp_f32 v230, v230\n"
      "s_nop 0\n"
      "v_mul_f32 v234, v238, v250\n"
      "v_fmac_f32 v234, v230, v254\n"
      "v_rcp_f32 v234, v234\n"
      "s_nop 0\n"
      "v_mul_f32 v238, v238, v234\n"
      "v_mul_f32 v230, v230, v234\n"
      "v_max_f32 v234, v239, v231\n"
      "v_sub_f32 v239, v239, v234\n"
      "v_sub_f32 v231, v231, v234\n"
      "v_exp_f32 v239, v239\n"
      "v_exp_f32 v231, v231\n"
      "s_nop 0\n"
      "v_mul_f32 v234, v239, v251\n"
      "v_fmac_f32 v234, v231, v255\n"
      "v_rcp_f32 v234, v234\n"
      "s_nop 0\n"
      "v_mul_f32 v239, v239, v234\n"
      "v_mul_f32 v231, v231, v234\n"
      "s_cmp_eq_u32 s55, 0\n"
      "s_cbranch_scc0 L_epi1_%=\n"
      "ds_write_b128 v233, a[64:67] offset:0\n"
      "ds_write_b128 v233, a[68:71] offset:1024\n"
      "ds_write_b128 v233, a[72:75] offset:2048\n"
      "ds_write_b128 v233, a[76:79] offset:3072\n"
      "ds_write_b128 v233, a[80:83] offset:4096\n"
      "ds_write_b128 v233, a[84:87] offset:5120\n"
      "ds_write_b128 v233, a[88:91] offset:6144\n"
      "ds_write_b128 v233, a[92:95] offset:7168\n"
      "ds_write_b128 v233, a[96:99] offset:8192\n"
      "ds_write_b128 v233, a[100:103] offset:9216\n"
      "ds_write_b128 v233, a[104:107] offset:10240\n"
      "ds_write_b128 v233, a[108:111] offset:11264\n"
      "ds_write_b128 v233, a[112:115] offset:12288\n"
      "ds_write_b128 v233, a[116:119] offset:13312\n"
      "ds_write_b128 v233, a[120:123] offset:14336\n"
      "ds_write_b128 v233, a[124:127] offset:15360\n"
      "s_waitcnt lgkmcnt(0)\n"
      "s_barrier\n"
      "ds_read_b128 v[160:163], v232 offset:0\n"
      "ds_read_b128 v[164:167], v232 offset:1024\n"
      "ds_read_b128 v[168:171], v232 offset:2048\n"
      "ds_read_b128 v[172:175], v232 offset:3072\n"
      "ds_read_b128 v[176:179], v232 offset:4096\n"
      "ds_read_b128 v[180:183], v232 offset:5120\n"
      "ds_read_b128 v[184:187], v232 offset:6144\n"
      "ds_read_b128 v[188:191], v232 offset:7168\n"
      "ds_read_b128 v[192:195], v232 offset:8192\n"
      "ds_read_b128 v[196:199], v232 offset:9216\n"
      "ds_read_b128 v[200:203], v232 offset:10240\n"
      "ds_read_b128 v[204:207], v232 offset:11264\n"
      "ds_read_b128 v[208:211], v232 offset:12288\n"
      "ds_read_b128 v[212:215], v232 offset:13312\n"
      "ds_read_b128 v[216:219], v232 offset:14336\n"
      "ds_read_b128 v[220:223], v232 offset:15360\n"
      "s_waitcnt lgkmcnt(12)\n"
      "v_accvgpr_read_b32 v248, a0\n"
      "v_accvgpr_read_b32 v249, a1\n"
      "v_accvgpr_read_b32 v250, a2\n"
      "v_accvgpr_read_b32 v251, a3\n"
      "v_accvgpr_read_b32 v252, a4\n"
      "v_accvgpr_read_b32 v253, a5\n"
      "v_accvgpr_read_b32 v254, a6\n"
      "v_accvgpr_read_b32 v255, a7\n"
      "v_pk_mul_f32 v[160:161], v[160:161], v[228:229] op_sel:[0,0] op_sel_hi:[1,0]\n"
      "v_pk_mul_f32 v[162:163], v[162:163], v[228:229] op_sel:[0,0] op_sel_hi:[1,0]\n"
      "v_pk_mul_f32 v[164:165], v[164:165], v[228:229] op_sel:[0,0] op_sel_hi:[1,0]\n"
      "v_pk_mul_f32 v[166:167], v[166:167], v[228:229] op_sel:[0,0] op_sel_hi:[1,0]\n"
      "v_pk_fma_f32 v[160:161], v[248:249], v[236:237], v[160:161] op_sel:[0,0,0] op_sel_hi:[1,0,1]\n"
      "v_pk_fma_f32 v[162:163], v[250:251], v[236:237], v[162:163] op_sel:[0,0,0] op_sel_hi:[1,0,1]\n"
      "v_pk_fma_f32 v[164:165], v[252:253], v[236:237], v[164:165] op_sel:[0,0,0] op_sel_hi:[1,0,1]\n"
      "v_pk_fma_f32 v[166:167], v[254:255], v[236:237], v[166:167] op_sel:[0,0,0] op_sel_hi:[1,0,1]\n"
      "v_cvt_pk_bf16_f32 v160, v160, v161\n"
      "v_cvt_pk_bf16_f32 v161, v162, v163\n"
      "v_cvt_pk_bf16_f32 v162, v164, v165\n"
      "v_cvt_pk_bf16_f32 v163, v166, v167\n"
      "global_store_dwordx4 v235, v[160:163], s[58:59] offset:-4096 sc1\n"
      "v_accvgpr_read_b32 v248, a8\n"
      "v_accvgpr_read_b32 v249, a9\n"
      "v_accvgpr_read_b32 v250, a10\n"
      "v_accvgpr_read_b32 v251, a11\n"
      "v_accvgpr_read_b32 v252, a12\n"
      "v_accvgpr_read_b32 v253, a13\n"
      "v_accvgpr_read_b32 v254, a14\n"
      "v_accvgpr_read_b32 v255, a15\n"
      "v_pk_mul_f32 v[168:169], v[168:169], v[228:229] op_sel:[0,1] op_sel_hi:[1,1]\n"
      "v_pk_mul_f32 v[170:171], v[170:171], v[228:229] op_sel:[0,1] op_sel_hi:[1,1]\n"
      "v_pk_mul_f32 v[172:173], v[172:173], v[228:229] op_sel:[0,1] op_sel_hi:[1,1]\n"
      "v_pk_mul_f32 v[174:175], v[174:175], v[228:229] op_sel:[0,1] op_sel_hi:[1,1]\n"
      "v_pk_fma_f32 v[168:169], v[248:249], v[236:237], v[168:169] op_sel:[0,1,0] op_sel_hi:[1,1,1]\n"
      "v_pk_fma_f32 v[170:171], v[250:251], v[236:237], v[170:171] op_sel:[0,1,0] op_sel_hi:[1,1,1]\n"
      "v_pk_fma_f32 v[172:173], v[252:253], v[236:237], v[172:173] op_sel:[0,1,0] op_sel_hi:[1,1,1]\n"
      "v_pk_fma_f32 v[174:175], v[254:255], v[236:237], v[174:175] op_sel:[0,1,0] op_sel_hi:[1,1,1]\n"
      "v_cvt_pk_bf16_f32 v168, v168, v169\n"
      "v_cvt_pk_bf16_f32 v169, v170, v171\n"
      "v_cvt_pk_bf16_f32 v170, v172, v173\n"
      "v_cvt_pk_bf16_f32 v171, v174, v175\n"
      "global_store_dwordx4 v235, v[168:171], s[58:59] offset:-3072 sc1\n"
      "s_waitcnt lgkmcnt(8)\n"
      "v_accvgpr_read_b32 v248, a16\n"
      "v_accvgpr_read_b32 v249, a17\n"
      "v_accvgpr_read_b32 v250, a18\n"
      "v_accvgpr_read_b32 v251, a19\n"
      "v_accvgpr_read_b32 v252, a20\n"
      "v_accvgpr_read_b32 v253, a21\n"
      "v_accvgpr_read_b32 v254, a22\n"
      "v_accvgpr_read_b32 v255, a23\n"
      "v_pk_mul_f32 v[176:177], v[176:177], v[230:231] op_sel:[0,0] op_sel_hi:[1,0]\n"
      "v_pk_mul_f32 v[178:179], v[178:179], v[230:231] op_sel:[0,0] op_sel_hi:[1,0]\n"
      "v_pk_mul_f32 v[180:181], v[180:181], v[230:231] op_sel:[0,0] op_sel_hi:[1,0]\n"
      "v_pk_mul_f32 v[182:183], v[182:183], v[230:231] op_sel:[0,0] op_sel_hi:[1,0]\n"
      "v_pk_fma_f32 v[176:177], v[248:249], v[238:239], v[176:177] op_sel:[0,0,0] op_sel_hi:[1,0,1]\n"
      "v_pk_fma_f32 v[178:179], v[250:251], v[238:239], v[178:179] op_sel:[0,0,0] op_sel_hi:[1,0,1]\n"
      "v_pk_fma_f32 v[180:181], v[252:253], v[238:239], v[180:181] op_sel:[0,0,0] op_sel_hi:[1,0,1]\n"
      "v_pk_fma_f32 v[182:183], v[254:255], v[238:239], v[182:183] op_sel:[0,0,0] op_sel_hi:[1,0,1]\n"
      "v_cvt_pk_bf16_f32 v176, v176, v177\n"
      "v_cvt_pk_bf16_f32 v177, v178, v179\n"
      "v_cvt_pk_bf16_f32 v178, v180, v181\n"
      "v_cvt_pk_bf16_f32 v179, v182, v183\n"
      "global_store_dwordx4 v235, v[176:179], s[58:59] offset:-2048 sc1\n"
      "v_accvgpr_read_b32 v248, a24\n"
      "v_accvgpr_read_b32 v249, a25\n"
      "v_accvgpr_read_b32 v250, a26\n"
      "v_accvgpr_read_b32 v251, a27\n"
      "v_accvgpr_read_b32 v252, a28\n"
      "v_accvgpr_read_b32 v253, a29\n"
      "v_accvgpr_read_b32 v254, a30\n"
      "v_accvgpr_read_b32 v255, a31\n"
      "v_pk_mul_f32 v[184:185], v[184:185], v[230:231] op_sel:[0,1] op_sel_hi:[1,1]\n"
      "v_pk_mul_f32 v[186:187], v[186:187], v[230:231] op_sel:[0,1] op_sel_hi:[1,1]\n"
      "v_pk_mul_f32 v[188:189], v[188:189], v[230:231] op_sel:[0,1] op_sel_hi:[1,1]\n"
      "v_pk_mul_f32 v[190:191], v[190:191], v[230:231] op_sel:[0,1] op_sel_hi:[1,1]\n"
      "v_pk_fma_f32 v[184:185], v[248:249], v[238:239], v[184:185] op_sel:[0,1,0] op_sel_hi:[1,1,1]\n"
      "v_pk_fma_f32 v[186:187], v[250:251], v[238:239], v[186:187] op_sel:[0,1,0] op_sel_hi:[1,1,1]\n"
      "v_pk_fma_f32 v[188:189], v[252:253], v[238:239], v[188:189] op_sel:[0,1,0] op_sel_hi:[1,1,1]\n"
      "v_pk_fma_f32 v[190:191], v[254:255], v[238:239], v[190:191] op_sel:[0,1,0] op_sel_hi:[1,1,1]\n"
      "v_cvt_pk_bf16_f32 v184, v184, v185\n"
      "v_cvt_pk_bf16_f32 v185, v186, v187\n"
      "v_cvt_pk_bf16_f32 v186, v188, v189\n"
      "v_cvt_pk_bf16_f32 v187, v190, v191\n"
      "global_store_dwordx4 v235, v[184:187], s[58:59] offset:-1024 sc1\n"
      "s_waitcnt lgkmcnt(4)\n"
      "v_accvgpr_read_b32 v248, a32\n"
      "v_accvgpr_read_b32 v249, a33\n"
      "v_accvgpr_read_b32 v250, a34\n"
      "v_accvgpr_read_b32 v251, a35\n"
      "v_accvgpr_read_b32 v252, a36\n"
      "v_accvgpr_read_b32 v253, a37\n"
      "v_accvgpr_read_b32 v254, a38\n"
      "v_accvgpr_read_b32 v255, a39\n"
      "v_pk_mul_f32 v[192:193], v[192:193], v[228:229] op_sel:[0,0] op_sel_hi:[1,0]\n"
      "v_pk_mul_f32 v[194:195], v[194:195], v[228:229] op_sel:[0,0] op_sel_hi:[1,0]\n"
      "v_pk_mul_f32 v[196:197], v[196:197], v[228:229] op_sel:[0,0] op_sel_hi:[1,0]\n"
      "v_pk_mul_f32 v[198:199], v[198:199], v[228:229] op_sel:[0,0] op_sel_hi:[1,0]\n"
      "v_pk_fma_f32 v[192:193], v[248:249], v[236:237], v[192:193] op_sel:[0,0,0] op_sel_hi:[1,0,1]\n"
      "v_pk_fma_f32 v[194:195], v[250:251], v[236:237], v[194:195] op_sel:[0,0,0] op_sel_hi:[1,0,1]\n"
      "v_pk_fma_f32 v[196:197], v[252:253], v[236:237], v[196:197] op_sel:[0,0,0] op_sel_hi:[1,0,1]\n"
      "v_pk_fma_f32 v[198:199], v[254:255], v[236:237], v[198:199] op_sel:[0,0,0] op_sel_hi:[1,0,1]\n"
      "v_cvt_pk_bf16_f32 v192, v192, v193\n"
      "v_cvt_pk_bf16_f32 v193, v194, v195\n"
      "v_cvt_pk_bf16_f32 v194, v196, v197\n"
      "v_cvt_pk_bf16_f32 v195, v198, v199\n"
      "global_store_dwordx4 v235, v[192:195], s[58:59] offset:0 sc1\n"
      "v_accvgpr_read_b32 v248, a40\n"
      "v_accvgpr_read_b32 v249, a41\n"
      "v_accvgpr_read_b32 v250, a42\n"
      "v_accvgpr_read_b32 v251, a43\n"
      "v_accvgpr_read_b32 v252, a44\n"
      "v_accvgpr_read_b32 v253, a45\n"
      "v_accvgpr_read_b32 v254, a46\n"
      "v_accvgpr_read_b32 v255, a47\n"
      "v_pk_mul_f32 v[200:201], v[200:201], v[228:229] op_sel:[0,1] op_sel_hi:[1,1]\n"
      "v_pk_mul_f32 v[202:203], v[202:203], v[228:229] op_sel:[0,1] op_sel_hi:[1,1]\n"
      "v_pk_mul_f32 v[204:205], v[204:205], v[228:229] op_sel:[0,1] op_sel_hi:[1,1]\n"
      "v_pk_mul_f32 v[206:207], v[206:207], v[228:229] op_sel:[0,1] op_sel_hi:[1,1]\n"
      "v_pk_fma_f32 v[200:201], v[248:249], v[236:237], v[200:201] op_sel:[0,1,0] op_sel_hi:[1,1,1]\n"
      "v_pk_fma_f32 v[202:203], v[250:251], v[236:237], v[202:203] op_sel:[0,1,0] op_sel_hi:[1,1,1]\n"
      "v_pk_fma_f32 v[204:205], v[252:253], v[236:237], v[204:205] op_sel:[0,1,0] op_sel_hi:[1,1,1]\n"
      "v_pk_fma_f32 v[206:207], v[254:255], v[236:237], v[206:207] op_sel:[0,1,0] op_sel_hi:[1,1,1]\n"
      "v_cvt_pk_bf16_f32 v200, v200, v201\n"
      "v_cvt_pk_bf16_f32 v201, v202, v203\n"
      "v_cvt_pk_bf16_f32 v202, v204, v205\n"
      "v_cvt_pk_bf16_f32 v203, v206, v207\n"
      "global_store_dwordx4 v235, v[200:203], s[58:59] offset:1024 sc1\n"
      "s_waitcnt lgkmcnt(0)\n"
      "v_accvgpr_read_b32 v248, a48\n"
      "v_accvgpr_read_b32 v249, a49\n"
      "v_accvgpr_read_b32 v250, a50\n"
      "v_accvgpr_read_b32 v251, a51\n"
      "v_accvgpr_read_b32 v252, a52\n"
      "v_accvgpr_read_b32 v253, a53\n"
      "v_accvgpr_read_b32 v254, a54\n"
      "v_accvgpr_read_b32 v255, a55\n"
      "v_pk_mul_f32 v[208:209], v[208:209], v[230:231] op_sel:[0,0] op_sel_hi:[1,0]\n"
      "v_pk_mul_f32 v[210:211], v[210:211], v[230:231] op_sel:[0,0] op_sel_hi:[1,0]\n"
      "v_pk_mul_f32 v[212:213], v[212:213], v[230:231] op_sel:[0,0] op_sel_hi:[1,0]\n"
      "v_pk_mul_f32 v[214:215], v[214:215], v[230:231] op_sel:[0,0] op_sel_hi:[1,0]\n"
      "v_pk_fma_f32 v[208:209], v[248:249], v[238:239], v[208:209] op_sel:[0,0,0] op_sel_hi:[1,0,1]\n"
      "v_pk_fma_f32 v[210:211], v[250:251], v[238:239], v[210:211] op_sel:[0,0,0] op_sel_hi:[1,0,1]\n"
      "v_pk_fma_f32 v[212:213], v[252:253], v[238:239], v[212:213] op_sel:[0,0,0] op_sel_hi:[1,0,1]\n"
      "v_pk_fma_f32 v[214:215], v[254:255], v[238:239], v[214:215] op_sel:[0,0,0] op_sel_hi:[1,0,1]\n"
      "v_cvt_pk_bf16_f32 v208, v208, v209\n"
      "v_cvt_pk_bf16_f32 v209, v210, v211\n"
      "v_cvt_pk_bf16_f32 v210, v212, v213\n"
      "v_cvt_pk_bf16_f32 v211, v214, v215\n"
      "global_store_dwordx4 v235, v[208:211], s[58:59] offset:2048 sc1\n"
      "v_accvgpr_read_b32 v248, a56\n"
      "v_accvgpr_read_b32 v249, a57\n"
      "v_accvgpr_read_b32 v250, a58\n"
      "v_accvgpr_read_b32 v251, a59\n"
      "v_accvgpr_read_b32 v252, a60\n"
      "v_accvgpr_read_b32 v253, a61\n"
      "v_accvgpr_read_b32 v254, a62\n"
      "v_accvgpr_read_b32 v255, a63\n"
      "v_pk_mul_f32 v[216:217], v[216:217], v[230:231] op_sel:[0,1] op_sel_hi:[1,1]\n"
      "v_pk_mul_f32 v[218:219], v[218:219], v[230:231] op_sel:[0,1] op_sel_hi:[1,1]\n"
      "v_pk_mul_f32 v[220:221], v[220:221], v[230:231] op_sel:[0,1] op_sel_hi:[1,1]\n"
      "v_pk_mul_f32 v[222:223], v[222:223], v[230:231] op_sel:[0,1] op_sel_hi:[1,1]\n"
      "v_pk_fma_f32 v[216:217], v[248:249], v[238:239], v[216:217] op_sel:[0,1,0] op_sel_hi:[1,1,1]\n"
      "v_pk_fma_f32 v[218:219], v[250:251], v[238:239], v[218:219] op_sel:[0,1,0] op_sel_hi:[1,1,1]\n"
      "v_pk_fma_f32 v[220:221], v[252:253], v[238:239], v[220:221] op_sel:[0,1,0] op_sel_hi:[1,1,1]\n"
      "v_pk_fma_f32 v[222:223], v[254:255], v[238:239], v[222:223] op_sel:[0,1,0] op_sel_hi:[1,1,1]\n"
      "v_cvt_pk_bf16_f32 v216, v216, v217\n"
      "v_cvt_pk_bf16_f32 v217, v218, v219\n"
      "v_cvt_pk_bf16_f32 v218, v220, v221\n"
      "v_cvt_pk_bf16_f32 v219, v222, v223\n"
      "global_store_dwordx4 v235, v[216:219], s[58:59] offset:3072 sc1\n"
      "s_branch L_epiend_%=\n"
      "L_epi1_%=:\n"
      "ds_write_b128 v232, a[0:3] offset:0\n"
      "ds_write_b128 v232, a[4:7] offset:1024\n"
      "ds_write_b128 v232, a[8:11] offset:2048\n"
      "ds_write_b128 v232, a[12:15] offset:3072\n"
      "ds_write_b128 v232, a[16:19] offset:4096\n"
      "ds_write_b128 v232, a[20:23] offset:5120\n"
      "ds_write_b128 v232, a[24:27] offset:6144\n"
      "ds_write_b128 v232, a[28:31] offset:7168\n"
      "ds_write_b128 v232, a[32:35] offset:8192\n"
      "ds_write_b128 v232, a[36:39] offset:9216\n"
      "ds_write_b128 v232, a[40:43] offset:10240\n"
      "ds_write_b128 v232, a[44:47] offset:11264\n"
      "ds_write_b128 v232, a[48:51] offset:12288\n"
      "ds_write_b128 v232, a[52:55] offset:13312\n"
      "ds_write_b128 v232, a[56:59] offset:14336\n"
      "ds_write_b128 v232, a[60:63] offset:15360\n"
      "s_waitcnt lgkmcnt(0)\n"
      "s_barrier\n"
      "ds_read_b128 v[160:163], v233 offset:0\n"
      "ds_read_b128 v[164:167], v233 offset:1024\n"
      "ds_read_b128 v[168:171], v233 offset:2048\n"
      "ds_read_b128 v[172:175], v233 offset:3072\n"
      "ds_read_b128 v[176:179], v233 offset:4096\n"
      "ds_read_b128 v[180:183], v233 offset:5120\n"
      "ds_read_b128 v[184:187], v233 offset:6144\n"
      "ds_read_b128 v[188:191], v233 offset:7168\n"
      "ds_read_b128 v[192:195], v233 offset:8192\n"
      "ds_read_b128 v[196:199], v233 offset:9216\n"
      "ds_read_b128 v[200:203], v233 offset:10240\n"
      "ds_read_b128 v[204:207], v233 offset:11264\n"
      "ds_read_b128 v[208:211], v233 offset:12288\n"
      "ds_read_b128 v[212:215], v233 offset:13312\n"
      "ds_read_b128 v[216:219], v233 offset:14336\n"
      "ds_read_b128 v[220:223], v233 offset:15360\n"
      "s_waitcnt lgkmcnt(12)\n"
      "v_accvgpr_read_b32 v248, a64\n"
      "v_accvgpr_read_b32 v249, a65\n"
      "v_accvgpr_read_b32 v250, a66\n"
      "v_accvgpr_read_b32 v251, a67\n"
      "v_accvgpr_read_b32 v252, a68\n"
      "v_accvgpr_read_b32 v253, a69\n"
      "v_accvgpr_read_b32 v254, a70\n"
      "v_accvgpr_read_b32 v255, a71\n"
      "v_pk_mul_f32 v[160:161], v[160:161], v[228:229] op_sel:[0,0] op_sel_hi:[1,0]\n"
      "v_pk_mul_f32 v[162:163], v[162:163], v[228:229] op_sel:[0,0] op_sel_hi:[1,0]\n"
      "v_pk_mul_f32 v[164:165], v[164:165], v[228:229] op_sel:[0,0] op_sel_hi:[1,0]\n"
      "v_pk_mul_f32 v[166:167], v[166:167], v[228:229] op_sel:[0,0] op_sel_hi:[1,0]\n"
      "v_pk_fma_f32 v[160:161], v[248:249], v[236:237], v[160:161] op_sel:[0,0,0] op_sel_hi:[1,0,1]\n"
      "v_pk_fma_f32 v[162:163], v[250:251], v[236:237], v[162:163] op_sel:[0,0,0] op_sel_hi:[1,0,1]\n"
      "v_pk_fma_f32 v[164:165], v[252:253], v[236:237], v[164:165] op_sel:[0,0,0] op_sel_hi:[1,0,1]\n"
      "v_pk_fma_f32 v[166:167], v[254:255], v[236:237], v[166:167] op_sel:[0,0,0] op_sel_hi:[1,0,1]\n"
      "v_cvt_pk_bf16_f32 v160, v160, v161\n"
      "v_cvt_pk_bf16_f32 v161, v162, v163\n"
      "v_cvt_pk_bf16_f32 v162, v164, v165\n"
      "v_cvt_pk_bf16_f32 v163, v166, v167\n"
      "global_store_dwordx4 v235, v[160:163], s[58:59] offset:-4096 sc1\n"
      "v_accvgpr_read_b32 v248, a72\n"
      "v_accvgpr_read_b32 v249, a73\n"
      "v_accvgpr_read_b32 v250, a74\n"
      "v_accvgpr_read_b32 v251, a75\n"
      "v_accvgpr_read_b32 v252, a76\n"
      "v_accvgpr_read_b32 v253, a77\n"
      "v_accvgpr_read_b32 v254, a78\n"
      "v_accvgpr_read_b32 v255, a79\n"
      "v_pk_mul_f32 v[168:169], v[168:169], v[228:229] op_sel:[0,1] op_sel_hi:[1,1]\n"
      "v_pk_mul_f32 v[170:171], v[170:171], v[228:229] op_sel:[0,1] op_sel_hi:[1,1]\n"
      "v_pk_mul_f32 v[172:173], v[172:173], v[228:229] op_sel:[0,1] op_sel_hi:[1,1]\n"
      "v_pk_mul_f32 v[174:175], v[174:175], v[228:229] op_sel:[0,1] op_sel_hi:[1,1]\n"
      "v_pk_fma_f32 v[168:169], v[248:249], v[236:237], v[168:169] op_sel:[0,1,0] op_sel_hi:[1,1,1]\n"
      "v_pk_fma_f32 v[170:171], v[250:251], v[236:237], v[170:171] op_sel:[0,1,0] op_sel_hi:[1,1,1]\n"
      "v_pk_fma_f32 v[172:173], v[252:253], v[236:237], v[172:173] op_sel:[0,1,0] op_sel_hi:[1,1,1]\n"
      "v_pk_fma_f32 v[174:175], v[254:255], v[236:237], v[174:175] op_sel:[0,1,0] op_sel_hi:[1,1,1]\n"
      "v_cvt_pk_bf16_f32 v168, v168, v169\n"
      "v_cvt_pk_bf16_f32 v169, v170, v171\n"
      "v_cvt_pk_bf16_f32 v170, v172, v173\n"
      "v_cvt_pk_bf16_f32 v171, v174, v175\n"
      "global_store_dwordx4 v235, v[168:171], s[58:59] offset:-3072 sc1\n"
      "s_waitcnt lgkmcnt(8)\n"
      "v_accvgpr_read_b32 v248, a80\n"
      "v_accvgpr_read_b32 v249, a81\n"
      "v_accvgpr_read_b32 v250, a82\n"
      "v_accvgpr_read_b32 v251, a83\n"
      "v_accvgpr_read_b32 v252, a84\n"
      "v_accvgpr_read_b32 v253, a85\n"
      "v_accvgpr_read_b32 v254, a86\n"
      "v_accvgpr_read_b32 v255, a87\n"
      "v_pk_mul_f32 v[176:177], v[176:177], v[230:231] op_sel:[0,0] op_sel_hi:[1,0]\n"
      "v_pk_mul_f32 v[178:179], v[178:179], v[230:231] op_sel:[0,0] op_sel_hi:[1,0]\n"
      "v_pk_mul_f32 v[180:181], v[180:181], v[230:231] op_sel:[0,0] op_sel_hi:[1,0]\n"
      "v_pk_mul_f32 v[182:183], v[182:183], v[230:231] op_sel:[0,0] op_sel_hi:[1,0]\n"
      "v_pk_fma_f32 v[176:177], v[248:249], v[238:239], v[176:177] op_sel:[0,0,0] op_sel_hi:[1,0,1]\n"
      "v_pk_fma_f32 v[178:179], v[250:251], v[238:239], v[178:179] op_sel:[0,0,0] op_sel_hi:[1,0,1]\n"
      "v_pk_fma_f32 v[180:181], v[252:253], v[238:239], v[180:181] op_sel:[0,0,0] op_sel_hi:[1,0,1]\n"
      "v_pk_fma_f32 v[182:183], v[254:255], v[238:239], v[182:183] op_sel:[0,0,0] op_sel_hi:[1,0,1]\n"
      "v_cvt_pk_bf16_f32 v176, v176, v177\n"
      "v_cvt_pk_bf16_f32 v177, v178, v179\n"
      "v_cvt_pk_bf16_f32 v178, v180, v181\n"
      "v_cvt_pk_bf16_f32 v179, v182, v183\n"
      "global_store_dwordx4 v235, v[176:179], s[58:59] offset:-2048 sc1\n"
      "v_accvgpr_read_b32 v248, a88\n"
      "v_accvgpr_read_b32 v249, a89\n"
      "v_accvgpr_read_b32 v250, a90\n"
      "v_accvgpr_read_b32 v251, a91\n"
      "v_accvgpr_read_b32 v252, a92\n"
      "v_accvgpr_read_b32 v253, a93\n"
      "v_accvgpr_read_b32 v254, a94\n"
      "v_accvgpr_read_b32 v255, a95\n"
      "v_pk_mul_f32 v[184:185], v[184:185], v[230:231] op_sel:[0,1] op_sel_hi:[1,1]\n"
      "v_pk_mul_f32 v[186:187], v[186:187], v[230:231] op_sel:[0,1] op_sel_hi:[1,1]\n"
      "v_pk_mul_f32 v[188:189], v[188:189], v[230:231] op_sel:[0,1] op_sel_hi:[1,1]\n"
      "v_pk_mul_f32 v[190:191], v[190:191], v[230:231] op_sel:[0,1] op_sel_hi:[1,1]\n"
      "v_pk_fma_f32 v[184:185], v[248:249], v[238:239], v[184:185] op_sel:[0,1,0] op_sel_hi:[1,1,1]\n"
      "v_pk_fma_f32 v[186:187], v[250:251], v[238:239], v[186:187] op_sel:[0,1,0] op_sel_hi:[1,1,1]\n"
      "v_pk_fma_f32 v[188:189], v[252:253], v[238:239], v[188:189] op_sel:[0,1,0] op_sel_hi:[1,1,1]\n"
      "v_pk_fma_f32 v[190:191], v[254:255], v[238:239], v[190:191] op_sel:[0,1,0] op_sel_hi:[1,1,1]\n"
      "v_cvt_pk_bf16_f32 v184, v184, v185\n"
      "v_cvt_pk_bf16_f32 v185, v186, v187\n"
      "v_cvt_pk_bf16_f32 v186, v188, v189\n"
      "v_cvt_pk_bf16_f32 v187, v190, v191\n"
      "global_store_dwordx4 v235, v[184:187], s[58:59] offset:-1024 sc1\n"
      "s_waitcnt lgkmcnt(4)\n"
      "v_accvgpr_read_b32 v248, a96\n"
      "v_accvgpr_read_b32 v249, a97\n"
      "v_accvgpr_read_b32 v250, a98\n"
      "v_accvgpr_read_b32 v251, a99\n"
      "v_accvgpr_read_b32 v252, a100\n"
      "v_accvgpr_read_b32 v253, a101\n"
      "v_accvgpr_read_b32 v254, a102\n"
      "v_accvgpr_read_b32 v255, a103\n"
      "v_pk_mul_f32 v[192:193], v[192:193], v[228:229] op_sel:[0,0] op_sel_hi:[1,0]\n"
      "v_pk_mul_f32 v[194:195], v[194:195], v[228:229] op_sel:[0,0] op_sel_hi:[1,0]\n"
      "v_pk_mul_f32 v[196:197], v[196:197], v[228:229] op_sel:[0,0] op_sel_hi:[1,0]\n"
      "v_pk_mul_f32 v[198:199], v[198:199], v[228:229] op_sel:[0,0] op_sel_hi:[1,0]\n"
      "v_pk_fma_f32 v[192:193], v[248:249], v[236:237], v[192:193] op_sel:[0,0,0] op_sel_hi:[1,0,1]\n"
      "v_pk_fma_f32 v[194:195], v[250:251], v[236:237], v[194:195] op_sel:[0,0,0] op_sel_hi:[1,0,1]\n"
      "v_pk_fma_f32 v[196:197], v[252:253], v[236:237], v[196:197] op_sel:[0,0,0] op_sel_hi:[1,0,1]\n"
      "v_pk_fma_f32 v[198:199], v[254:255], v[236:237], v[198:199] op_sel:[0,0,0] op_sel_hi:[1,0,1]\n"
      "v_cvt_pk_bf16_f32 v192, v192, v193\n"
      "v_cvt_pk_bf16_f32 v193, v194, v195\n"
      "v_cvt_pk_bf16_f32 v194, v196, v197\n"
      "v_cvt_pk_bf16_f32 v195, v198, v199\n"
      "global_store_dwordx4 v235, v[192:195], s[58:59] offset:0 sc1\n"
      "v_accvgpr_read_b32 v248, a104\n"
      "v_accvgpr_read_b32 v249, a105\n"
      "v_accvgpr_read_b32 v250, a106\n"
      "v_accvgpr_read_b32 v251, a107\n"
      "v_accvgpr_read_b32 v252, a108\n"
      "v_accvgpr_read_b32 v253, a109\n"
      "v_accvgpr_read_b32 v254, a110\n"
      "v_accvgpr_read_b32 v255, a111\n"
      "v_pk_mul_f32 v[200:201], v[200:201], v[228:229] op_sel:[0,1] op_sel_hi:[1,1]\n"
      "v_pk_mul_f32 v[202:203], v[202:203], v[228:229] op_sel:[0,1] op_sel_hi:[1,1]\n"
      "v_pk_mul_f32 v[204:205], v[204:205], v[228:229] op_sel:[0,1] op_sel_hi:[1,1]\n"
      "v_pk_mul_f32 v[206:207], v[206:207], v[228:229] op_sel:[0,1] op_sel_hi:[1,1]\n"
      "v_pk_fma_f32 v[200:201], v[248:249], v[236:237], v[200:201] op_sel:[0,1,0] op_sel_hi:[1,1,1]\n"
      "v_pk_fma_f32 v[202:203], v[250:251], v[236:237], v[202:203] op_sel:[0,1,0] op_sel_hi:[1,1,1]\n"
      "v_pk_fma_f32 v[204:205], v[252:253], v[236:237], v[204:205] op_sel:[0,1,0] op_sel_hi:[1,1,1]\n"
      "v_pk_fma_f32 v[206:207], v[254:255], v[236:237], v[206:207] op_sel:[0,1,0] op_sel_hi:[1,1,1]\n"
      "v_cvt_pk_bf16_f32 v200, v200, v201\n"
      "v_cvt_pk_bf16_f32 v201, v202, v203\n"
      "v_cvt_pk_bf16_f32 v202, v204, v205\n"
      "v_cvt_pk_bf16_f32 v203, v206, v207\n"
      "global_store_dwordx4 v235, v[200:203], s[58:59] offset:1024 sc1\n"
      "s_waitcnt lgkmcnt(0)\n"
      "v_accvgpr_read_b32 v248, a112\n"
      "v_accvgpr_read_b32 v249, a113\n"
      "v_accvgpr_read_b32 v250, a114\n"
      "v_accvgpr_read_b32 v251, a115\n"
      "v_accvgpr_read_b32 v252, a116\n"
      "v_accvgpr_read_b32 v253, a117\n"
      "v_accvgpr_read_b32 v254, a118\n"
      "v_accvgpr_read_b32 v255, a119\n"
      "v_pk_mul_f32 v[208:209], v[208:209], v[230:231] op_sel:[0,0] op_sel_hi:[1,0]\n"
      "v_pk_mul_f32 v[210:211], v[210:211], v[230:231] op_sel:[0,0] op_sel_hi:[1,0]\n"
      "v_pk_mul_f32 v[212:213], v[212:213], v[230:231] op_sel:[0,0] op_sel_hi:[1,0]\n"
      "v_pk_mul_f32 v[214:215], v[214:215], v[230:231] op_sel:[0,0] op_sel_hi:[1,0]\n"
      "v_pk_fma_f32 v[208:209], v[248:249], v[238:239], v[208:209] op_sel:[0,0,0] op_sel_hi:[1,0,1]\n"
      "v_pk_fma_f32 v[210:211], v[250:251], v[238:239], v[210:211] op_sel:[0,0,0] op_sel_hi:[1,0,1]\n"
      "v_pk_fma_f32 v[212:213], v[252:253], v[238:239], v[212:213] op_sel:[0,0,0] op_sel_hi:[1,0,1]\n"
      "v_pk_fma_f32 v[214:215], v[254:255], v[238:239], v[214:215] op_sel:[0,0,0] op_sel_hi:[1,0,1]\n"
      "v_cvt_pk_bf16_f32 v208, v208, v209\n"
      "v_cvt_pk_bf16_f32 v209, v210, v211\n"
      "v_cvt_pk_bf16_f32 v210, v212, v213\n"
      "v_cvt_pk_bf16_f32 v211, v214, v215\n"
      "global_store_dwordx4 v235, v[208:211], s[58:59] offset:2048 sc1\n"
      "v_accvgpr_read_b32 v248, a120\n"
      "v_accvgpr_read_b32 v249, a121\n"
      "v_accvgpr_read_b32 v250, a122\n"
      "v_accvgpr_read_b32 v251, a123\n"
      "v_accvgpr_read_b32 v252, a124\n"
      "v_accvgpr_read_b32 v253, a125\n"
      "v_accvgpr_read_b32 v254, a126\n"
      "v_accvgpr_read_b32 v255, a127\n"
      "v_pk_mul_f32 v[216:217], v[216:217], v[230:231] op_sel:[0,1] op_sel_hi:[1,1]\n"
      "v_pk_mul_f32 v[218:219], v[218:219], v[230:231] op_sel:[0,1] op_sel_hi:[1,1]\n"
      "v_pk_mul_f32 v[220:221], v[220:221], v[230:231] op_sel:[0,1] op_sel_hi:[1,1]\n"
      "v_pk_mul_f32 v[222:223], v[222:223], v[230:231] op_sel:[0,1] op_sel_hi:[1,1]\n"
      "v_pk_fma_f32 v[216:217], v[248:249], v[238:239], v[216:217] op_sel:[0,1,0] op_sel_hi:[1,1,1]\n"
      "v_pk_fma_f32 v[218:219], v[250:251], v[238:239], v[218:219] op_sel:[0,1,0] op_sel_hi:[1,1,1]\n"
      "v_pk_fma_f32 v[220:221], v[252:253], v[238:239], v[220:221] op_sel:[0,1,0] op_sel_hi:[1,1,1]\n"
      "v_pk_fma_f32 v[222:223], v[254:255], v[238:239], v[222:223] op_sel:[0,1,0] op_sel_hi:[1,1,1]\n"
      "v_cvt_pk_bf16_f32 v216, v216, v217\n"
      "v_cvt_pk_bf16_f32 v217, v218, v219\n"
      "v_cvt_pk_bf16_f32 v218, v220, v221\n"
      "v_cvt_pk_bf16_f32 v219, v222, v223\n"
      "global_store_dwordx4 v235, v[216:219], s[58:59] offset:3072 sc1\n"
      "L_epiend_%=:\n"
      "s_nop 1\n"
      "L_h3_%=:\n"
      "v_mov_b32 v248, 0\n"
      "v_mov_b32 v249, 0\n"
      "v_mov_b32 v250, 0\n"
      "v_mov_b32 v251, 0\n"
      "s_nop 1\n"
      "v_mfma_f32_32x32x16_bf16 a[0:15], v[248:251], v[248:251], 0\n"
      "v_mov_b32 v144, 0\n"
      "v_mov_b32 v145, 0\n"
      "v_mfma_f32_32x32x16_bf16 a[16:31], v[248:251], v[248:251], 0\n"
      "v_mov_b32 v146, 0\n"
      "v_mov_b32 v147, 0\n"
      "v_mfma_f32_32x32x16_bf16 a[32:47], v[248:251], v[248:251], 0\n"
      "v_mov_b32 v148, 0\n"
      "v_mov_b32 v149, 0\n"
      "v_mfma_f32_32x32x16_bf16 a[48:63], v[248:251], v[248:251], 0\n"
      "v_mov_b32 v150, 0\n"
      "v_mov_b32 v151, 0\n"
      "v_mfma_f32_32x32x16_bf16 a[64:79], v[248:251], v[248:251], 0\n"
      "v_mov_b32 v152, 0\n"
      "v_mov_b32 v153, 0\n"
      "v_mfma_f32_32x32x16_bf16 a[80:95], v[248:251], v[248:251], 0\n"
      "v_mov_b32 v154, 0\n"
      "v_mov_b32 v155, 0\n"
      "v_mfma_f32_32x32x16_bf16 a[96:111], v[248:251], v[248:251], 0\n"
      "v_mov_b32 v156, 0\n"
      "v_mov_b32 v157, 0\n"
      "v_mfma_f32_32x32x16_bf16 a[112:127], v[248:251], v[248:251], 0\n"
      "v_mov_b32 v158, 0\n"
      "v_mov_b32 v159, 0\n"
      "L_h4_%=:\n"
      "s_cmp_eq_u32 s40, s79\n"
      "s_cbranch_scc0 L_h6_%=\n"
      "s_and_b32 s53, %20, 31\n"
      "s_lshl_b32 s53, s53, 2\n"
      "v_bfe_u32 v161, v241, 4, 3\n"
      "v_lshl_add_u32 v161, v161, 7, s53\n"
      "global_load_dword v243, v161, %15 sc1\n"
      "L_h6_%=:\n"
      "s_sub_u32 s53, s40, 2\n"
      "s_cmp_eq_u32 s53, s79\n"
      "s_cbranch_scc0 L_h7_%=\n"
      "s_and_b32 s53, %20, 31\n"
      "s_lshl_b32 s53, s53, 2\n"
      "v_bfe_u32 v161, v241, 4, 3\n"
      "v_lshl_add_u32 v161, v161, 7, s53\n"
      "s_lshl_b32 s53, %14, 2\n"
      "s_add_u32 s53, s53, 4096\n"
      "v_lshl_add_u32 v162, v161, 6, s53\n"
      "s_mov_b32 exec_lo, 0xff\n"
      "s_mov_b32 exec_hi, 0\n"
      "global_store_dword v162, v243, %15 sc1\n"
      "s_mov_b64 exec, -1\n"
      "L_h7_%=:\n"
      "s_bitcmp1_b32 0x14, s40\n"
      "s_cbranch_scc0 L_h5_%=\n"
      "s_waitcnt lgkmcnt(0)\n"
      "s_barrier\n"
      "v_bfe_u32 v165, v241, 4, 6\n"
      "s_lshr_b32 s53, s40, 2\n"
      "s_mul_i32 s53, s53, 160\n"
      "v_add_u32 v166, 0, v165\n"
      "v_mul_u32_u24 v167, 52429, v166\n"
      "v_lshrrev_b32 v167, 19, v167\n"
      "v_mul_u32_u24 v160, %19, v167\n"
      "v_mul_u32_u24 v167, 160, v167\n"
      "v_lshlrev_b32 v166, 4, v166\n"
      "v_sub_u32 v166, v166, v167\n"
      "v_add3_u32 v160, v160, v166, s53\n"
      "s_add_u32 m0, s49, 32768\n"
      "s_nop 0\n"
      "global_load_lds_dwordx4 v160, %16\n"
      "v_add_u32 v166, 64, v165\n"
      "v_mul_u32_u24 v167, 52429, v166\n"
      "v_lshrrev_b32 v167, 19, v167\n"
      "v_mul_u32_u24 v161, %19, v167\n"
      "v_mul_u32_u24 v167, 160, v167\n"
      "v_lshlrev_b32 v166, 4, v166\n"
      "v_sub_u32 v166, v166, v167\n"
      "v_add3_u32 v161, v161, v166, s53\n"
      "s_add_u32 m0, s49, 33792\n"
      "s_nop 0\n"
      "global_load_lds_dwordx4 v161, %16\n"
      "v_add_u32 v166, 128, v165\n"
      "v_mul_u32_u24 v167, 52429, v166\n"
      "v_lshrrev_b32 v167, 19, v167\n"
      "v_mul_u32_u24 v162, %19, v167\n"
      "v_mul_u32_u24 v167, 160, v167\n"
      "v_lshlrev_b32 v166, 4, v166\n"
      "v_sub_u32 v166, v166, v167\n"
      "v_add3_u32 v162, v162, v166, s53\n"
      "s_add_u32 m0, s49, 34816\n"
      "s_nop 0\n"
      "global_load_lds_dwordx4 v162, %16\n"
      "v_add_u32 v166, 192, v165\n"
      "v_mul_u32_u24 v167, 52429, v166\n"
      "v_lshrrev_b32 v167, 19, v167\n"
      "v_mul_u32_u24 v163, %19, v167\n"
      "v_mul_u32_u24 v167, 160, v167\n"
      "v_lshlrev_b32 v166, 4, v166\n"
      "v_sub_u32 v166, v166, v167\n"
      "v_add3_u32 v163, v163, v166, s53\n"
      "s_add_u32 m0, s49, 35840\n"
      "s_nop 0\n"
      "global_load_lds_dwordx4 v163, %16\n"
      "v_add_u32 v166, 256, v165\n"
      "v_mul_u32_u24 v167, 52429, v166\n"
      "v_lshrrev_b32 v167, 19, v167\n"
      "v_mul_u32_u24 v164, %19, v167\n"
      "v_mul_u32_u24 v167, 160, v167\n"
      "v_lshlrev_b32 v166, 4, v166\n"
      "v_sub_u32 v166, v166, v167\n"
      "v_add3_u32 v164, v164, v166, s53\n"
      "s_add_u32 m0, s49, 36864\n"
      "s_nop 0\n"
      "global_load_lds_dwordx4 v164, %16\n"
      "L_h5_%=:\n"
      "s_setpc_b64 s[56:57]\n"
      "L_rt2_%=:\n"
      "v_bfe_u32 v248, v241, 6, 4\n"
      "v_mul_u32_u24 v248, 160, v248\n"
      "v_bfe_u32 v249, v241, 4, 2\n"
      "v_lshl_add_u32 v248, v249, 3, v248\n"
      "s_add_u32 s53, s49, 32768\n"
      "v_add_u32 v248, s53, v248\n"
      "v_and_b32 v250, 0x3f0, v241\n"
      "ds_read_b64_tr_b16 a[192:193], v248 offset:0\n"
      "ds_read_b64_tr_b16 a[194:195], v248 offset:2560\n"
      "ds_read_b64_tr_b16 a[196:197], v248 offset:32\n"
      "ds_read_b64_tr_b16 a[198:199], v248 offset:2592\n"
      "ds_read_b64_tr_b16 a[200:201], v248 offset:64\n"
      "ds_read_b64_tr_b16 a[202:203], v248 offset:2624\n"
      "ds_read_b64_tr_b16 a[204:205], v248 offset:96\n"
      "ds_read_b64_tr_b16 a[206:207], v248 offset:2656\n"
      "ds_read_b64_tr_b16 a[208:209], v248 offset:128\n"
      "ds_read_b64_tr_b16 a[210:211], v248 offset:2688\n"
      "s_cmp_eq_u32 s40, 3\n"
      "s_cselect_b64 s[60:61], %17, %18\n"
      "s_waitcnt lgkmcnt(0)\n"
      "global_store_dwordx4 v250, a[192:195], s[60:61] offset:-2048 sc1\n"
      "global_store_dwordx4 v250, a[196:199], s[60:61] offset:-1024 sc1\n"
      "global_store_dwordx4 v250, a[200:203], s[60:61] offset:0 sc1\n"
      "global_store_dwordx4 v250, a[204:207], s[60:61] offset:1024 sc1\n"
      "global_store_dwordx4 v250, a[208:211], s[60:61] offset:2048 sc1\n"
      "s_nop 1\n"
      "s_setpc_b64 s[56:57]\n"
      "L_end_%=:\n"
      "s_mov_b32 m0, s50\n"
      : "+{s[56:57]}"(p_ckv0), "+{s[58:59]}"(p_ckv1), "+{s[62:63]}"(p_wuk), "+{s[64:65]}"(p_wuv), "+{s[66:67]}"(p_x0), "+{s[68:69]}"(p_x1),
        "+{s[70:71]}"(p_wq), "+{s[72:73]}"(kimg), "+{s[74:75]}"(vimg)
      : "s"(c1), "s"(c2), "s"(lds0), "s"(w), "s"(16 - j), "s"(j), "s"(ws + WS_HS), "s"(rsrc), "s"(rd0), "s"(rd1), "s"(rld), "s"(32 * j + h),
        "s"((const char*)x + (size_t)lnrow * DM * 2), "s"(ws + WS_LN + lnrow * 8), "s"(ws + WS_LN + 1024 * (g ? j : 15 - j)),
        "s"((b >> 3) * 4 + w)
      : "v0", "v1", "v2", "v3", "v4", "v5", "v6", "v7", "v8", "v9", "v10", "v11", "v12", "v13", "v14", "v15", "v16", "v17", "v18", "v19", "v20", "v21", "v22", "v23", "v24", "v25", "v26", "v27", "v28", "v29", "v30", "v31", "v32", "v33", "v34", "v35", "v36", "v37", "v38", "v39", "v40", "v41", "v42", "v43", "v44", "v45", "v46", "v47", "v48", "v49", "v50", "v51", "v52", "v53", "v54", "v55", "v56", "v57", "v58", "v59", "v60", "v61", "v62", "v63", "v64", "v65", "v66", "v67", "v68", "v69", "v70", "v71", "v72", "v73", "v74", "v75", "v76", "v77", "v78", "v79", "v80", "v81", "v82", "v83", "v84", "v85", "v86", "v87", "v88", "v89", "v90", "v91", "v92", "v93", "v94", "v95", "v96", "v97", "v98", "v99", "v100", "v101", "v102", "v103", "v104", "v105", "v106", "v107", "v108", "v109", "v110", "v111", "v112", "v113", "v114", "v115", "v116", "v117", "v118", "v119", "v120", "v121", "v122", "v123", "v124", "v125", "v126", "v127", "v128", "v129", "v130", "v131", "v132", "v133", "v134", "v135", "v136", "v137", "v138", "v139", "v140", "v141", "v142", "v143", "v144", "v145", "v146", "v147", "v148", "v149", "v150", "v151", "v152", "v153", "v154", "v155", "v156", "v157", "v158", "v159", "v160", "v161", "v162", "v163", "v164", "v165", "v166", "v167", "v168", "v169", "v170", "v171", "v172", "v173", "v174", "v175", "v176", "v177", "v178", "v179", "v180", "v181", "v182", "v183", "v184", "v185", "v186", "v187", "v188", "v189", "v190", "v191", "v192", "v193", "v194", "v195", "v196", "v197", "v198", "v199", "v200", "v201", "v202", "v203", "v204", "v205", "v206", "v207", "v208", "v209", "v210", "v211", "v212", "v213", "v214", "v215", "v216", "v217", "v218", "v219", "v220", "v221", "v222", "v223", "v224", "v225", "v226", "v227", "v228", "v229", "v230", "v231", "v232", "v233", "v234", "v235", "v236", "v237", "v238", "v239", "v240", "v241", "v242", "v243", "v244", "v245", "v246", "v247", "v248", "v249", "v250", "v251", "v252", "v253", "v254", "v255", "a0", "a1", "a2", "a3", "a4", "a5", "a6", "a7", "a8", "a9", "a10", "a11", "a12", "a13", "a14", "a15", "a16", "a17", "a18", "a19", "a20", "a21", "a22", "a23", "a24", "a25", "a26", "a27", "a28", "a29", "a30", "a31", "a32", "a33", "a34", "a35", "a36", "a37", "a38", "a39", "a40", "a41", "a42", "a43", "a44", "a45", "a46", "a47", "a48", "a49", "a50", "a51", "a52", "a53", "a54", "a55", "a56", "a57", "a58", "a59", "a60", "a61", "a62", "a63", "a64", "a65", "a66", "a67", "a68", "a69", "a70", "a71", "a72", "a73", "a74", "a75", "a76", "a77", "a78", "a79", "a80", "a81", "a82", "a83", "a84", "a85", "a86", "a87", "a88", "a89", "a90", "a91", "a92", "a93", "a94", "a95", "a96", "a97", "a98", "a99", "a100", "a101", "a102", "a103", "a104", "a105", "a106", "a107", "a108", "a109", "a110", "a111", "a112", "a113", "a114", "a115", "a116", "a117", "a118", "a119", "a120", "a121", "a122", "a123", "a124", "a125", "a126", "a127", "a128", "a129", "a130", "a131", "a132", "a133", "a134", "a135", "a136", "a137", "a138", "a139", "a140", "a141", "a142", "a143", "a144", "a145", "a146", "a147", "a148", "a149", "a150", "a151", "a152", "a153", "a154", "a155", "a156", "a157", "a158", "a159", "a160", "a161", "a162", "a163", "a164", "a165", "a166", "a167", "a168", "a169", "a170", "a171", "a172", "a173", "a174", "a175", "a176", "a177", "a178", "a179", "a180", "a181", "a182", "a183", "a184", "a185", "a186", "a187", "a188", "a189", "a190", "a191", "a192", "a193", "a194", "a195", "a196", "a197", "a198", "a199", "a200", "a201", "a202", "a203", "a204", "a205", "a206", "a207", "a208", "a209", "a210", "a211", "a212", "a213", "a214", "a215", "a216", "a217", "a218", "a219", "a220", "a221", "a222", "a223", "a224", "a225", "a226", "a227", "a228", "a229", "a230", "a231", "a232", "a233", "a234", "a235", "a236", "a237", "a238", "a239", "a240", "a241", "a242", "a243", "a244", "a245", "a246", "a247", "a248", "a249", "a250", "a251", "a252", "a253", "a254", "a255", "s40", "s41", "s42", "s43", "s44", "s45", "s46", "s47", "s48", "s49", "s50", "s51", "s52", "s53", "s54", "s55", "s60", "s61", "s76", "s77", "s78", "s79", "s80", "s81", "s82", "s83", "s84", "s85", "s86", "s87", "s88", "s89", "s90", "s91", "s92", "s93", "s94", "s95", "vcc", "scc", "memory");
}

DI size_t h_off(int row, int nb, int q) {
  return ((size_t)((row >> 5) * 20 + (nb >> 1)) * 2 + ((row >> 4) & 1)) * 1024 + ((row & 15) + 16 * q) * 16 + (nb & 1) * 8;
}
DI float gelu_tanh(float v) {
  const float u = v * (0.7978845608028654f + 0.035677408136300125f * v * v);
  return v * __builtin_amdgcn_rcpf(1.f + __builtin_amdgcn_exp2f(-2.885390081777927f * u));
}
DI void st128_wt(void* p, u32x4 v) { asm volatile("global_store_dwordx4 %0, %1, off sc1\n\ts_nop 1" ::"v"(p), "v"(v) : "memory"); }
DI void st64_wt(void* p, u32x2 v) { asm volatile("global_store_dwordx2 %0, %1, off sc1\n\ts_nop 1" ::"v"(p), "v"(v) : "memory"); }
template <class T> DI T ld128_sc1(const void* sbase, unsigned voff, int imm) {
  T r;
  asm volatile("global_load_dwordx4 %0, %1, %2 offset:%3 sc1" : "=v"(r) : "v"(voff), "s"(sbase), "i"(imm) : "memory");
  return r;
}
template <class T> DI T ld128(const void* sbase, unsigned voff, int imm) {
  T r;
  asm volatile("global_load_dwordx4 %0, %1, %2 offset:%3" : "=v"(r) : "v"(voff), "s"(sbase), "i"(imm) : "memory");
  return r;
}
DI void team_wait(unsigned* cnt, unsigned want) {
#pragma unroll 1
  for (unsigned spins = 0; spins < (1u << 18); ++spins) {
    if (rfl(__hip_atomic_load(cnt, __ATOMIC_RELAXED, __HIP_MEMORY_SCOPE_AGENT)) >= want) break;
    __builtin_amdgcn_s_sleep(1);
  }
}
#define BAR() asm volatile("s_barrier" ::: "memory")
#define VM0() asm volatile("s_waitcnt vmcnt(0)" ::: "memory")
__global__ __launch_bounds__(512, 2) void k_tail(const bf16_t* __restrict__ x, const bf16_t* __restrict__ b1, const bf16_t* __restrict__ b2, char* ws,
                                                 bf16_t* __restrict__ out) {
  extern __shared__ __attribute__((aligned(16))) char lds[];
  const int tid = threadIdx.x, lane = tid & 63, w = rfl(tid >> 6);
  const int b = blockIdx.x, T = 4 * ((b & 7) >> 1) + ((b >> 3) & 3), ti = 8 * (b & 1) + (b >> 5);
  const int R = 2 * T + (ti & 1), g = ti >> 1;
  const unsigned lds0 = (unsigned)(size_t)lds, l16 = lane * 16;
  const int m = lane & 15, q = lane >> 4;
  unsigned* cnt = (unsigned*)(ws + WS_TC) + T * 512;
  constexpr int W1L = 20480;
  auto w1_step = [&](int s) {
#pragma unroll
    for (int nb = 0; nb < 7; ++nb) glds1(ws + WS_W1 + (s * 108 + min(7 * ti + nb, 107)) * 1024, l16, lds0 + W1L + (s * 7 + nb) * 1024);
  };
  u32x2 hv[5];
  if (w < 4) {
    const char* ag = ws + WS_CTX + (size_t)R * 128 * 4096 + l16;
    const char* wg = ws + WS_WO + (size_t)g * 128 * 5120 + l16;
    f32x4 acc[4][5] = {};
    constexpr int PD = 4;
    bf16x8 af[PD][4], wf[PD][5];
    auto load = [&](int s, int slot) {
#pragma unroll
      for (int i = 0; i < 4; ++i) af[slot][i] = *(const bf16x8*)(ag + (size_t)s * 4096 + i * 1024);
#pragma unroll
      for (int i = 0; i < 5; ++i) wf[slot][i] = *(const bf16x8*)(wg + (size_t)s * 5120 + i * 1024);
    };
    auto comp = [&](int slot) {
#pragma unroll
      for (int rb = 0; rb < 4; ++rb)
#pragma unroll
        for (int nb = 0; nb < 5; ++nb) acc[rb][nb] = MFMA16(wf[slot][nb], af[slot][rb], acc[rb][nb]);
    };
    const int s0 = 32 * w;
#pragma unroll
    for (int p = 0; p < PD; ++p) { load(s0 + p, p); SBAR(); }
#pragma unroll
    for (int nb = 0; nb < 5; ++nb) hv[nb] = *(const u32x2*)(x + (size_t)(64 * R + 16 * w + m) * DM + 80 * g + 16 * nb + 4 * q);
    SBAR();
#pragma unroll 1
    for (int i = 0; i + PD < 32; i += PD) {
#pragma unroll
      for (int p = 0; p < PD; ++p) { comp(p); SBAR(); load(s0 + i + PD + p, p); SBAR(); }
      if (i == 24) BAR();
    }
#pragma unroll
    for (int p = 0; p < PD; ++p) comp(p);
#pragma unroll
    for (int rb = 0; rb < 4; ++rb)
#pragma unroll
      for (int nb = 0; nb < 5; ++nb) *(LDS_AS f32x4*)((LDS_AS char*)lds + ((w * 4 + rb) * 5 + nb) * 1024 + l16) = acc[rb][nb];
    LGKM_BAR();
    f32x4 v[5];
#pragma unroll
    for (int nb = 0; nb < 5; ++nb) {
      v[nb] = *(const LDS_AS f32x4*)((const LDS_AS char*)lds + ((0 * 4 + w) * 5 + nb) * 1024 + l16);
#pragma unroll
      for (int u = 1; u < 4; ++u) v[nb] += *(const LDS_AS f32x4*)((const LDS_AS char*)lds + ((u * 4 + w) * 5 + nb) * 1024 + l16);
    }
    PIN(v[0]); PIN(v[1]); PIN(v[2]); PIN(v[3]); PIN(v[4]);
    LGKM_BAR();
    const int row = 64 * R + 16 * w + m;
    float s = 0.f;
#pragma unroll
    for (int nb = 0; nb < 5; ++nb) {
      const u32x2 xr = hv[nb];
      hv[nb] = (u32x2){cvtpk(v[nb][0] + bf_lo(xr[0]), v[nb][1] + bf_hi(xr[0])), cvtpk(v[nb][2] + bf_lo(xr[1]), v[nb][3] + bf_hi(xr[1]))};
      v[nb] = (f32x4){bf_lo(hv[nb][0]), bf_hi(hv[nb][0]), bf_lo(hv[nb][1]), bf_hi(hv[nb][1])};
      s += (v[nb][0] + v[nb][1]) + (v[nb][2] + v[nb][3]);
    }
    char* hb = ws + WS_H;
    if (g & 1) {
      st64_wt(hb + h_off(row, 5 * g, q), hv[0]);
      st128_wt(hb + h_off(row, 5 * g + 1, q), (u32x4){hv[1][0], hv[1][1], hv[2][0], hv[2][1]});
      st128_wt(hb + h_off(row, 5 * g + 3, q), (u32x4){hv[3][0], hv[3][1], hv[4][0], hv[4][1]});
    } else {
      st128_wt(hb + h_off(row, 5 * g, q), (u32x4){hv[0][0], hv[0][1], hv[1][0], hv[1][1]});
      st128_wt(hb + h_off(row, 5 * g + 2, q), (u32x4){hv[2][0], hv[2][1], hv[3][0], hv[3][1]});
      st64_wt(hb + h_off(row, 5 * g + 4, q), hv[4]);
    }
#pragma unroll
    for (int nb = 0; nb < 5; ++nb) *(LDS_AS u32x2*)((LDS_AS char*)lds + 4096 + (w * 5 + nb) * 512 + lane * 8) = hv[nb];
    s += __shfl_xor(s, 16); s += __shfl_xor(s, 32);
    const float mean = s * (1.f / 80.f);
    float m2 = 0.f;
#pragma unroll
    for (int nb = 0; nb < 5; ++nb)
#pragma unroll
      for (int e = 0; e < 4; ++e) { const float d = v[nb][e] - mean; m2 += d * d; }
    m2 += __shfl_xor(m2, 16); m2 += __shfl_xor(m2, 32);
    if (q == 0) st64_wt(ws + WS_ST + ((size_t)row * 8 + g) * 8, (u32x2){__float_as_uint(mean), __float_as_uint(m2)});
    asm volatile("s_waitcnt vmcnt(0) lgkmcnt(0)" ::: "memory");
    BAR();
    if (tid == 0) __hip_atomic_fetch_add(cnt, 1u, __ATOMIC_RELAXED, __HIP_MEMORY_SCOPE_AGENT);
    if (w == 0) team_wait(cnt, 16u);
  } else {
    {
      unsigned long long pc;
      asm volatile("s_getpc_b64 %0" : "=s"(pc));
      const unsigned o = min((unsigned)((b >> 3) * 4 + (w - 4)), (unsigned)TAIL_KB) * 1024u;
      glds1((const char*)pc + o, l16, lds0 + W1L + (12 + w) * 7168);
      VM0();
    }
    const int hw = w - 4;
    BAR();
#pragma unroll 1
    for (int s = hw + (hw == 0 ? 12 : 8); s < 20; s += 4) w1_step(s);
    BAR();
    BAR();
#pragma unroll 1
    for (int s = hw; s < 9; s += 4) w1_step(s);
    BAR();
    VM0();
    const bf16x8 ones = {0x3f80, 0x3f80, 0x3f80, 0x3f80, 0x3f80, 0x3f80, 0x3f80, 0x3f80};
    f32x4 cs[7] = {};
#pragma unroll
    for (int k = 0; k < 5; ++k)
#pragma unroll
      for (int nb = 0; nb < 7; ++nb) cs[nb] = MFMA16(lds_rd128(lds, W1L + ((hw + 4 * k) * 7 + nb) * 1024 + l16), ones, cs[nb]);
    if (m == 0) {
#pragma unroll
      for (int nb = 0; nb < 7; ++nb) *(LDS_AS f32x4*)((LDS_AS char*)lds + ((hw * 7 + nb) * 4 + q) * 16) = cs[nb];
    }
    asm volatile("s_waitcnt lgkmcnt(0)" ::: "memory");
  }
  u32x2 br[7];
#pragma unroll
  for (int nb = 0; nb < 7; ++nb)
    asm volatile("global_load_dwordx2 %0, %1, %2" : "=v"(br[nb]) : "v"(q * 8), "s"(b1 + min(16 * (7 * ti + nb), FF - 16)) : "memory");
  BAR();
  const int s0 = 7 * w - (w == 7), n3 = w < 6 ? 7 : 6;
  const char* wg3 = ws + WS_W2 + (size_t)(g * 54 + s0) * 5120;
  bf16x8 w3[7][5];
  auto loadW = [&](int i) {
    const char* p = wg3 + (size_t)min(i, n3 - 1) * 5120 + 2048;
#pragma unroll
    for (int nb = 0; nb < 5; ++nb) w3[i][nb] = ld128<bf16x8>(p, l16, nb * 1024 - 2048);
  };
  {
    const char* ag = ws + WS_H + (size_t)(4 * T + (w >> 1)) * 40 * 1024 + (w & 1) * 1024;
    const int frow = 128 * T + 16 * w;
    bf16x8 af[20];
    f32x4 sv[4];
    auto ldA = [&](int s) { af[s] = ld128_sc1<bf16x8>(ag + (s & ~1) * 2048, l16, (s & 1) * 2048); };
#pragma unroll
    for (int s = 0; s < 8; ++s) ldA(s);
    f32x4 csum[7];
#pragma unroll
    for (int nb = 0; nb < 7; ++nb) {
      csum[nb] = *(const LDS_AS f32x4*)((const LDS_AS char*)lds + (nb * 4 + q) * 16);
#pragma unroll
      for (int u = 1; u < 4; ++u) csum[nb] += *(const LDS_AS f32x4*)((const LDS_AS char*)lds + ((u * 7 + nb) * 4 + q) * 16);
      PIN(csum[nb]);
    }
    f32x4 acc[7] = {};
    bf16x8 wf[2][7];
#pragma unroll
    for (int nb = 0; nb < 7; ++nb) wf[0][nb] = lds_rd128(lds, W1L + nb * 1024 + l16);
#define P2_STEP(s, N)                                                                                                      \
    asm volatile("s_waitcnt vmcnt(" #N ")" : "+v"(af[s])::"memory");                                                       \
    if (s + 8 < 20) ldA(s + 8);                                                                                            \
    else if (s < 16) sv[s - 12] = ld128_sc1<f32x4>(ws + WS_ST + (size_t)frow * 64, m * 64, 16 * ((s - 12) & 3));            \
    else if (s > 16) loadW((s - 17) & 3);                                                                                  \
    if (s + 1 < 20) { _Pragma("unroll") for (int nb = 0; nb < 7; ++nb) wf[(s + 1) & 1][nb] = lds_rd128(lds, W1L + ((s + 1) * 7 + nb) * 1024 + l16); } \
    SBAR();                                                                                                                \
    _Pragma("unroll") for (int nb = 0; nb < 7; ++nb) acc[nb] = MFMA16(wf[s & 1][nb], af[s], acc[nb]);                        \
    SBAR();
    P2_STEP(0, 7) P2_STEP(1, 7) P2_STEP(2, 7) P2_STEP(3, 7) P2_STEP(4, 7) P2_STEP(5, 7) P2_STEP(6, 7) P2_STEP(7, 7) P2_STEP(8, 7) P2_STEP(9, 7)
    P2_STEP(10, 7) P2_STEP(11, 7) P2_STEP(12, 7) P2_STEP(13, 7) P2_STEP(14, 7) P2_STEP(15, 7) P2_STEP(16, 7) P2_STEP(17, 6) P2_STEP(18, 10) P2_STEP(19, 14)
#undef P2_STEP
    asm volatile("s_waitcnt vmcnt(15)"
                 : "+v"(sv[0]), "+v"(sv[1]), "+v"(sv[2]), "+v"(sv[3]), "+v"(br[0]), "+v"(br[1]), "+v"(br[2]), "+v"(br[3]), "+v"(br[4]), "+v"(br[5]), "+v"(br[6])::"memory");
    float mean = 0.f, m2 = 0.f;
#pragma unroll
    for (int i = 0; i < 4; ++i) { mean += sv[i][0] + sv[i][2]; m2 += sv[i][1] + sv[i][3]; }
    mean *= 0.125f;
#pragma unroll
    for (int i = 0; i < 4; ++i) { const float d0 = sv[i][0] - mean, d1 = sv[i][2] - mean; m2 += 80.f * (d0 * d0 + d1 * d1); }
    const float rstd = rsqrtf(m2 * (1.f / DM) + LN_EPS);
    u32x2 gv[7];
#pragma unroll
    for (int nb = 0; nb < 7; ++nb) {
      const float bb[4] = {bf_lo(br[nb][0]), bf_hi(br[nb][0]), bf_lo(br[nb][1]), bf_hi(br[nb][1])};
      float y[4];
#pragma unroll
      for (int e = 0; e < 4; ++e) y[e] = gelu_tanh(rstd * (acc[nb][e] - mean * csum[nb][e]) + bb[e]);
      gv[nb] = (u32x2){cvtpk(y[0], y[1]), cvtpk(y[2], y[3])};
    }
    char* gdst = ws + WS_G + ((size_t)(frow >> 6) * 54 * 4 + ((frow >> 4) & 3)) * 1024 + l16 + (size_t)((7 * ti) >> 1) * 4096;
    if (ti & 1) {
      st64_wt(gdst + 8, gv[0]);
      st128_wt(gdst + 4096, (u32x4){gv[1][0], gv[1][1], gv[2][0], gv[2][1]});
      if (ti != 15) {
        st128_wt(gdst + 8192, (u32x4){gv[3][0], gv[3][1], gv[4][0], gv[4][1]});
        st128_wt(gdst + 12288, (u32x4){gv[5][0], gv[5][1], gv[6][0], gv[6][1]});
      }
    } else {
      st128_wt(gdst, (u32x4){gv[0][0], gv[0][1], gv[1][0], gv[1][1]});
      st128_wt(gdst + 4096, (u32x4){gv[2][0], gv[2][1], gv[3][0], gv[3][1]});
      st128_wt(gdst + 8192, (u32x4){gv[4][0], gv[4][1], gv[5][0], gv[5][1]});
      st64_wt(gdst + 12288, gv[6]);
    }
    VM0();
    BAR();
    if (tid == 7 * 64) __hip_atomic_fetch_add(cnt + 64, 1u, __ATOMIC_RELAXED, __HIP_MEMORY_SCOPE_AGENT);
  }
  {
    const int n = n3;
    const char* ag = ws + WS_G + (size_t)(R * 54 + s0) * 4096;
    loadW(3);
    SBAR();
    if (w == 7) team_wait(cnt + 64, 16u);
    BAR();
    f32x4 acc[4][5] = {};
    bf16x8 af[7][4];
    auto loadA = [&](int i) {
#pragma unroll
      for (int rb = 0; rb < 4; ++rb) af[i][rb] = ld128_sc1<bf16x8>(ag + (size_t)min(i, n - 1) * 4096, l16, rb * 1024);
    };
    loadA(0); loadA(1); loadA(2);
#define P3_STEP(i, N, NEXT)                                                                                                \
    asm volatile("s_waitcnt vmcnt(" #N ")"                                                                                 \
                 : "+v"(af[i][0]), "+v"(af[i][1]), "+v"(af[i][2]), "+v"(af[i][3]), "+v"(w3[i][0]), "+v"(w3[i][1]), "+v"(w3[i][2]), "+v"(w3[i][3]), \
                   "+v"(w3[i][4])::"memory");                                                                              \
    if (i < 6 || n == 7) {                                                                                                 \
      _Pragma("unroll") for (int rb = 0; rb < 4; ++rb)                                                                     \
        _Pragma("unroll") for (int nb = 0; nb < 5; ++nb) acc[rb][nb] = MFMA16(w3[i][nb], af[i][rb], acc[rb][nb]);           \
    }                                                                                                                      \
    SBAR();                                                                                                                \
    NEXT;                                                                                                                  \
    SBAR();
    P3_STEP(0, 8, loadA(3); loadW(4))
    P3_STEP(1, 13, loadA(4); loadW(5))
    P3_STEP(2, 18, loadA(5); loadW(6))
    P3_STEP(3, 23, loadA(6))
    P3_STEP(4, 18, )
    P3_STEP(5, 9, )
    P3_STEP(6, 0, )
#undef P3_STEP
    const int nb0 = 3 * (w >> 2);
    u32x2 eb[3];
#pragma unroll
    for (int i = 0; i < 3; ++i) eb[i] = *(const u32x2*)(b2 + 80 * g + 16 * min(nb0 + i, 4) + 4 * q);
  u32x2 hres[3];
#pragma unroll
  for (int i = 0; i < 3; ++i) hres[i] = *(const LDS_AS u32x2*)((const LDS_AS char*)lds + 4096 + ((w & 3) * 5 + min(3 * (w >> 2) + i, 4)) * 512 + lane * 8);
    PIN(hres[0]); PIN(hres[1]); PIN(hres[2]);
    LGKM_BAR();
#pragma unroll
    for (int rb = 0; rb < 4; ++rb)
#pragma unroll
      for (int nb = 0; nb < 5; ++nb) *(LDS_AS f32x4*)((LDS_AS char*)lds + ((w * 4 + rb) * 5 + nb) * 1024 + l16) = acc[rb][nb];
    LGKM_BAR();
    const int row = 64 * R + 16 * (w & 3) + m;
#pragma unroll
    for (int i = 0; i < 3; ++i) {
      const int nb = min(nb0 + i, 4);
      f32x4 v = *(const LDS_AS f32x4*)((const LDS_AS char*)lds + ((w & 3) * 5 + nb) * 1024 + l16);
#pragma unroll
      for (int u = 1; u < 8; ++u) v += *(const LDS_AS f32x4*)((const LDS_AS char*)lds + ((u * 4 + (w & 3)) * 5 + nb) * 1024 + l16);
      const u32x2 hr = hres[i], bb = eb[i];
      const u32x2 ov = {cvtpk(v[0] + bf_lo(bb[0]) + bf_lo(hr[0]), v[1] + bf_hi(bb[0]) + bf_hi(hr[0])),
                        cvtpk(v[2] + bf_lo(bb[1]) + bf_lo(hr[1]), v[3] + bf_hi(bb[1]) + bf_hi(hr[1]))};
      if (nb0 + i < 5) *(u32x2*)(out + (size_t)row * DM + 80 * g + 16 * nb + 4 * q) = ov;
    }
  }
}

extern "C" void kernel_launch(void* const* d_in, const int* in_sizes, int n_in, void* d_out, int out_size, void* d_ws, size_t ws_size,
                              hipStream_t stream) {
  const bf16_t* x = (const bf16_t*)d_in[0];
  const bf16_t* ckv = (const bf16_t*)d_in[1];
  const bf16_t* wq = (const bf16_t*)d_in[2];
  const bf16_t* wuk = (const bf16_t*)d_in[3];
  const bf16_t* wuv = (const bf16_t*)d_in[4];
  const bf16_t* wo = (const bf16_t*)d_in[5];
  const bf16_t* w1 = (const bf16_t*)d_in[7];
  const bf16_t* b1 = (const bf16_t*)d_in[8];
  const bf16_t* w2 = (const bf16_t*)d_in[9];
  const bf16_t* b2 = (const bf16_t*)d_in[10];
  char* ws = (char*)d_ws;
  (void)hipFuncSetAttribute((const void*)k_front, hipFuncAttributeMaxDynamicSharedMemorySize, 163840);
  (void)hipFuncSetAttribute((const void*)k_tail, hipFuncAttributeMaxDynamicSharedMemorySize, 163840);
  k_front<<<256, 256, 163840, stream>>>(x, ckv, wuk, wuv, wq, wo, w1, w2, ws);
  k_tail<<<256, 512, 163840, stream>>>(x, b1, b2, ws, (bf16_t*)d_out);
}
```
